```python
import torch

def _softmax(x, axis=-1):
    return torch.softmax(x, dim=axis)
def _gelu(x):
    return torch.nn.functional.gelu(x, approximate="tanh")
def _silu(x):
    return torch.nn.functional.silu(x)
def _sigmoid(x):
    return torch.sigmoid(x)
def _layernorm(x, axis=-1, eps=1e-5):
    m = x.mean(dim=axis, keepdim=True)
    v = x.var(dim=axis, keepdim=True, unbiased=False)
    return (x - m) / torch.sqrt(v + eps)
def _scatter_add(tgt, idx, src, axis=0):
    return tgt.clone().index_add_(axis, idx, src)
def _segment_sum(src, seg, n):
    out = torch.zeros((n,) + src.shape[1:], dtype=src.dtype)
    return out.index_add_(0, seg, src)
def _pad(x, pw):
    flat = tuple(v for pair in reversed(pw) for v in pair)
    return torch.nn.functional.pad(x, flat)

kernel_name = 'recurrent_scan__softmax_full_mha__glu_gated_geglu__prompt'

def setup_inputs():

    _w = (lambda a: (lambda t: t.to(torch.bfloat16) if t.is_floating_point() else t)(torch.as_tensor(a)))
    def _A_setup():
        B, S, D = 1, 2048, 512
        return {
            "x": _w((lambda *s: torch.randn(*s, dtype=torch.bfloat16) * 0.05)(B, S, D)),
            "wqkv": _w((lambda *s: torch.randn(*s, dtype=torch.bfloat16) * 0.2)(D, 3 * D)),
            "wo": _w((lambda *s: torch.randn(*s, dtype=torch.bfloat16) * 0.2)(D, D)),
        }
    _A_in = _A_setup()

    def _B_setup():
        B, S, Dm, Df = 1, 2048, 512, 1376
        return {
            "x": _w((lambda *s: torch.randn(*s, dtype=torch.bfloat16) * 0.05)(B, S, Dm)),
            "w_gate": _w((lambda *s: torch.randn(*s, dtype=torch.bfloat16) * 0.1)(Dm, Df) * (Dm ** -0.5)),
            "w_up": _w((lambda *s: torch.randn(*s, dtype=torch.bfloat16) * 0.1)(Dm, Df) * (Dm ** -0.5)),
            "w_down": _w((lambda *s: torch.randn(*s, dtype=torch.bfloat16) * 0.1)(Df, Dm) * (Df ** -0.5)),
        }
    _B_in = _B_setup()

    return {
        "A_x": _A_in["x"],
        "A_wqkv": _A_in["wqkv"],
        "A_wo": _A_in["wo"],
        "B_x": _B_in["x"],
        "B_w_gate": _B_in["w_gate"],
        "B_w_up": _B_in["w_up"],
        "B_w_down": _B_in["w_down"],
    }

def reference(A_x, A_wqkv, A_wo, B_x, B_w_gate, B_w_up, B_w_down):
    def _A_ref(x, wqkv, wo):
        B, S, D = x.shape
        H = 32; Dh = D // H
        qkv = (lambda x, *d: x.permute(*d))((x @ wqkv).reshape(B, S, 3, H, Dh), 2, 0, 3, 1, 4)
        q, k, v = qkv[0], qkv[1], qkv[2]
        scale = Dh ** -0.5
        scores = torch.einsum("bhqd,bhkd->bhqk", q, k) * scale
        p = _softmax(scores, axis=-1)
        o = torch.einsum("bhqk,bhkd->bhqd", p, v)
        return (lambda x, *d: x.permute(*d))(o, 0, 2, 1, 3).reshape(B, S, D) @ wo

    def _B_ref(x, w_gate, w_up, w_down):
        g = torch.einsum("bsd,df->bsf", x, w_gate)
        u = torch.einsum("bsd,df->bsf", x, w_up)
        h = _gelu(g) * u
        return torch.einsum("bsf,fd->bsd", h, w_down)

    x = A_x
    xn = _layernorm(x, axis=-1)
    s = _A_ref(xn, A_wqkv, A_wo)
    g = _silu(_B_ref(xn, B_w_gate, B_w_up, B_w_down))
    return x + s * g
```

```cpp
#include <hip/hip_runtime.h>
#include <hip/hip_bf16.h>
typedef unsigned short u16;
__device__ inline float bf2f(u16 v) { return __uint_as_float((unsigned)v << 16); }
__device__ inline u16 f2bf(float f) { unsigned u = __float_as_uint(f); u += 0x7FFF + ((u >> 16) & 1); return u >> 16; }
constexpr int S = 2048, D = 512, F = 1376, H = 32, DH = 16;
__global__ void k_ln(const u16* x, float* xn) {
  int r = blockIdx.x, t = threadIdx.x; __shared__ float red[2][8];
  float a = bf2f(x[r * D + t]); float s = a, q = a * a;
  for (int o = 32; o; o >>= 1) { s += __shfl_xor(s, o); q += __shfl_xor(q, o); }
  if ((t & 63) == 0) { red[0][t >> 6] = s; red[1][t >> 6] = q; } __syncthreads();
  s = 0; q = 0; for (int i = 0; i < 8; i++) { s += red[0][i]; q += red[1][i]; }
  float m = s / D, v = q / D - m * m; xn[r * D + t] = (a - m) * rsqrtf(v + 1e-5f);
}
__global__ void k_gemm(const float* A, const u16* B, float* C, int K, int N) {
  int n = blockIdx.x * 64 + threadIdx.x, m0 = blockIdx.y * 8; if (n >= N) return;
  float acc[8] = {0};
  for (int k = 0; k < K; k++) { float b = bf2f(B[k * N + n]); for (int i = 0; i < 8; i++) acc[i] += A[(m0 + i) * K + k] * b; }
  for (int i = 0; i < 8; i++) C[(m0 + i) * N + n] = acc[i];
}
__global__ void k_attn(const float* qkv, float* o) {
  int h = blockIdx.y, r = blockIdx.x * 64 + threadIdx.x; float q[16], acc[16] = {0}, m = -1e30f, l = 0;
  for (int d = 0; d < 16; d++) q[d] = bf2f(f2bf(qkv[r * 1536 + h * 16 + d])) * 0.25f;
  for (int j = 0; j < S; j++) { float s = 0; for (int d = 0; d < 16; d++) s += q[d] * bf2f(f2bf(qkv[j * 1536 + 512 + h * 16 + d]));
    float mn = fmaxf(m, s), al = __expf(m - mn), p = __expf(s - mn); l = l * al + p; m = mn;
    for (int d = 0; d < 16; d++) acc[d] = acc[d] * al + p * bf2f(f2bf(qkv[j * 1536 + 1024 + h * 16 + d])); }
  for (int d = 0; d < 16; d++) o[r * D + h * 16 + d] = bf2f(f2bf(acc[d] / l));
}
__global__ void k_geglu(const float* g, const float* u, float* h) { int i = blockIdx.x * 256 + threadIdx.x; float a = g[i];
  float t = tanhf(0.7978845608f * (a + 0.044715f * a * a * a)); h[i] = bf2f(f2bf(0.5f * a * (1 + t) * u[i])); }
__global__ void k_fin(const u16* x, const float* s, const float* d, u16* out) { int i = blockIdx.x * 256 + threadIdx.x; float a = d[i];
  out[i] = f2bf(bf2f(x[i]) + s[i] * (a / (1 + __expf(-a)))); }
extern "C" void kernel_launch(void* const* d_in, const int* in_sizes, int n_in, void* d_out, int out_size, void* d_ws, size_t ws_size, hipStream_t st) {
  const u16 *x = (const u16*)d_in[0], *wqkv = (const u16*)d_in[1], *wo = (const u16*)d_in[2], *wg = (const u16*)d_in[4], *wu = (const u16*)d_in[5], *wd = (const u16*)d_in[6];
  float* w = (float*)d_ws; float *xn = w, *qkv = xn + S * D, *o = qkv + S * 1536, *g = o + S * D, *u = g + S * F, *h = u + S * F, *s = h + S * F, *d = s + S * D;
  k_ln<<<S, 512, 0, st>>>(x, xn);
  k_gemm<<<dim3(24, S / 8), 64, 0, st>>>(xn, wqkv, qkv, D, 1536);
  k_gemm<<<dim3(22, S / 8), 64, 0, st>>>(xn, wg, g, D, F);
  k_gemm<<<dim3(22, S / 8), 64, 0, st>>>(xn, wu, u, D, F);
  k_attn<<<dim3(S / 64, H), 64, 0, st>>>(qkv, o);
  k_geglu<<<S * F / 256, 256, 0, st>>>(g, u, h);
  k_gemm<<<dim3(8, S / 8), 64, 0, st>>>(o, wo, s, D, D);
  k_gemm<<<dim3(8, S / 8), 64, 0, st>>>(h, wd, d, F, D);
  k_fin<<<S * D / 256, 256, 0, st>>>(x, s, d, (u16*)d_out);
}
```

```cpp
#include <hip/hip_runtime.h>

typedef unsigned u32;
typedef unsigned long long u64;
typedef unsigned short u16;
typedef u32 u32x2 __attribute__((ext_vector_type(2)));
typedef u32 u32x4 __attribute__((ext_vector_type(4)));
typedef float f32x2 __attribute__((ext_vector_type(2)));
typedef float f32x4 __attribute__((ext_vector_type(4)));
typedef float f32x16 __attribute__((ext_vector_type(16)));
typedef __bf16 bf16x2 __attribute__((ext_vector_type(2)));
typedef __bf16 bf16x8 __attribute__((ext_vector_type(8)));
typedef short s16x4 __attribute__((ext_vector_type(4)));
#define LAS __attribute__((address_space(3)))
#define GAS __attribute__((address_space(1)))
#define DI __device__ __forceinline__
#define PIN(v) asm volatile("" : "+v"(v))
#define SBAR() __builtin_amdgcn_sched_barrier(0)

constexpr u32 WS_FLAG = 4096;
constexpr u32 WS_Q = 1u << 20;
constexpr u32 WS_K = 3u << 20;
constexpr u32 WS_V = 5u << 20;
constexpr u32 WS_O = 7u << 20;
constexpr u32 WS_H = 9u << 20;
constexpr u32 WS_END = 15u << 20;
constexpr u64 EPOCH_STEP = 0x9E3779B97F4A7C15ull;
constexpr u32 SLOT = 28672;
constexpr u32 SCR = 5 * SLOT;
constexpr u32 MERGE = 13 * 2048;
constexpr u32 HCNT = 163776;
constexpr u32 FREG = 40960;

#define STAMP(i)
#define RSTAMP(i)
#define STAMP_DECL
#define STAMP_OUT()

DI u32 fresh_lane() { u32 l; asm volatile("v_mbcnt_lo_u32_b32 %0, -1, 0\n\tv_mbcnt_hi_u32_b32 %0, -1, %0" : "=v"(l)); __builtin_assume(l < 64); return l; }
DI u32 rfl(u32 v) { return __builtin_amdgcn_readfirstlane(v); }
DI u32 cvtpk(float lo, float hi) { f32x2 v = {lo, hi}; return __builtin_bit_cast(u32, __builtin_convertvector(v, bf16x2)); }
DI f32x16 mfma32(u32x4 a, u32x4 b, f32x16 c) {
  return __builtin_amdgcn_mfma_f32_32x32x16_bf16(__builtin_bit_cast(bf16x8, a), __builtin_bit_cast(bf16x8, b), c, 0, 0, 0);
}
DI float dot2(u32 a, u32 b, float c) {
  return __builtin_amdgcn_fdot2_f32_bf16(__builtin_bit_cast(bf16x2, a), __builtin_bit_cast(bf16x2, b), c, false);
}
DI float dot8(u32x4 a, u32x4 b, float c) { return dot2(a[3], b[3], dot2(a[2], b[2], dot2(a[1], b[1], dot2(a[0], b[0], c)))); }
DI float xhalf_sum(float v) {
  auto r = __builtin_amdgcn_permlane32_swap(__float_as_uint(v), __float_as_uint(v), false, false);
  return __uint_as_float(r[0]) + __uint_as_float(r[1]);
}
DI u32x4 lds_r128(u32 a) { return *(const LAS u32x4*)(uintptr_t)a; }
DI u32x4 lds_tr(u32 a, u32 d) {
  u32x2 lo = __builtin_bit_cast(u32x2, __builtin_amdgcn_ds_read_tr16_b64_v4i16((LAS s16x4*)(uintptr_t)a));
  u32x2 hi = __builtin_bit_cast(u32x2, __builtin_amdgcn_ds_read_tr16_b64_v4i16((LAS s16x4*)(uintptr_t)(a + d)));
  return u32x4{lo[0], lo[1], hi[0], hi[1]};
}
DI void glds(u32 voff, const void* sbase, u32 lds) {
  asm volatile("s_mov_b32 m0, %2\n\ts_nop 0\n\tglobal_load_lds_dwordx4 %0, %1" ::"v"(voff), "s"(sbase), "s"(lds) : "memory");
}
template <int N> DI void wait_bar() { asm volatile("s_waitcnt vmcnt(%0) lgkmcnt(0)\n\ts_barrier" ::"n"(N) : "memory"); }
template <int N> DI void wait_vm() { asm volatile("s_waitcnt vmcnt(%0)" ::"n"(N) : "memory"); }
typedef __amdgpu_buffer_rsrc_t rsrc_t;
DI void st_sc1(rsrc_t r, u32 voff, u32 soff, u32x4 v) { u32 a = voff + soff; PIN(a); __builtin_amdgcn_raw_buffer_store_b128(v, r, a, 0, 16); }
DI u32x4 ld_sc1(rsrc_t r, u32 voff, u32 soff) { return __builtin_amdgcn_raw_buffer_load_b128(r, voff, soff, 16); }
DI u32 split2(float v) {
  u32 hi = cvtpk(v, v) & 0xffffu;
  float rem = v - __uint_as_float(hi << 16);
  return hi | (cvtpk(rem, rem) << 16);
}
DI void poll_flags(rsrc_t wsr, u32 idx, u64 want) {
#pragma nounroll
  for (u32 spin = 0; spin < (1u << 20); ++spin) {
    asm volatile("" ::: "memory");
    const u32x2 v = __builtin_amdgcn_raw_buffer_load_b64(wsr, WS_FLAG + idx * 128u, 0, 16);
    if (__all(v[0] == (u32)want && v[1] == (u32)(want >> 32))) break;
    __builtin_amdgcn_s_sleep(2);
  }
  asm volatile("" ::: "memory");
}
DI void set_flag(char* ws, u32 idx, u64 val) {
  __hip_atomic_store((GAS u64*)(ws + WS_FLAG + idx * 128u), val, __ATOMIC_RELAXED, __HIP_MEMORY_SCOPE_AGENT);
}

typedef float f32x8 __attribute__((ext_vector_type(8)));
typedef u32 u32x16 __attribute__((ext_vector_type(16)));
DI u32x4 cs_frag(u32 a) {
  const f32x4 p = *(const LAS f32x4*)(uintptr_t)a;
  const float c = (p[0] + p[1]) + (p[2] + p[3]);
  const u32 hi = cvtpk(c, c);
  const float rem = c - __uint_as_float(hi & 0xffff0000u);
  return u32x4{hi, cvtpk(rem, rem), 0u, 0u};
}
DI u32x4 geglu(f32x16 a, float rstd) {
  const float k0 = -2.f * 1.4426950408889634f * 0.7978845608028654f * rstd, r2 = rstd * rstd, k1 = k0 * 0.044715f * r2;
  const float c = __builtin_amdgcn_rcpf(r2);
  u32x4 o;
#pragma unroll
  for (int p = 0; p < 4; ++p) {
    const f32x2 ga = {a[2 * p], a[2 * p + 1]}, ub = {a[8 + 2 * p], a[9 + 2 * p]};
    const f32x2 z = ga * __builtin_elementwise_fma(ga * ga, f32x2{k1, k1}, f32x2{k0, k0});
    const f32x2 d = __builtin_elementwise_fma(f32x2{__builtin_amdgcn_exp2f(z[0]), __builtin_amdgcn_exp2f(z[1])}, f32x2{c, c}, f32x2{c, c});
    const f32x2 hv = ga * ub * f32x2{__builtin_amdgcn_rcpf(d[0]), __builtin_amdgcn_rcpf(d[1])};
    o[p] = cvtpk(hv[0], hv[1]);
  }
  return o;
}

__global__ __launch_bounds__(512, 2) void fused_block(const char* __restrict__ x, const char* __restrict__ wqkv, const char* __restrict__ wo,
                                                      const char* __restrict__ wg, const char* __restrict__ wu, const char* __restrict__ wd,
                                                      char* __restrict__ out, char* __restrict__ ws) {
  __shared__ __attribute__((aligned(16))) char smem[163840];
  u32x4 srdx, srdq;
  asm volatile("s_load_dwordx2 s[76:77], %2, 0x0\n\ts_load_dwordx2 s[80:81], %2, 0x8" : "=&{s[76:79]}"(srdx), "=&{s[80:83]}"(srdq)
               : "s"(__builtin_amdgcn_kernarg_segment_ptr()) : "memory");
  const u32 w = rfl(threadIdx.x >> 6);
  u32 lane = fresh_lane();
  const u32 b = blockIdx.x, j = 4 * (b & 3) + ((b >> 3) & 3), g = 8 * ((b >> 2) & 1) + (b >> 5);
  const u32 role = w >> 2, t = w & 3;
  u32 r31 = lane & 31, h = lane >> 5;
#define RELANE() do { lane = fresh_lane(); r31 = lane & 31; h = lane >> 5; } while (0)
  asm volatile("" ::"s"((u32)(uintptr_t)(LAS char*)smem));
  u32 cwarm = 0;
  if (w == 7) {
    asm volatile("s_getpc_b64 s[68:69]\n"
                 ".Lfused_code_a:\n"
                 "s_and_b32 s69, s69, 0xffff\n"
                 "s_mov_b32 s70, .Lfused_code_z-.Lfused_code_a\n"
                 "s_mov_b32 s71, 0x20000\n"
                 "buffer_load_dword %0, %1, s[68:71], 0 offen\n" : "=&v"(cwarm) : "v"(lane < 12 ? ((b >> 3) * 12 + lane) * 128 : 0x7fffffffu)
                 : "s68", "s69", "s70", "s71", "scc", "memory");
  }
  STAMP_DECL;
  STAMP(0); RSTAMP(0);
  const rsrc_t wsr = __builtin_amdgcn_make_buffer_rsrc(ws, 0, WS_END, 0x00020000);

  const u32 U0 = j * 6 < 80 ? j * 6 : 80;
  const u32 tile32 = 4 * g + t, head = 2 * j + role;
#define ROT ((g + 2 * (j & 3)) & 7)
  const u32 rot = ROT;
  f32x4 nm;
  u32 ep0, ep1;
  u32x16 pa, pb;
#define PAR_IN "{s[36:51]}"(pa), "{s[52:67]}"(pb)
  const u32 vx0 = (g * 128 + w * 16 + (lane >> 3)) * 1024 + (((lane & 7) ^ (lane >> 4)) << 4), vx1 = (vx0 + 8192) ^ 64;
  const u32 nq = w * 64 + lane;
  const u32 vq0 = (nq * 43691 >> 19) * 2880 + nq * 16 + j * 192, vq1 = ((nq + 256) * 43691 >> 19) * 2880 + nq * 16 + (j * 192 + 4096);
#define DMA_IN [vx0] "v"(vx0), [vx1] "v"(vx1), [vq0] "v"(vq0), [vq1] "v"(vq1), [rot] "s"(rot), \
               [sx] "s"(w * 2048), [sq] "s"(16384 + w * 1024)
  asm volatile(
      "s_mov_b32 s78, 2097152\n"
      "s_mov_b32 s79, 0x20000\n"
      "s_mov_b32 s82, 1572864\n"
      "s_mov_b32 s83, 0x20000\n"
      "s_waitcnt lgkmcnt(0)\n"
      "s_and_b32 s77, s77, 0xffff\n"
      "s_and_b32 s81, s81, 0xffff\n"
      "s_add_u32 s72, %[rot], 0\n"
      "s_and_b32 s72, s72, 7\n"
      "s_lshl_b32 s68, s72, 7\n"
      "s_mul_i32 s69, s72, 196608\n"
      "s_add_u32 m0, %[sx], 0\n"
      "s_nop 0\n"
      "buffer_load_dwordx4 %[vx0], s[76:79], s68 offen lds\n"
      "s_add_u32 m0, %[sx], 1024\n"
      "s_nop 0\n"
      "buffer_load_dwordx4 %[vx1], s[76:79], s68 offen lds\n"
      "s_add_u32 m0, %[sq], 0\n"
      "s_nop 0\n"
      "buffer_load_dwordx4 %[vq0], s[80:83], s69 offen lds\n"
      "s_cmp_eq_u32 %[role], 0\n"
      "s_cbranch_scc1 1f\n"
      "s_add_u32 m0, %[sq], 4096\n"
      "s_nop 0\n"
      "buffer_load_dwordx4 %[vq1], s[80:83], s69 offen lds\n"
      "1:\n"
      "s_add_u32 s72, %[rot], 1\n"
      "s_and_b32 s72, s72, 7\n"
      "s_lshl_b32 s68, s72, 7\n"
      "s_mul_i32 s69, s72, 196608\n"
      "s_add_u32 m0, %[sx], 28672\n"
      "s_nop 0\n"
      "buffer_load_dwordx4 %[vx0], s[76:79], s68 offen lds\n"
      "s_add_u32 m0, %[sx], 29696\n"
      "s_nop 0\n"
      "buffer_load_dwordx4 %[vx1], s[76:79], s68 offen lds\n"
      "s_add_u32 m0, %[sq], 28672\n"
      "s_nop 0\n"
      "buffer_load_dwordx4 %[vq0], s[80:83], s69 offen lds\n"
      "s_cmp_eq_u32 %[role], 0\n"
      "s_cbranch_scc1 2f\n"
      "s_add_u32 m0, %[sq], 32768\n"
      "s_nop 0\n"
      "buffer_load_dwordx4 %[vq1], s[80:83], s69 offen lds\n"
      "2:\n"
    : "+{s[76:79]}"(srdx), "+{s[80:83]}"(srdq) : DMA_IN, [role] "s"(role) : "memory", "scc", "s68", "s69", "s70", "s71", "s72", "s73");
  u32x4 muf;
  float rstd;
  u32x16 x0, x1, x2, x3, x4, x5, x6, x7;
  const rsrc_t srdg = __builtin_amdgcn_make_buffer_rsrc((void*)((role ? wu : wg) + U0 * 32), 0, 512 * 2752 - U0 * 32, 0x00020000);
  u32x4 xa, wq, pat2;
  const u32 sg = role * 12320 + t * 3072;
  const u32 pv = ((lane & 3) == 0 && ((lane >> 4) & 1) == ((lane >> 2) & 1)) ? 0x3f803f80u : 0u;
  const u32x4 pat = {pv, pv, pv, pv};
  const u32 wb = (8 * h + ((lane & 15) >> 2)) * 192 + (lane & 3) * 8;
  {
    {
      const u32 row = 32 * t + r31;
      const u32 xb = row * 128 + ((h ^ ((row >> 1) & 7)) << 4);
#pragma unroll
      for (u32 i = 0; i < 4; ++i) {
        xa[i] = xb ^ ((i ^ t) << 5);
        wq[i] = 16384 + wb + ((lane >> 4) & 1) * 32 + role * 128 + (i ^ t) * 3072;
      }
    }
#define QKV_OUT "=&{v[120:123]}"(muf), "=&{v124}"(rstd), "=&{v[28:31]}"(pat2), "+{v[8:11]}"(xa), "+{v[12:15]}"(wq), [ep0] "=&s"(ep0), [ep1] "=&s"(ep1), XOUT
#define XOUT "=&{v[128:143]}"(x0), "=&{v[144:159]}"(x1), "=&{v[160:175]}"(x2), "=&{v[176:191]}"(x3), "=&{v[192:207]}"(x4), "=&{v[208:223]}"(x5), \
             "=&{v[224:239]}"(x6), "=&{v[240:255]}"(x7)
#define QKV_IN DMA_IN, [srdx] "s"(srdx), [srdq] "s"(srdq), [srdg] "s"(srdg), "{v[92:95]}"(pat), [sg] "s"(sg), [n0] "s"(t * 192), [srd] "s"(wsr), [var] "s"(rfl(2 * role + (j >= 11 - role ? 1 : 0))), [gi] "s"(g), [ji] "s"(j), [ti] "s"(t), [ri] "s"(role), [wid] "s"(w), [vsn] "v"(WS_O + (tile32 * 32 + head) * 1024 + lane * 16), "v"(cwarm)
    asm volatile(
      "s_cmp_eq_u32 %[var], 1\n"
      "s_cbranch_scc1 41f\n"
      "s_cmp_eq_u32 %[var], 2\n"
      "s_cbranch_scc1 42f\n"
      "s_cmp_eq_u32 %[var], 3\n"
      "s_cbranch_scc1 43f\n"
      "v_mov_b32 v2, 0\n"
      "v_mov_b32 v3, 0\n"
      "s_mov_b32 s71, 0xffff0000\n"
      "v_mov_b32 v116, 0\n"
      "buffer_load_dwordx2 v[116:117], v116, %[srd], 0 offen sc1\n"
      "s_movk_i32 s73, 2560\n"
      "v_mov_b32 v104, 163776\n"
      "ds_write_b64 v104, v[2:3]\n"
      "v_add_u32 v72, 57344, v8\n"
      "v_add_u32 v73, 57344, v9\n"
      "v_add_u32 v74, 57344, v10\n"
      "v_add_u32 v75, 57344, v11\n"
      "v_add_u32 v76, 57344, v12\n"
      "v_add_u32 v77, 57344, v13\n"
      "v_add_u32 v78, 57344, v14\n"
      "v_add_u32 v79, 57344, v15\n"
      "v_add_u32 v80, 114688, v8\n"
      "v_add_u32 v81, 114688, v9\n"
      "v_add_u32 v82, 114688, v10\n"
      "v_add_u32 v83, 114688, v11\n"
      "v_add_u32 v84, 114688, v12\n"
      "v_add_u32 v85, 114688, v13\n"
      "v_add_u32 v86, 114688, v14\n"
      "v_add_u32 v87, 114688, v15\n"
      "v_mbcnt_lo_u32_b32 v91, -1, 0\n"
      "v_mbcnt_hi_u32_b32 v91, -1, v91\n"
      "v_add3_u32 v91, v91, %[n0], 0\n"
      "v_mul_u32_u24 v88, 0xaaab, v91\n"
      "v_lshrrev_b32 v88, 19, v88\n"
      "v_lshlrev_b32 v91, 4, v91\n"
      "v_mad_u32_u24 v88, v88, s73, v91\n"
      "v_mbcnt_lo_u32_b32 v91, -1, 0\n"
      "v_mbcnt_hi_u32_b32 v91, -1, v91\n"
      "v_add3_u32 v91, v91, %[n0], 64\n"
      "v_mul_u32_u24 v89, 0xaaab, v91\n"
      "v_lshrrev_b32 v89, 19, v89\n"
      "v_lshlrev_b32 v91, 4, v91\n"
      "v_mad_u32_u24 v89, v89, s73, v91\n"
      "v_mbcnt_lo_u32_b32 v91, -1, 0\n"
      "v_mbcnt_hi_u32_b32 v91, -1, v91\n"
      "v_add3_u32 v91, v91, %[n0], 64\n"
      "v_add_u32 v91, 64, v91\n"
      "v_mul_u32_u24 v90, 0xaaab, v91\n"
      "v_lshrrev_b32 v90, 19, v90\n"
      "v_lshlrev_b32 v91, 4, v91\n"
      "v_mad_u32_u24 v90, v90, s73, v91\n"
      "v_mbcnt_lo_u32_b32 v104, -1, 0\n"
      "v_mbcnt_hi_u32_b32 v104, -1, v104\n"
      "v_lshlrev_b32 v104, 4, v104\n"
      "v_and_b32 v105, 0x1f0, v104\n"
      "v_lshrrev_b32 v106, 2, v105\n"
      "v_add_u32 v105, 143360, v105\n"
      "v_lshl_add_u32 v106, %[n0], 1, v106\n"
      "v_add_u32 v106, 144896, v106\n"
      "v_and_b32 v108, 0x330, v104\n"
      "v_and_b32 v107, 0x40, v104\n"
      "v_lshl_or_b32 v108, v107, 1, v108\n"
      "v_and_b32 v107, 0x80, v104\n"
      "v_lshrrev_b32 v107, 1, v107\n"
      "v_or_b32 v108, v108, v107\n"
      "s_add_u32 s73, %[ji], 16\n"
      "s_mul_i32 s73, s73, 86\n"
      "s_lshr_b32 s73, s73, 8\n"
      "v_mov_b32 v109, s73\n"
      "v_mov_b32 v91, s73\n"
      "s_add_u32 s73, %[ji], 32\n"
      "s_mul_i32 s73, s73, 86\n"
      "s_lshr_b32 s73, s73, 8\n"
      "v_mov_b32 v110, s73\n"
      "v_cmp_lt_u32 vcc, 63, v104\n"
      "s_mul_i32 s73, %[ji], 86\n"
      "s_lshr_b32 s73, s73, 8\n"
      "v_cndmask_b32 v109, v109, v110, vcc\n"
      "v_mov_b32 v110, s73\n"
      "v_cmp_lt_u32 vcc, 255, v104\n"
      "s_lshl_b32 s73, %[ti], 6\n"
      "v_add_u32 v109, s73, v109\n"
      "v_cndmask_b32 v91, v91, v110, vcc\n"
      "v_and_b32 v110, 0x30, v104\n"
      "s_lshl_b32 s73, %[gi], 4\n"
      "v_add_u32 v109, v109, v110\n"
      "v_cmp_lt_u32 vcc, 127, v104\n"
      "v_add_u32 v91, s73, v91\n"
      "s_nop 0\n"
      "v_cndmask_b32 v91, v109, v91, vcc\n"
      "v_lshlrev_b32 v91, 7, v91\n"
      "v_add_u32 v91, 4096, v91\n"
      "s_waitcnt vmcnt(4) lgkmcnt(0)\n"
      "s_barrier\n"
      "ds_read_b128 v[128:131], v8 offset:0\n"
      "ds_read_b64_tr_b16 v[48:49], v12 offset:0\n"
      "ds_read_b64_tr_b16 v[50:51], v12 offset:768\n"
      "ds_read_b64_tr_b16 v[52:53], v12 offset:64\n"
      "ds_read_b64_tr_b16 v[54:55], v12 offset:832\n"
      "ds_read_b128 v[132:135], v9 offset:0\n"
      "ds_read_b64_tr_b16 v[56:57], v13 offset:0\n"
      "ds_read_b64_tr_b16 v[58:59], v13 offset:768\n"
      "ds_read_b64_tr_b16 v[60:61], v13 offset:64\n"
      "ds_read_b64_tr_b16 v[62:63], v13 offset:832\n"
      "s_waitcnt lgkmcnt(5)\n"
      "s_add_u32 s72, %[rot], 2\n"
      "s_and_b32 s72, s72, 7\n"
      "s_lshl_b32 s68, s72, 7\n"
      "s_mul_i32 s69, s72, 196608\n"
      "v_mfma_f32_32x32x16_bf16 v[16:31], v[48:51], v[128:131], 0\n"
      "ds_read_b128 v[136:139], v10 offset:0\n"
      "ds_read_b64_tr_b16 v[64:65], v14 offset:0\n"
      "ds_read_b64_tr_b16 v[66:67], v14 offset:768\n"
      "v_lshlrev_b32 v4, 16, v128\n"
      "v_and_b32 v5, s71, v128\n"
      "v_fmac_f32 v2, v4, v4\n"
      "v_fmac_f32 v3, v5, v5\n"
      "v_lshlrev_b32 v6, 16, v129\n"
      "v_and_b32 v7, s71, v129\n"
      "v_fmac_f32 v2, v6, v6\n"
      "v_fmac_f32 v3, v7, v7\n"
      "v_mfma_f32_16x16x32_bf16 v[120:123], v[92:95], v[48:51], 0\n"
      "s_add_u32 m0, %[sx], 57344\n"
      "v_mfma_f32_32x32x16_bf16 v[32:47], v[52:55], v[128:131], 0\n"
      "buffer_load_dwordx4 %[vx0], %[srdx], s68 offen lds\n"
      "ds_read_b64_tr_b16 v[68:69], v14 offset:64\n"
      "ds_read_b64_tr_b16 v[70:71], v14 offset:832\n"
      "s_add_u32 m0, %[sx], 58368\n"
      "s_nop 0\n"
      "buffer_load_dwordx4 %[vx1], %[srdx], s68 offen lds\n"
      "s_add_u32 m0, %[sq], 57344\n"
      "s_nop 0\n"
      "buffer_load_dwordx4 %[vq0], %[srdq], s69 offen lds\n"
      "v_lshlrev_b32 v4, 16, v130\n"
      "v_and_b32 v5, s71, v130\n"
      "v_fmac_f32 v2, v4, v4\n"
      "v_fmac_f32 v3, v5, v5\n"
      "v_lshlrev_b32 v6, 16, v131\n"
      "v_and_b32 v7, s71, v131\n"
      "v_fmac_f32 v2, v6, v6\n"
      "v_fmac_f32 v3, v7, v7\n"
      "v_mfma_f32_16x16x32_bf16 v[124:127], v[92:95], v[52:55], 0\n"
      "s_waitcnt lgkmcnt(5)\n"
      "s_add_u32 s72, %[rot], 3\n"
      "s_and_b32 s72, s72, 7\n"
      "s_lshl_b32 s68, s72, 7\n"
      "s_mul_i32 s69, s72, 196608\n"
      "v_mfma_f32_32x32x16_bf16 v[16:31], v[56:59], v[132:135], v[16:31]\n"
      "ds_read_b128 v[140:143], v11 offset:0\n"
      "ds_read_b64_tr_b16 v[48:49], v15 offset:0\n"
      "ds_read_b64_tr_b16 v[50:51], v15 offset:768\n"
      "s_add_u32 m0, %[sx], 86016\n"
      "v_mfma_f32_32x32x16_bf16 v[32:47], v[60:63], v[132:135], v[32:47]\n"
      "buffer_load_dwordx4 %[vx0], %[srdx], s68 offen lds\n"
      "ds_read_b64_tr_b16 v[52:53], v15 offset:64\n"
      "ds_read_b64_tr_b16 v[54:55], v15 offset:832\n"
      "s_add_u32 m0, %[sx], 87040\n"
      "s_nop 0\n"
      "buffer_load_dwordx4 %[vx1], %[srdx], s68 offen lds\n"
      "s_add_u32 m0, %[sq], 86016\n"
      "s_nop 0\n"
      "buffer_load_dwordx4 %[vq0], %[srdq], s69 offen lds\n"
      "s_waitcnt vmcnt(7)\n"
      "s_barrier\n"
      "s_waitcnt lgkmcnt(5)\n"
      "s_add_u32 s72, %[rot], 4\n"
      "s_and_b32 s72, s72, 7\n"
      "s_lshl_b32 s68, s72, 7\n"
      "v_mfma_f32_32x32x16_bf16 v[16:31], v[64:67], v[136:139], v[16:31]\n"
      "ds_read_b128 v[144:147], v8 offset:28672\n"
      "ds_read_b64_tr_b16 v[56:57], v12 offset:28672\n"
      "ds_read_b64_tr_b16 v[58:59], v12 offset:29440\n"
      "v_lshlrev_b32 v4, 16, v136\n"
      "v_and_b32 v5, s71, v136\n"
      "v_fmac_f32 v2, v4, v4\n"
      "v_fmac_f32 v3, v5, v5\n"
      "v_lshlrev_b32 v6, 16, v137\n"
      "v_and_b32 v7, s71, v137\n"
      "v_fmac_f32 v2, v6, v6\n"
      "v_fmac_f32 v3, v7, v7\n"
      "s_mul_i32 s69, s72, 196608\n"
      "s_add_u32 m0, %[sx], 114688\n"
      "v_mfma_f32_32x32x16_bf16 v[32:47], v[68:71], v[136:139], v[32:47]\n"
      "buffer_load_dwordx4 %[vx0], %[srdx], s68 offen lds\n"
      "ds_read_b64_tr_b16 v[60:61], v12 offset:28736\n"
      "ds_read_b64_tr_b16 v[62:63], v12 offset:29504\n"
      "v_lshlrev_b32 v4, 16, v138\n"
      "v_and_b32 v5, s71, v138\n"
      "v_fmac_f32 v2, v4, v4\n"
      "v_fmac_f32 v3, v5, v5\n"
      "v_lshlrev_b32 v6, 16, v139\n"
      "v_and_b32 v7, s71, v139\n"
      "v_fmac_f32 v2, v6, v6\n"
      "v_fmac_f32 v3, v7, v7\n"
      "s_waitcnt lgkmcnt(5)\n"
      "s_add_u32 m0, %[sx], 115712\n"
      "v_mfma_f32_32x32x16_bf16 v[16:31], v[48:51], v[140:143], v[16:31]\n"
      "buffer_load_dwordx4 %[vx1], %[srdx], s68 offen lds\n"
      "ds_read_b128 v[148:151], v9 offset:28672\n"
      "ds_read_b64_tr_b16 v[64:65], v13 offset:28672\n"
      "ds_read_b64_tr_b16 v[66:67], v13 offset:29440\n"
      "v_mfma_f32_32x32x16_bf16 v[32:47], v[52:55], v[140:143], v[32:47]\n"
      "ds_read_b64_tr_b16 v[68:69], v13 offset:28736\n"
      "ds_read_b64_tr_b16 v[70:71], v13 offset:29504\n"
      "s_waitcnt lgkmcnt(5)\n"
      "s_add_u32 m0, %[sq], 114688\n"
      "v_mfma_f32_32x32x16_bf16 v[16:31], v[56:59], v[144:147], v[16:31]\n"
      "buffer_load_dwordx4 %[vq0], %[srdq], s69 offen lds\n"
      "ds_read_b128 v[152:155], v10 offset:28672\n"
      "ds_read_b64_tr_b16 v[48:49], v14 offset:28672\n"
      "ds_read_b64_tr_b16 v[50:51], v14 offset:29440\n"
      "v_lshlrev_b32 v4, 16, v144\n"
      "v_and_b32 v5, s71, v144\n"
      "v_fmac_f32 v2, v4, v4\n"
      "v_fmac_f32 v3, v5, v5\n"
      "v_lshlrev_b32 v6, 16, v145\n"
      "v_and_b32 v7, s71, v145\n"
      "v_fmac_f32 v2, v6, v6\n"
      "v_fmac_f32 v3, v7, v7\n"
      "v_mfma_f32_16x16x32_bf16 v[120:123], v[92:95], v[56:59], v[120:123]\n"
      "v_mfma_f32_32x32x16_bf16 v[32:47], v[60:63], v[144:147], v[32:47]\n"
      "ds_read_b64_tr_b16 v[52:53], v14 offset:28736\n"
      "ds_read_b64_tr_b16 v[54:55], v14 offset:29504\n"
      "v_lshlrev_b32 v4, 16, v146\n"
      "v_and_b32 v5, s71, v146\n"
      "v_fmac_f32 v2, v4, v4\n"
      "v_fmac_f32 v3, v5, v5\n"
      "v_lshlrev_b32 v6, 16, v147\n"
      "v_and_b32 v7, s71, v147\n"
      "v_fmac_f32 v2, v6, v6\n"
      "v_fmac_f32 v3, v7, v7\n"
      "v_mfma_f32_16x16x32_bf16 v[124:127], v[92:95], v[60:63], v[124:127]\n"
      "s_waitcnt lgkmcnt(5)\n"
      "v_mfma_f32_32x32x16_bf16 v[16:31], v[64:67], v[148:151], v[16:31]\n"
      "ds_read_b128 v[156:159], v11 offset:28672\n"
      "ds_read_b64_tr_b16 v[56:57], v15 offset:28672\n"
      "ds_read_b64_tr_b16 v[58:59], v15 offset:29440\n"
      "v_mfma_f32_32x32x16_bf16 v[32:47], v[68:71], v[148:151], v[32:47]\n"
      "ds_read_b64_tr_b16 v[60:61], v15 offset:28736\n"
      "ds_read_b64_tr_b16 v[62:63], v15 offset:29504\n"
      "s_waitcnt vmcnt(6)\n"
      "s_barrier\n"
      "s_waitcnt lgkmcnt(5)\n"
      "s_add_u32 s72, %[rot], 5\n"
      "s_and_b32 s72, s72, 7\n"
      "s_lshl_b32 s68, s72, 7\n"
      "v_mfma_f32_32x32x16_bf16 v[16:31], v[48:51], v[152:155], v[16:31]\n"
      "ds_read_b128 v[160:163], v72 offset:0\n"
      "ds_read_b64_tr_b16 v[64:65], v76 offset:0\n"
      "ds_read_b64_tr_b16 v[66:67], v76 offset:768\n"
      "v_lshlrev_b32 v4, 16, v152\n"
      "v_and_b32 v5, s71, v152\n"
      "v_fmac_f32 v2, v4, v4\n"
      "v_fmac_f32 v3, v5, v5\n"
      "v_lshlrev_b32 v6, 16, v153\n"
      "v_and_b32 v7, s71, v153\n"
      "v_fmac_f32 v2, v6, v6\n"
      "v_fmac_f32 v3, v7, v7\n"
      "s_mul_i32 s69, s72, 196608\n"
      "s_add_u32 m0, %[sx], 0\n"
      "v_mfma_f32_32x32x16_bf16 v[32:47], v[52:55], v[152:155], v[32:47]\n"
      "buffer_load_dwordx4 %[vx0], %[srdx], s68 offen lds\n"
      "ds_read_b64_tr_b16 v[68:69], v76 offset:64\n"
      "ds_read_b64_tr_b16 v[70:71], v76 offset:832\n"
      "v_lshlrev_b32 v4, 16, v154\n"
      "v_and_b32 v5, s71, v154\n"
      "v_fmac_f32 v2, v4, v4\n"
      "v_fmac_f32 v3, v5, v5\n"
      "v_lshlrev_b32 v6, 16, v155\n"
      "v_and_b32 v7, s71, v155\n"
      "v_fmac_f32 v2, v6, v6\n"
      "v_fmac_f32 v3, v7, v7\n"
      "s_waitcnt lgkmcnt(5)\n"
      "s_add_u32 m0, %[sx], 1024\n"
      "v_mfma_f32_32x32x16_bf16 v[16:31], v[56:59], v[156:159], v[16:31]\n"
      "buffer_load_dwordx4 %[vx1], %[srdx], s68 offen lds\n"
      "ds_read_b128 v[164:167], v73 offset:0\n"
      "ds_read_b64_tr_b16 v[48:49], v77 offset:0\n"
      "ds_read_b64_tr_b16 v[50:51], v77 offset:768\n"
      "v_mfma_f32_32x32x16_bf16 v[32:47], v[60:63], v[156:159], v[32:47]\n"
      "ds_read_b64_tr_b16 v[52:53], v77 offset:64\n"
      "ds_read_b64_tr_b16 v[54:55], v77 offset:832\n"
      "s_waitcnt lgkmcnt(5)\n"
      "s_add_u32 m0, %[sq], 0\n"
      "v_mfma_f32_32x32x16_bf16 v[16:31], v[64:67], v[160:163], v[16:31]\n"
      "buffer_load_dwordx4 %[vq0], %[srdq], s69 offen lds\n"
      "ds_read_b128 v[168:171], v74 offset:0\n"
      "ds_read_b64_tr_b16 v[56:57], v78 offset:0\n"
      "ds_read_b64_tr_b16 v[58:59], v78 offset:768\n"
      "v_lshlrev_b32 v4, 16, v160\n"
      "v_and_b32 v5, s71, v160\n"
      "v_fmac_f32 v2, v4, v4\n"
      "v_fmac_f32 v3, v5, v5\n"
      "v_lshlrev_b32 v6, 16, v161\n"
      "v_and_b32 v7, s71, v161\n"
      "v_fmac_f32 v2, v6, v6\n"
      "v_fmac_f32 v3, v7, v7\n"
      "v_mfma_f32_16x16x32_bf16 v[120:123], v[92:95], v[64:67], v[120:123]\n"
      "v_mfma_f32_32x32x16_bf16 v[32:47], v[68:71], v[160:163], v[32:47]\n"
      "ds_read_b64_tr_b16 v[60:61], v78 offset:64\n"
      "ds_read_b64_tr_b16 v[62:63], v78 offset:832\n"
      "v_lshlrev_b32 v4, 16, v162\n"
      "v_and_b32 v5, s71, v162\n"
      "v_fmac_f32 v2, v4, v4\n"
      "v_fmac_f32 v3, v5, v5\n"
      "v_lshlrev_b32 v6, 16, v163\n"
      "v_and_b32 v7, s71, v163\n"
      "v_fmac_f32 v2, v6, v6\n"
      "v_fmac_f32 v3, v7, v7\n"
      "v_mfma_f32_16x16x32_bf16 v[124:127], v[92:95], v[68:71], v[124:127]\n"
      "s_waitcnt lgkmcnt(5)\n"
      "v_mfma_f32_32x32x16_bf16 v[16:31], v[48:51], v[164:167], v[16:31]\n"
      "ds_read_b128 v[172:175], v75 offset:0\n"
      "ds_read_b64_tr_b16 v[64:65], v79 offset:0\n"
      "ds_read_b64_tr_b16 v[66:67], v79 offset:768\n"
      "v_mfma_f32_32x32x16_bf16 v[32:47], v[52:55], v[164:167], v[32:47]\n"
      "ds_read_b64_tr_b16 v[68:69], v79 offset:64\n"
      "ds_read_b64_tr_b16 v[70:71], v79 offset:832\n"
      "s_waitcnt vmcnt(6)\n"
      "s_barrier\n"
      "s_waitcnt lgkmcnt(5)\n"
      "s_add_u32 s72, %[rot], 6\n"
      "s_and_b32 s72, s72, 7\n"
      "s_lshl_b32 s68, s72, 7\n"
      "v_mfma_f32_32x32x16_bf16 v[16:31], v[56:59], v[168:171], v[16:31]\n"
      "ds_read_b128 v[176:179], v72 offset:28672\n"
      "ds_read_b64_tr_b16 v[48:49], v76 offset:28672\n"
      "ds_read_b64_tr_b16 v[50:51], v76 offset:29440\n"
      "v_lshlrev_b32 v4, 16, v168\n"
      "v_and_b32 v5, s71, v168\n"
      "v_fmac_f32 v2, v4, v4\n"
      "v_fmac_f32 v3, v5, v5\n"
      "v_lshlrev_b32 v6, 16, v169\n"
      "v_and_b32 v7, s71, v169\n"
      "v_fmac_f32 v2, v6, v6\n"
      "v_fmac_f32 v3, v7, v7\n"
      "s_mul_i32 s69, s72, 196608\n"
      "s_add_u32 m0, %[sx], 28672\n"
      "v_mfma_f32_32x32x16_bf16 v[32:47], v[60:63], v[168:171], v[32:47]\n"
      "buffer_load_dwordx4 %[vx0], %[srdx], s68 offen lds\n"
      "ds_read_b64_tr_b16 v[52:53], v76 offset:28736\n"
      "ds_read_b64_tr_b16 v[54:55], v76 offset:29504\n"
      "v_lshlrev_b32 v4, 16, v170\n"
      "v_and_b32 v5, s71, v170\n"
      "v_fmac_f32 v2, v4, v4\n"
      "v_fmac_f32 v3, v5, v5\n"
      "v_lshlrev_b32 v6, 16, v171\n"
      "v_and_b32 v7, s71, v171\n"
      "v_fmac_f32 v2, v6, v6\n"
      "v_fmac_f32 v3, v7, v7\n"
      "s_waitcnt lgkmcnt(5)\n"
      "s_add_u32 m0, %[sx], 29696\n"
      "v_mfma_f32_32x32x16_bf16 v[16:31], v[64:67], v[172:175], v[16:31]\n"
      "buffer_load_dwordx4 %[vx1], %[srdx], s68 offen lds\n"
      "ds_read_b128 v[180:183], v73 offset:28672\n"
      "ds_read_b64_tr_b16 v[56:57], v77 offset:28672\n"
      "ds_read_b64_tr_b16 v[58:59], v77 offset:29440\n"
      "v_mfma_f32_32x32x16_bf16 v[32:47], v[68:71], v[172:175], v[32:47]\n"
      "ds_read_b64_tr_b16 v[60:61], v77 offset:28736\n"
      "ds_read_b64_tr_b16 v[62:63], v77 offset:29504\n"
      "s_waitcnt lgkmcnt(5)\n"
      "s_add_u32 m0, %[sq], 28672\n"
      "v_mfma_f32_32x32x16_bf16 v[16:31], v[48:51], v[176:179], v[16:31]\n"
      "buffer_load_dwordx4 %[vq0], %[srdq], s69 offen lds\n"
      "ds_read_b128 v[184:187], v74 offset:28672\n"
      "ds_read_b64_tr_b16 v[64:65], v78 offset:28672\n"
      "ds_read_b64_tr_b16 v[66:67], v78 offset:29440\n"
      "v_lshlrev_b32 v4, 16, v176\n"
      "v_and_b32 v5, s71, v176\n"
      "v_fmac_f32 v2, v4, v4\n"
      "v_fmac_f32 v3, v5, v5\n"
      "v_lshlrev_b32 v6, 16, v177\n"
      "v_and_b32 v7, s71, v177\n"
      "v_fmac_f32 v2, v6, v6\n"
      "v_fmac_f32 v3, v7, v7\n"
      "v_mfma_f32_16x16x32_bf16 v[120:123], v[92:95], v[48:51], v[120:123]\n"
      "v_mfma_f32_32x32x16_bf16 v[32:47], v[52:55], v[176:179], v[32:47]\n"
      "ds_read_b64_tr_b16 v[68:69], v78 offset:28736\n"
      "ds_read_b64_tr_b16 v[70:71], v78 offset:29504\n"
      "v_lshlrev_b32 v4, 16, v178\n"
      "v_and_b32 v5, s71, v178\n"
      "v_fmac_f32 v2, v4, v4\n"
      "v_fmac_f32 v3, v5, v5\n"
      "v_lshlrev_b32 v6, 16, v179\n"
      "v_and_b32 v7, s71, v179\n"
      "v_fmac_f32 v2, v6, v6\n"
      "v_fmac_f32 v3, v7, v7\n"
      "v_mfma_f32_16x16x32_bf16 v[124:127], v[92:95], v[52:55], v[124:127]\n"
      "s_waitcnt lgkmcnt(5)\n"
      "v_mfma_f32_32x32x16_bf16 v[16:31], v[56:59], v[180:183], v[16:31]\n"
      "ds_read_b128 v[188:191], v75 offset:28672\n"
      "ds_read_b64_tr_b16 v[48:49], v79 offset:28672\n"
      "ds_read_b64_tr_b16 v[50:51], v79 offset:29440\n"
      "v_mfma_f32_32x32x16_bf16 v[32:47], v[60:63], v[180:183], v[32:47]\n"
      "ds_read_b64_tr_b16 v[52:53], v79 offset:28736\n"
      "ds_read_b64_tr_b16 v[54:55], v79 offset:29504\n"
      "s_waitcnt vmcnt(6)\n"
      "s_barrier\n"
      "s_waitcnt lgkmcnt(5)\n"
      "s_add_u32 s72, %[rot], 7\n"
      "s_and_b32 s72, s72, 7\n"
      "s_lshl_b32 s68, s72, 7\n"
      "v_mfma_f32_32x32x16_bf16 v[16:31], v[64:67], v[184:187], v[16:31]\n"
      "ds_read_b128 v[192:195], v80 offset:0\n"
      "ds_read_b64_tr_b16 v[56:57], v84 offset:0\n"
      "ds_read_b64_tr_b16 v[58:59], v84 offset:768\n"
      "v_lshlrev_b32 v4, 16, v184\n"
      "v_and_b32 v5, s71, v184\n"
      "v_fmac_f32 v2, v4, v4\n"
      "v_fmac_f32 v3, v5, v5\n"
      "v_lshlrev_b32 v6, 16, v185\n"
      "v_and_b32 v7, s71, v185\n"
      "v_fmac_f32 v2, v6, v6\n"
      "v_fmac_f32 v3, v7, v7\n"
      "s_mul_i32 s69, s72, 196608\n"
      "s_add_u32 m0, %[sx], 57344\n"
      "v_mfma_f32_32x32x16_bf16 v[32:47], v[68:71], v[184:187], v[32:47]\n"
      "buffer_load_dwordx4 %[vx0], %[srdx], s68 offen lds\n"
      "ds_read_b64_tr_b16 v[60:61], v84 offset:64\n"
      "ds_read_b64_tr_b16 v[62:63], v84 offset:832\n"
      "v_lshlrev_b32 v4, 16, v186\n"
      "v_and_b32 v5, s71, v186\n"
      "v_fmac_f32 v2, v4, v4\n"
      "v_fmac_f32 v3, v5, v5\n"
      "v_lshlrev_b32 v6, 16, v187\n"
      "v_and_b32 v7, s71, v187\n"
      "v_fmac_f32 v2, v6, v6\n"
      "v_fmac_f32 v3, v7, v7\n"
      "s_waitcnt lgkmcnt(5)\n"
      "s_add_u32 m0, %[sx], 58368\n"
      "v_mfma_f32_32x32x16_bf16 v[16:31], v[48:51], v[188:191], v[16:31]\n"
      "buffer_load_dwordx4 %[vx1], %[srdx], s68 offen lds\n"
      "ds_read_b128 v[196:199], v81 offset:0\n"
      "ds_read_b64_tr_b16 v[64:65], v85 offset:0\n"
      "ds_read_b64_tr_b16 v[66:67], v85 offset:768\n"
      "v_mfma_f32_32x32x16_bf16 v[32:47], v[52:55], v[188:191], v[32:47]\n"
      "ds_read_b64_tr_b16 v[68:69], v85 offset:64\n"
      "ds_read_b64_tr_b16 v[70:71], v85 offset:832\n"
      "s_waitcnt lgkmcnt(5)\n"
      "s_add_u32 m0, %[sq], 57344\n"
      "v_mfma_f32_32x32x16_bf16 v[16:31], v[56:59], v[192:195], v[16:31]\n"
      "buffer_load_dwordx4 %[vq0], %[srdq], s69 offen lds\n"
      "ds_read_b128 v[200:203], v82 offset:0\n"
      "ds_read_b64_tr_b16 v[48:49], v86 offset:0\n"
      "ds_read_b64_tr_b16 v[50:51], v86 offset:768\n"
      "v_lshlrev_b32 v4, 16, v192\n"
      "v_and_b32 v5, s71, v192\n"
      "v_fmac_f32 v2, v4, v4\n"
      "v_fmac_f32 v3, v5, v5\n"
      "v_lshlrev_b32 v6, 16, v193\n"
      "v_and_b32 v7, s71, v193\n"
      "v_fmac_f32 v2, v6, v6\n"
      "v_fmac_f32 v3, v7, v7\n"
      "v_mfma_f32_16x16x32_bf16 v[120:123], v[92:95], v[56:59], v[120:123]\n"
      "v_mfma_f32_32x32x16_bf16 v[32:47], v[60:63], v[192:195], v[32:47]\n"
      "ds_read_b64_tr_b16 v[52:53], v86 offset:64\n"
      "ds_read_b64_tr_b16 v[54:55], v86 offset:832\n"
      "v_lshlrev_b32 v4, 16, v194\n"
      "v_and_b32 v5, s71, v194\n"
      "v_fmac_f32 v2, v4, v4\n"
      "v_fmac_f32 v3, v5, v5\n"
      "v_lshlrev_b32 v6, 16, v195\n"
      "v_and_b32 v7, s71, v195\n"
      "v_fmac_f32 v2, v6, v6\n"
      "v_fmac_f32 v3, v7, v7\n"
      "v_mfma_f32_16x16x32_bf16 v[124:127], v[92:95], v[60:63], v[124:127]\n"
      "s_waitcnt lgkmcnt(5)\n"
      "v_mfma_f32_32x32x16_bf16 v[16:31], v[64:67], v[196:199], v[16:31]\n"
      "ds_read_b128 v[204:207], v83 offset:0\n"
      "ds_read_b64_tr_b16 v[56:57], v87 offset:0\n"
      "ds_read_b64_tr_b16 v[58:59], v87 offset:768\n"
      "v_mfma_f32_32x32x16_bf16 v[32:47], v[68:71], v[196:199], v[32:47]\n"
      "ds_read_b64_tr_b16 v[60:61], v87 offset:64\n"
      "ds_read_b64_tr_b16 v[62:63], v87 offset:832\n"
      "s_waitcnt vmcnt(6)\n"
      "s_barrier\n"
      "s_waitcnt lgkmcnt(5)\n"
      "s_add_u32 s72, %[rot], 0\n"
      "s_and_b32 s72, s72, 7\n"
      "v_mfma_f32_32x32x16_bf16 v[16:31], v[48:51], v[200:203], v[16:31]\n"
      "ds_read_b128 v[208:211], v8 offset:0\n"
      "ds_read_b64_tr_b16 v[64:65], v12 offset:0\n"
      "ds_read_b64_tr_b16 v[66:67], v12 offset:768\n"
      "v_lshlrev_b32 v4, 16, v200\n"
      "v_and_b32 v5, s71, v200\n"
      "v_fmac_f32 v2, v4, v4\n"
      "v_fmac_f32 v3, v5, v5\n"
      "v_lshlrev_b32 v6, 16, v201\n"
      "v_and_b32 v7, s71, v201\n"
      "v_fmac_f32 v2, v6, v6\n"
      "v_fmac_f32 v3, v7, v7\n"
      "s_mul_i32 s70, s72, 176128\n"
      "s_add_u32 m0, %[sg], 86016\n"
      "v_mfma_f32_32x32x16_bf16 v[32:47], v[52:55], v[200:203], v[32:47]\n"
      "buffer_load_dwordx4 v88, %[srdg], s70 offen lds\n"
      "ds_read_b64_tr_b16 v[68:69], v12 offset:64\n"
      "ds_read_b64_tr_b16 v[70:71], v12 offset:832\n"
      "v_lshlrev_b32 v4, 16, v202\n"
      "v_and_b32 v5, s71, v202\n"
      "v_fmac_f32 v2, v4, v4\n"
      "v_fmac_f32 v3, v5, v5\n"
      "v_lshlrev_b32 v6, 16, v203\n"
      "v_and_b32 v7, s71, v203\n"
      "v_fmac_f32 v2, v6, v6\n"
      "v_fmac_f32 v3, v7, v7\n"
      "s_waitcnt lgkmcnt(5)\n"
      "s_add_u32 m0, %[sg], 87040\n"
      "v_mfma_f32_32x32x16_bf16 v[16:31], v[56:59], v[204:207], v[16:31]\n"
      "buffer_load_dwordx4 v89, %[srdg], s70 offen lds\n"
      "ds_read_b128 v[212:215], v9 offset:0\n"
      "ds_read_b64_tr_b16 v[48:49], v13 offset:0\n"
      "ds_read_b64_tr_b16 v[50:51], v13 offset:768\n"
      "v_mfma_f32_32x32x16_bf16 v[32:47], v[60:63], v[204:207], v[32:47]\n"
      "ds_read_b64_tr_b16 v[52:53], v13 offset:64\n"
      "ds_read_b64_tr_b16 v[54:55], v13 offset:832\n"
      "s_waitcnt lgkmcnt(5)\n"
      "s_add_u32 m0, %[sg], 88064\n"
      "v_mfma_f32_32x32x16_bf16 v[16:31], v[64:67], v[208:211], v[16:31]\n"
      "buffer_load_dwordx4 v90, %[srdg], s70 offen lds\n"
      "ds_read_b128 v[216:219], v10 offset:0\n"
      "ds_read_b64_tr_b16 v[56:57], v14 offset:0\n"
      "ds_read_b64_tr_b16 v[58:59], v14 offset:768\n"
      "v_lshlrev_b32 v4, 16, v208\n"
      "v_and_b32 v5, s71, v208\n"
      "v_fmac_f32 v2, v4, v4\n"
      "v_fmac_f32 v3, v5, v5\n"
      "v_lshlrev_b32 v6, 16, v209\n"
      "v_and_b32 v7, s71, v209\n"
      "v_fmac_f32 v2, v6, v6\n"
      "v_fmac_f32 v3, v7, v7\n"
      "v_mfma_f32_16x16x32_bf16 v[120:123], v[92:95], v[64:67], v[120:123]\n"
      "v_mfma_f32_32x32x16_bf16 v[32:47], v[68:71], v[208:211], v[32:47]\n"
      "ds_read_b64_tr_b16 v[60:61], v14 offset:64\n"
      "ds_read_b64_tr_b16 v[62:63], v14 offset:832\n"
      "v_lshlrev_b32 v4, 16, v210\n"
      "v_and_b32 v5, s71, v210\n"
      "v_fmac_f32 v2, v4, v4\n"
      "v_fmac_f32 v3, v5, v5\n"
      "v_lshlrev_b32 v6, 16, v211\n"
      "v_and_b32 v7, s71, v211\n"
      "v_fmac_f32 v2, v6, v6\n"
      "v_fmac_f32 v3, v7, v7\n"
      "v_mfma_f32_16x16x32_bf16 v[124:127], v[92:95], v[68:71], v[124:127]\n"
      "s_waitcnt lgkmcnt(5)\n"
      "v_mfma_f32_32x32x16_bf16 v[16:31], v[48:51], v[212:215], v[16:31]\n"
      "ds_read_b128 v[220:223], v11 offset:0\n"
      "ds_read_b64_tr_b16 v[64:65], v15 offset:0\n"
      "ds_read_b64_tr_b16 v[66:67], v15 offset:768\n"
      "v_mfma_f32_32x32x16_bf16 v[32:47], v[52:55], v[212:215], v[32:47]\n"
      "ds_read_b64_tr_b16 v[68:69], v15 offset:64\n"
      "ds_read_b64_tr_b16 v[70:71], v15 offset:832\n"
      "s_waitcnt vmcnt(6)\n"
      "s_barrier\n"
      "s_waitcnt lgkmcnt(5)\n"
      "s_add_u32 s72, %[rot], 1\n"
      "s_and_b32 s72, s72, 7\n"
      "v_mfma_f32_32x32x16_bf16 v[16:31], v[56:59], v[216:219], v[16:31]\n"
      "ds_read_b128 v[224:227], v8 offset:28672\n"
      "ds_read_b64_tr_b16 v[48:49], v12 offset:28672\n"
      "ds_read_b64_tr_b16 v[50:51], v12 offset:29440\n"
      "v_lshlrev_b32 v4, 16, v216\n"
      "v_and_b32 v5, s71, v216\n"
      "v_fmac_f32 v2, v4, v4\n"
      "v_fmac_f32 v3, v5, v5\n"
      "v_lshlrev_b32 v6, 16, v217\n"
      "v_and_b32 v7, s71, v217\n"
      "v_fmac_f32 v2, v6, v6\n"
      "v_fmac_f32 v3, v7, v7\n"
      "s_mul_i32 s70, s72, 176128\n"
      "s_add_u32 m0, %[sg], 114688\n"
      "v_mfma_f32_32x32x16_bf16 v[32:47], v[60:63], v[216:219], v[32:47]\n"
      "buffer_load_dwordx4 v88, %[srdg], s70 offen lds\n"
      "ds_read_b64_tr_b16 v[52:53], v12 offset:28736\n"
      "ds_read_b64_tr_b16 v[54:55], v12 offset:29504\n"
      "v_lshlrev_b32 v4, 16, v218\n"
      "v_and_b32 v5, s71, v218\n"
      "v_fmac_f32 v2, v4, v4\n"
      "v_fmac_f32 v3, v5, v5\n"
      "v_lshlrev_b32 v6, 16, v219\n"
      "v_and_b32 v7, s71, v219\n"
      "v_fmac_f32 v2, v6, v6\n"
      "v_fmac_f32 v3, v7, v7\n"
      "s_waitcnt lgkmcnt(5)\n"
      "s_add_u32 m0, %[sg], 115712\n"
      "v_mfma_f32_32x32x16_bf16 v[16:31], v[64:67], v[220:223], v[16:31]\n"
      "buffer_load_dwordx4 v89, %[srdg], s70 offen lds\n"
      "ds_read_b128 v[228:231], v9 offset:28672\n"
      "ds_read_b64_tr_b16 v[56:57], v13 offset:28672\n"
      "ds_read_b64_tr_b16 v[58:59], v13 offset:29440\n"
      "v_mfma_f32_32x32x16_bf16 v[32:47], v[68:71], v[220:223], v[32:47]\n"
      "ds_read_b64_tr_b16 v[60:61], v13 offset:28736\n"
      "ds_read_b64_tr_b16 v[62:63], v13 offset:29504\n"
      "s_waitcnt lgkmcnt(5)\n"
      "s_add_u32 m0, %[sg], 116736\n"
      "v_mfma_f32_32x32x16_bf16 v[16:31], v[48:51], v[224:227], v[16:31]\n"
      "buffer_load_dwordx4 v90, %[srdg], s70 offen lds\n"
      "ds_read_b128 v[232:235], v10 offset:28672\n"
      "ds_read_b64_tr_b16 v[64:65], v14 offset:28672\n"
      "ds_read_b64_tr_b16 v[66:67], v14 offset:29440\n"
      "v_lshlrev_b32 v4, 16, v224\n"
      "v_and_b32 v5, s71, v224\n"
      "v_fmac_f32 v2, v4, v4\n"
      "v_fmac_f32 v3, v5, v5\n"
      "v_lshlrev_b32 v6, 16, v225\n"
      "v_and_b32 v7, s71, v225\n"
      "v_fmac_f32 v2, v6, v6\n"
      "v_fmac_f32 v3, v7, v7\n"
      "v_mfma_f32_16x16x32_bf16 v[120:123], v[92:95], v[48:51], v[120:123]\n"
      "v_mfma_f32_32x32x16_bf16 v[32:47], v[52:55], v[224:227], v[32:47]\n"
      "ds_read_b64_tr_b16 v[68:69], v14 offset:28736\n"
      "ds_read_b64_tr_b16 v[70:71], v14 offset:29504\n"
      "v_lshlrev_b32 v4, 16, v226\n"
      "v_and_b32 v5, s71, v226\n"
      "v_fmac_f32 v2, v4, v4\n"
      "v_fmac_f32 v3, v5, v5\n"
      "v_lshlrev_b32 v6, 16, v227\n"
      "v_and_b32 v7, s71, v227\n"
      "v_fmac_f32 v2, v6, v6\n"
      "v_fmac_f32 v3, v7, v7\n"
      "v_mfma_f32_16x16x32_bf16 v[124:127], v[92:95], v[52:55], v[124:127]\n"
      "s_waitcnt lgkmcnt(5)\n"
      "v_mfma_f32_32x32x16_bf16 v[16:31], v[56:59], v[228:231], v[16:31]\n"
      "ds_read_b128 v[236:239], v11 offset:28672\n"
      "ds_read_b64_tr_b16 v[48:49], v15 offset:28672\n"
      "ds_read_b64_tr_b16 v[50:51], v15 offset:29440\n"
      "v_mfma_f32_32x32x16_bf16 v[32:47], v[60:63], v[228:231], v[32:47]\n"
      "ds_read_b64_tr_b16 v[52:53], v15 offset:28736\n"
      "ds_read_b64_tr_b16 v[54:55], v15 offset:29504\n"
      "s_waitcnt vmcnt(6)\n"
      "s_barrier\n"
      "s_waitcnt lgkmcnt(5)\n"
      "s_add_u32 s72, %[rot], 2\n"
      "s_and_b32 s72, s72, 7\n"
      "v_mfma_f32_32x32x16_bf16 v[16:31], v[64:67], v[232:235], v[16:31]\n"
      "ds_read_b128 v[240:243], v72 offset:0\n"
      "ds_read_b64_tr_b16 v[56:57], v76 offset:0\n"
      "ds_read_b64_tr_b16 v[58:59], v76 offset:768\n"
      "v_lshlrev_b32 v4, 16, v232\n"
      "v_and_b32 v5, s71, v232\n"
      "v_fmac_f32 v2, v4, v4\n"
      "v_fmac_f32 v3, v5, v5\n"
      "v_lshlrev_b32 v6, 16, v233\n"
      "v_and_b32 v7, s71, v233\n"
      "v_fmac_f32 v2, v6, v6\n"
      "v_fmac_f32 v3, v7, v7\n"
      "s_mul_i32 s70, s72, 176128\n"
      "s_add_u32 m0, %[sg], 0\n"
      "v_mfma_f32_32x32x16_bf16 v[32:47], v[68:71], v[232:235], v[32:47]\n"
      "buffer_load_dwordx4 v88, %[srdg], s70 offen lds\n"
      "ds_read_b64_tr_b16 v[60:61], v76 offset:64\n"
      "ds_read_b64_tr_b16 v[62:63], v76 offset:832\n"
      "v_lshlrev_b32 v4, 16, v234\n"
      "v_and_b32 v5, s71, v234\n"
      "v_fmac_f32 v2, v4, v4\n"
      "v_fmac_f32 v3, v5, v5\n"
      "v_lshlrev_b32 v6, 16, v235\n"
      "v_and_b32 v7, s71, v235\n"
      "v_fmac_f32 v2, v6, v6\n"
      "v_fmac_f32 v3, v7, v7\n"
      "s_waitcnt lgkmcnt(5)\n"
      "s_add_u32 m0, %[sg], 1024\n"
      "v_mfma_f32_32x32x16_bf16 v[16:31], v[48:51], v[236:239], v[16:31]\n"
      "buffer_load_dwordx4 v89, %[srdg], s70 offen lds\n"
      "ds_read_b128 v[244:247], v73 offset:0\n"
      "ds_read_b64_tr_b16 v[64:65], v77 offset:0\n"
      "ds_read_b64_tr_b16 v[66:67], v77 offset:768\n"
      "v_mfma_f32_32x32x16_bf16 v[32:47], v[52:55], v[236:239], v[32:47]\n"
      "ds_read_b64_tr_b16 v[68:69], v77 offset:64\n"
      "ds_read_b64_tr_b16 v[70:71], v77 offset:832\n"
      "s_waitcnt lgkmcnt(5)\n"
      "s_add_u32 m0, %[sg], 2048\n"
      "v_mfma_f32_32x32x16_bf16 v[16:31], v[56:59], v[240:243], v[16:31]\n"
      "buffer_load_dwordx4 v90, %[srdg], s70 offen lds\n"
      "ds_read_b128 v[248:251], v74 offset:0\n"
      "ds_read_b64_tr_b16 v[48:49], v78 offset:0\n"
      "ds_read_b64_tr_b16 v[50:51], v78 offset:768\n"
      "v_lshlrev_b32 v4, 16, v240\n"
      "v_and_b32 v5, s71, v240\n"
      "v_fmac_f32 v2, v4, v4\n"
      "v_fmac_f32 v3, v5, v5\n"
      "v_lshlrev_b32 v6, 16, v241\n"
      "v_and_b32 v7, s71, v241\n"
      "v_fmac_f32 v2, v6, v6\n"
      "v_fmac_f32 v3, v7, v7\n"
      "v_mfma_f32_16x16x32_bf16 v[120:123], v[92:95], v[56:59], v[120:123]\n"
      "v_mfma_f32_32x32x16_bf16 v[32:47], v[60:63], v[240:243], v[32:47]\n"
      "ds_read_b64_tr_b16 v[52:53], v78 offset:64\n"
      "ds_read_b64_tr_b16 v[54:55], v78 offset:832\n"
      "v_lshlrev_b32 v4, 16, v242\n"
      "v_and_b32 v5, s71, v242\n"
      "v_fmac_f32 v2, v4, v4\n"
      "v_fmac_f32 v3, v5, v5\n"
      "v_lshlrev_b32 v6, 16, v243\n"
      "v_and_b32 v7, s71, v243\n"
      "v_fmac_f32 v2, v6, v6\n"
      "v_fmac_f32 v3, v7, v7\n"
      "v_mfma_f32_16x16x32_bf16 v[124:127], v[92:95], v[60:63], v[124:127]\n"
      "s_waitcnt lgkmcnt(5)\n"
      "v_mfma_f32_32x32x16_bf16 v[16:31], v[64:67], v[244:247], v[16:31]\n"
      "ds_read_b128 v[252:255], v75 offset:0\n"
      "ds_read_b64_tr_b16 v[56:57], v79 offset:0\n"
      "ds_read_b64_tr_b16 v[58:59], v79 offset:768\n"
      "v_mfma_f32_32x32x16_bf16 v[32:47], v[68:71], v[244:247], v[32:47]\n"
      "ds_read_b64_tr_b16 v[60:61], v79 offset:64\n"
      "ds_read_b64_tr_b16 v[62:63], v79 offset:832\n"
      "s_barrier\n"
      "s_waitcnt lgkmcnt(5)\n"
      "s_add_u32 s72, %[rot], 3\n"
      "s_and_b32 s72, s72, 7\n"
      "v_mfma_f32_32x32x16_bf16 v[16:31], v[48:51], v[248:251], v[16:31]\n"
      "v_lshlrev_b32 v4, 16, v248\n"
      "v_and_b32 v5, s71, v248\n"
      "v_fmac_f32 v2, v4, v4\n"
      "v_fmac_f32 v3, v5, v5\n"
      "v_lshlrev_b32 v6, 16, v249\n"
      "v_and_b32 v7, s71, v249\n"
      "v_fmac_f32 v2, v6, v6\n"
      "v_fmac_f32 v3, v7, v7\n"
      "s_mul_i32 s70, s72, 176128\n"
      "s_add_u32 m0, %[sg], 28672\n"
      "v_mfma_f32_32x32x16_bf16 v[32:47], v[52:55], v[248:251], v[32:47]\n"
      "buffer_load_dwordx4 v88, %[srdg], s70 offen lds\n"
      "v_lshlrev_b32 v4, 16, v250\n"
      "v_and_b32 v5, s71, v250\n"
      "v_fmac_f32 v2, v4, v4\n"
      "v_fmac_f32 v3, v5, v5\n"
      "v_lshlrev_b32 v6, 16, v251\n"
      "v_and_b32 v7, s71, v251\n"
      "v_fmac_f32 v2, v6, v6\n"
      "v_fmac_f32 v3, v7, v7\n"
      "s_waitcnt lgkmcnt(0)\n"
      "s_add_u32 m0, %[sg], 29696\n"
      "v_mfma_f32_32x32x16_bf16 v[16:31], v[56:59], v[252:255], v[16:31]\n"
      "buffer_load_dwordx4 v89, %[srdg], s70 offen lds\n"
      "s_add_u32 m0, %[sg], 30720\n"
      "v_mfma_f32_32x32x16_bf16 v[32:47], v[60:63], v[252:255], v[32:47]\n"
      "buffer_load_dwordx4 v90, %[srdg], s70 offen lds\n"
      "v_add_f32 v2, v2, v3\n"
      "s_lshl_b32 s72, %[ti], 2\n"
      "v_mov_b32 v4, v2\n"
      "v_add_u32 v5, s72, v105\n"
      "s_mul_i32 s68, %[ji], 393216\n"
      "v_permlane32_swap_b32 v4, v2\n"
      "s_lshl_b32 s69, %[gi], 12\n"
      "s_add_u32 s68, s68, s69\n"
      "v_add_f32 v2, v2, v4\n"
      "s_lshl_b32 s69, %[ti], 10\n"
      "s_add_u32 s68, s68, s69\n"
      "s_nop 1\n"
      "ds_write_b32 v5, v120 offset:0\n"
      "ds_write_b32 v5, v124 offset:512\n"
      "ds_write_b32 v106, v2 offset:0\n"
      "v_readfirstlane_b32 %[ep0], v116\n"
      "v_readfirstlane_b32 %[ep1], v117\n"
      "s_add_u32 s68, s68, 1048576\n"
      "s_waitcnt vmcnt(6) lgkmcnt(0)\n"
      "s_barrier\n"
      "ds_read_b32 v4, v106 offset:256\n"
      "ds_read_b32 v5, v106\n"
      "ds_read_b32 v6, v106 offset:128\n"
      "ds_read_b128 v[48:51], v105 offset:0\n"
      "ds_read_b128 v[52:55], v105 offset:512\n"
      "s_add_u32 s72, %[rot], 4\n"
      "s_and_b32 s72, s72, 7\n"
      "s_mul_i32 s70, s72, 176128\n"
      "s_add_u32 m0, %[sg], 57344\n"
      "s_nop 0\n"
      "buffer_load_dwordx4 v88, %[srdg], s70 offen lds\n"
      "s_add_u32 m0, %[sg], 58368\n"
      "s_nop 0\n"
      "buffer_load_dwordx4 v89, %[srdg], s70 offen lds\n"
      "s_add_u32 m0, %[sg], 59392\n"
      "s_nop 0\n"
      "buffer_load_dwordx4 v90, %[srdg], s70 offen lds\n"
      "v_mov_b32 v122, 0\n"
      "v_mov_b32 v123, 0\n"
      "v_mov_b32 v58, 0\n"
      "v_mov_b32 v59, 0\n"
      "v_mov_b32 v62, 0\n"
      "v_mov_b32 v63, 0\n"
      "s_mov_b32 s70, 0x3b000000\n"
      "s_waitcnt lgkmcnt(2)\n"
      "v_mul_f32 v4, 0x3b000000, v4\n"
      "v_add_f32 v5, v5, v6\n"
      "v_mul_f32 v6, v4, v4\n"
      "v_mul_f32 v4, -0.5, v4\n"
      "v_fma_f32 v5, v5, s70, -v6\n"
      "v_cvt_pk_bf16_f32 v7, v4, v4\n"
      "v_max_f32 v5, 0, v5\n"
      "v_and_b32 v7, s71, v7\n"
      "v_add_f32 v5, 0x3727c5ac, v5\n"
      "v_sub_f32 v7, v4, v7\n"
      "v_rsq_f32 v124, v5\n"
      "v_cvt_pk_bf16_f32 v120, v4, v7\n"
      "s_waitcnt lgkmcnt(0)\n"
      "v_mov_b32 v121, v120\n"
      "v_add_f32 v48, v48, v49\n"
      "v_add_f32 v50, v50, v51\n"
      "v_add_f32 v48, v48, v50\n"
      "v_cvt_pk_bf16_f32 v56, v48, v48\n"
      "v_and_b32 v49, s71, v56\n"
      "v_sub_f32 v48, v48, v49\n"
      "v_cvt_pk_bf16_f32 v57, v48, v48\n"
      "v_add_f32 v52, v52, v53\n"
      "v_add_f32 v54, v54, v55\n"
      "v_add_f32 v52, v52, v54\n"
      "v_cvt_pk_bf16_f32 v60, v52, v52\n"
      "v_and_b32 v53, s71, v60\n"
      "v_sub_f32 v52, v52, v53\n"
      "v_cvt_pk_bf16_f32 v61, v52, v52\n"
      "s_nop 1\n"
      "v_mfma_f32_32x32x16_bf16 v[16:31], v[56:59], v[120:123], v[16:31]\n"
      "v_mfma_f32_32x32x16_bf16 v[32:47], v[60:63], v[120:123], v[32:47]\n"
      "s_cmp_lt_u32 %[ji], 6\n"
      "s_cselect_b64 vcc, -1, 0\n"
      "s_cselect_b32 s72, 0x3eb8aa3b, 1.0\n"
      "v_cndmask_b32 v4, v108, v104, vcc\n"
      "v_mul_f32 v6, s72, v124\n"
      "v_add_u32 v4, s68, v4\n"
      "s_cmp_lt_u32 %[ji], 5\n"
      "s_cselect_b64 vcc, -1, 0\n"
      "s_cselect_b32 s72, 0x3eb8aa3b, 1.0\n"
      "v_cndmask_b32 v5, v108, v104, vcc\n"
      "v_mul_f32 v7, s72, v124\n"
      "v_add_u32 v5, s68, v5\n"
      "s_nop 1\n"
      "v_mul_f32 v16, v16, v6\n"
      "v_mul_f32 v17, v17, v6\n"
      "v_mul_f32 v18, v18, v6\n"
      "v_mul_f32 v19, v19, v6\n"
      "v_mul_f32 v20, v20, v6\n"
      "v_mul_f32 v21, v21, v6\n"
      "v_mul_f32 v22, v22, v6\n"
      "v_mul_f32 v23, v23, v6\n"
      "v_cvt_pk_bf16_f32 v64, v16, v17\n"
      "v_cvt_pk_bf16_f32 v65, v18, v19\n"
      "v_cvt_pk_bf16_f32 v66, v20, v21\n"
      "v_cvt_pk_bf16_f32 v67, v22, v23\n"
      "buffer_store_dwordx4 v[64:67], v4, %[srd], 0 offen sc1\n"
      "v_mul_f32 v24, v24, v6\n"
      "v_mul_f32 v25, v25, v6\n"
      "v_mul_f32 v26, v26, v6\n"
      "v_mul_f32 v27, v27, v6\n"
      "v_mul_f32 v28, v28, v6\n"
      "v_mul_f32 v29, v29, v6\n"
      "v_mul_f32 v30, v30, v6\n"
      "v_mul_f32 v31, v31, v6\n"
      "v_cvt_pk_bf16_f32 v68, v24, v25\n"
      "v_cvt_pk_bf16_f32 v69, v26, v27\n"
      "v_cvt_pk_bf16_f32 v70, v28, v29\n"
      "v_cvt_pk_bf16_f32 v71, v30, v31\n"
      "v_add_u32 v4, 65536, v4\n"
      "buffer_store_dwordx4 v[68:71], v4, %[srd], 0 offen sc1\n"
      "v_mul_f32 v32, v32, v7\n"
      "v_mul_f32 v33, v33, v7\n"
      "v_mul_f32 v34, v34, v7\n"
      "v_mul_f32 v35, v35, v7\n"
      "v_mul_f32 v36, v36, v7\n"
      "v_mul_f32 v37, v37, v7\n"
      "v_mul_f32 v38, v38, v7\n"
      "v_mul_f32 v39, v39, v7\n"
      "v_cvt_pk_bf16_f32 v64, v32, v33\n"
      "v_cvt_pk_bf16_f32 v65, v34, v35\n"
      "v_cvt_pk_bf16_f32 v66, v36, v37\n"
      "v_cvt_pk_bf16_f32 v67, v38, v39\n"
      "v_add_u32 v5, 131072, v5\n"
      "buffer_store_dwordx4 v[64:67], v5, %[srd], 0 offen sc1\n"
      "v_mul_f32 v40, v40, v7\n"
      "v_mul_f32 v41, v41, v7\n"
      "v_mul_f32 v42, v42, v7\n"
      "v_mul_f32 v43, v43, v7\n"
      "v_mul_f32 v44, v44, v7\n"
      "v_mul_f32 v45, v45, v7\n"
      "v_mul_f32 v46, v46, v7\n"
      "v_mul_f32 v47, v47, v7\n"
      "v_cvt_pk_bf16_f32 v68, v40, v41\n"
      "v_cvt_pk_bf16_f32 v69, v42, v43\n"
      "v_cvt_pk_bf16_f32 v70, v44, v45\n"
      "v_cvt_pk_bf16_f32 v71, v46, v47\n"
      "v_add_u32 v5, 65536, v5\n"
      "buffer_store_dwordx4 v[68:71], v5, %[srd], 0 offen sc1\n"
      "v_mov_b32 v112, -1\n"
      "v_mov_b32 v113, -1\n"
      "v_mov_b32 v114, -1\n"
      "v_mov_b32 v115, -1\n"
      "buffer_store_dwordx4 v[112:115], %[vsn], %[srd], 0 offen sc1\n"
      "v_bfe_u32 v6, v104, 8, 1\n"
      "v_mul_u32_u24 v6, 12288, v6\n"
      "v_add_u32 v6, 69632, v6\n"
      "v_add_u32 v12, v12, v6\n"
      "v_mov_b32 v8, v88\n"
      "v_mov_b32 v28, v92\n"
      "v_add_u32 v13, v13, v6\n"
      "v_mov_b32 v9, v89\n"
      "v_mov_b32 v29, v92\n"
      "v_add_u32 v14, v14, v6\n"
      "v_mov_b32 v10, v90\n"
      "v_mov_b32 v30, v92\n"
      "v_add_u32 v15, v15, v6\n"
      "v_mov_b32 v11, v91\n"
      "v_mov_b32 v31, v92\n"
      "s_branch 49f\n"
      "41:\n"
      "v_mov_b32 v2, 0\n"
      "v_mov_b32 v3, 0\n"
      "s_mov_b32 s71, 0xffff0000\n"
      "v_mov_b32 v116, 0\n"
      "buffer_load_dwordx2 v[116:117], v116, %[srd], 0 offen sc1\n"
      "s_movk_i32 s73, 2560\n"
      "v_mov_b32 v104, 163776\n"
      "ds_write_b64 v104, v[2:3]\n"
      "v_add_u32 v72, 57344, v8\n"
      "v_add_u32 v73, 57344, v9\n"
      "v_add_u32 v74, 57344, v10\n"
      "v_add_u32 v75, 57344, v11\n"
      "v_add_u32 v76, 57344, v12\n"
      "v_add_u32 v77, 57344, v13\n"
      "v_add_u32 v78, 57344, v14\n"
      "v_add_u32 v79, 57344, v15\n"
      "v_add_u32 v80, 114688, v8\n"
      "v_add_u32 v81, 114688, v9\n"
      "v_add_u32 v82, 114688, v10\n"
      "v_add_u32 v83, 114688, v11\n"
      "v_add_u32 v84, 114688, v12\n"
      "v_add_u32 v85, 114688, v13\n"
      "v_add_u32 v86, 114688, v14\n"
      "v_add_u32 v87, 114688, v15\n"
      "v_mbcnt_lo_u32_b32 v91, -1, 0\n"
      "v_mbcnt_hi_u32_b32 v91, -1, v91\n"
      "v_add3_u32 v91, v91, %[n0], 0\n"
      "v_mul_u32_u24 v88, 0xaaab, v91\n"
      "v_lshrrev_b32 v88, 19, v88\n"
      "v_lshlrev_b32 v91, 4, v91\n"
      "v_mad_u32_u24 v88, v88, s73, v91\n"
      "v_mbcnt_lo_u32_b32 v91, -1, 0\n"
      "v_mbcnt_hi_u32_b32 v91, -1, v91\n"
      "v_add3_u32 v91, v91, %[n0], 64\n"
      "v_mul_u32_u24 v89, 0xaaab, v91\n"
      "v_lshrrev_b32 v89, 19, v89\n"
      "v_lshlrev_b32 v91, 4, v91\n"
      "v_mad_u32_u24 v89, v89, s73, v91\n"
      "v_mbcnt_lo_u32_b32 v91, -1, 0\n"
      "v_mbcnt_hi_u32_b32 v91, -1, v91\n"
      "v_add3_u32 v91, v91, %[n0], 64\n"
      "v_add_u32 v91, 64, v91\n"
      "v_mul_u32_u24 v90, 0xaaab, v91\n"
      "v_lshrrev_b32 v90, 19, v90\n"
      "v_lshlrev_b32 v91, 4, v91\n"
      "v_mad_u32_u24 v90, v90, s73, v91\n"
      "v_mbcnt_lo_u32_b32 v104, -1, 0\n"
      "v_mbcnt_hi_u32_b32 v104, -1, v104\n"
      "v_lshlrev_b32 v104, 4, v104\n"
      "v_and_b32 v105, 0x1f0, v104\n"
      "v_lshrrev_b32 v106, 2, v105\n"
      "v_add_u32 v105, 143360, v105\n"
      "v_lshl_add_u32 v106, %[n0], 1, v106\n"
      "v_add_u32 v106, 144896, v106\n"
      "v_and_b32 v107, 0xf0, v104\n"
      "v_lshrrev_b32 v108, 9, v104\n"
      "v_lshl_or_b32 v107, v108, 8, v107\n"
      "v_bfe_u32 v108, v104, 8, 1\n"
      "v_lshl_or_b32 v107, v108, 16, v107\n"
      "v_lshrrev_b32 v108, 5, v104\n"
      "v_and_b32 v108, 16, v108\n"
      "s_add_u32 s73, %[ji], 16\n"
      "s_mul_i32 s73, s73, 86\n"
      "s_lshr_b32 s73, s73, 8\n"
      "v_mov_b32 v109, s73\n"
      "v_mov_b32 v91, s73\n"
      "s_add_u32 s73, %[ji], 32\n"
      "s_mul_i32 s73, s73, 86\n"
      "s_lshr_b32 s73, s73, 8\n"
      "v_mov_b32 v110, s73\n"
      "v_cmp_lt_u32 vcc, 63, v104\n"
      "s_mul_i32 s73, %[ji], 86\n"
      "s_lshr_b32 s73, s73, 8\n"
      "v_cndmask_b32 v109, v109, v110, vcc\n"
      "v_mov_b32 v110, s73\n"
      "v_cmp_lt_u32 vcc, 255, v104\n"
      "s_lshl_b32 s73, %[ti], 6\n"
      "v_add_u32 v109, s73, v109\n"
      "v_cndmask_b32 v91, v91, v110, vcc\n"
      "v_and_b32 v110, 0x30, v104\n"
      "s_lshl_b32 s73, %[gi], 4\n"
      "v_add_u32 v109, v109, v110\n"
      "v_cmp_lt_u32 vcc, 127, v104\n"
      "v_add_u32 v91, s73, v91\n"
      "s_nop 0\n"
      "v_cndmask_b32 v91, v109, v91, vcc\n"
      "v_lshlrev_b32 v91, 7, v91\n"
      "v_add_u32 v91, 4096, v91\n"
      "s_waitcnt vmcnt(4) lgkmcnt(0)\n"
      "s_barrier\n"
      "ds_read_b128 v[128:131], v8 offset:0\n"
      "ds_read_b64_tr_b16 v[48:49], v12 offset:0\n"
      "ds_read_b64_tr_b16 v[50:51], v12 offset:768\n"
      "ds_read_b64_tr_b16 v[52:53], v12 offset:64\n"
      "ds_read_b64_tr_b16 v[54:55], v12 offset:832\n"
      "ds_read_b128 v[132:135], v9 offset:0\n"
      "ds_read_b64_tr_b16 v[56:57], v13 offset:0\n"
      "ds_read_b64_tr_b16 v[58:59], v13 offset:768\n"
      "ds_read_b64_tr_b16 v[60:61], v13 offset:64\n"
      "ds_read_b64_tr_b16 v[62:63], v13 offset:832\n"
      "s_waitcnt lgkmcnt(5)\n"
      "s_add_u32 s72, %[rot], 2\n"
      "s_and_b32 s72, s72, 7\n"
      "s_lshl_b32 s68, s72, 7\n"
      "s_mul_i32 s69, s72, 196608\n"
      "v_mfma_f32_32x32x16_bf16 v[16:31], v[128:131], v[48:51], 0\n"
      "ds_read_b128 v[136:139], v10 offset:0\n"
      "ds_read_b64_tr_b16 v[64:65], v14 offset:0\n"
      "ds_read_b64_tr_b16 v[66:67], v14 offset:768\n"
      "v_lshlrev_b32 v4, 16, v128\n"
      "v_and_b32 v5, s71, v128\n"
      "v_fmac_f32 v2, v4, v4\n"
      "v_fmac_f32 v3, v5, v5\n"
      "v_lshlrev_b32 v6, 16, v129\n"
      "v_and_b32 v7, s71, v129\n"
      "v_fmac_f32 v2, v6, v6\n"
      "v_fmac_f32 v3, v7, v7\n"
      "v_mfma_f32_16x16x32_bf16 v[120:123], v[92:95], v[48:51], 0\n"
      "s_add_u32 m0, %[sx], 57344\n"
      "v_mfma_f32_32x32x16_bf16 v[32:47], v[128:131], v[52:55], 0\n"
      "buffer_load_dwordx4 %[vx0], %[srdx], s68 offen lds\n"
      "ds_read_b64_tr_b16 v[68:69], v14 offset:64\n"
      "ds_read_b64_tr_b16 v[70:71], v14 offset:832\n"
      "s_add_u32 m0, %[sx], 58368\n"
      "s_nop 0\n"
      "buffer_load_dwordx4 %[vx1], %[srdx], s68 offen lds\n"
      "s_add_u32 m0, %[sq], 57344\n"
      "s_nop 0\n"
      "buffer_load_dwordx4 %[vq0], %[srdq], s69 offen lds\n"
      "v_lshlrev_b32 v4, 16, v130\n"
      "v_and_b32 v5, s71, v130\n"
      "v_fmac_f32 v2, v4, v4\n"
      "v_fmac_f32 v3, v5, v5\n"
      "v_lshlrev_b32 v6, 16, v131\n"
      "v_and_b32 v7, s71, v131\n"
      "v_fmac_f32 v2, v6, v6\n"
      "v_fmac_f32 v3, v7, v7\n"
      "v_mfma_f32_16x16x32_bf16 v[124:127], v[92:95], v[52:55], 0\n"
      "s_waitcnt lgkmcnt(5)\n"
      "s_add_u32 s72, %[rot], 3\n"
      "s_and_b32 s72, s72, 7\n"
      "s_lshl_b32 s68, s72, 7\n"
      "s_mul_i32 s69, s72, 196608\n"
      "v_mfma_f32_32x32x16_bf16 v[16:31], v[132:135], v[56:59], v[16:31]\n"
      "ds_read_b128 v[140:143], v11 offset:0\n"
      "ds_read_b64_tr_b16 v[48:49], v15 offset:0\n"
      "ds_read_b64_tr_b16 v[50:51], v15 offset:768\n"
      "s_add_u32 m0, %[sx], 86016\n"
      "v_mfma_f32_32x32x16_bf16 v[32:47], v[132:135], v[60:63], v[32:47]\n"
      "buffer_load_dwordx4 %[vx0], %[srdx], s68 offen lds\n"
      "ds_read_b64_tr_b16 v[52:53], v15 offset:64\n"
      "ds_read_b64_tr_b16 v[54:55], v15 offset:832\n"
      "s_add_u32 m0, %[sx], 87040\n"
      "s_nop 0\n"
      "buffer_load_dwordx4 %[vx1], %[srdx], s68 offen lds\n"
      "s_add_u32 m0, %[sq], 86016\n"
      "s_nop 0\n"
      "buffer_load_dwordx4 %[vq0], %[srdq], s69 offen lds\n"
      "s_waitcnt vmcnt(7)\n"
      "s_barrier\n"
      "s_waitcnt lgkmcnt(5)\n"
      "s_add_u32 s72, %[rot], 4\n"
      "s_and_b32 s72, s72, 7\n"
      "s_lshl_b32 s68, s72, 7\n"
      "v_mfma_f32_32x32x16_bf16 v[16:31], v[136:139], v[64:67], v[16:31]\n"
      "ds_read_b128 v[144:147], v8 offset:28672\n"
      "ds_read_b64_tr_b16 v[56:57], v12 offset:28672\n"
      "ds_read_b64_tr_b16 v[58:59], v12 offset:29440\n"
      "v_lshlrev_b32 v4, 16, v136\n"
      "v_and_b32 v5, s71, v136\n"
      "v_fmac_f32 v2, v4, v4\n"
      "v_fmac_f32 v3, v5, v5\n"
      "v_lshlrev_b32 v6, 16, v137\n"
      "v_and_b32 v7, s71, v137\n"
      "v_fmac_f32 v2, v6, v6\n"
      "v_fmac_f32 v3, v7, v7\n"
      "s_mul_i32 s69, s72, 196608\n"
      "s_add_u32 m0, %[sx], 114688\n"
      "v_mfma_f32_32x32x16_bf16 v[32:47], v[136:139], v[68:71], v[32:47]\n"
      "buffer_load_dwordx4 %[vx0], %[srdx], s68 offen lds\n"
      "ds_read_b64_tr_b16 v[60:61], v12 offset:28736\n"
      "ds_read_b64_tr_b16 v[62:63], v12 offset:29504\n"
      "v_lshlrev_b32 v4, 16, v138\n"
      "v_and_b32 v5, s71, v138\n"
      "v_fmac_f32 v2, v4, v4\n"
      "v_fmac_f32 v3, v5, v5\n"
      "v_lshlrev_b32 v6, 16, v139\n"
      "v_and_b32 v7, s71, v139\n"
      "v_fmac_f32 v2, v6, v6\n"
      "v_fmac_f32 v3, v7, v7\n"
      "s_waitcnt lgkmcnt(5)\n"
      "s_add_u32 m0, %[sx], 115712\n"
      "v_mfma_f32_32x32x16_bf16 v[16:31], v[140:143], v[48:51], v[16:31]\n"
      "buffer_load_dwordx4 %[vx1], %[srdx], s68 offen lds\n"
      "ds_read_b128 v[148:151], v9 offset:28672\n"
      "ds_read_b64_tr_b16 v[64:65], v13 offset:28672\n"
      "ds_read_b64_tr_b16 v[66:67], v13 offset:29440\n"
      "v_mfma_f32_32x32x16_bf16 v[32:47], v[140:143], v[52:55], v[32:47]\n"
      "ds_read_b64_tr_b16 v[68:69], v13 offset:28736\n"
      "ds_read_b64_tr_b16 v[70:71], v13 offset:29504\n"
      "s_waitcnt lgkmcnt(5)\n"
      "s_add_u32 m0, %[sq], 114688\n"
      "v_mfma_f32_32x32x16_bf16 v[16:31], v[144:147], v[56:59], v[16:31]\n"
      "buffer_load_dwordx4 %[vq0], %[srdq], s69 offen lds\n"
      "ds_read_b128 v[152:155], v10 offset:28672\n"
      "ds_read_b64_tr_b16 v[48:49], v14 offset:28672\n"
      "ds_read_b64_tr_b16 v[50:51], v14 offset:29440\n"
      "v_lshlrev_b32 v4, 16, v144\n"
      "v_and_b32 v5, s71, v144\n"
      "v_fmac_f32 v2, v4, v4\n"
      "v_fmac_f32 v3, v5, v5\n"
      "v_lshlrev_b32 v6, 16, v145\n"
      "v_and_b32 v7, s71, v145\n"
      "v_fmac_f32 v2, v6, v6\n"
      "v_fmac_f32 v3, v7, v7\n"
      "v_mfma_f32_16x16x32_bf16 v[120:123], v[92:95], v[56:59], v[120:123]\n"
      "v_mfma_f32_32x32x16_bf16 v[32:47], v[144:147], v[60:63], v[32:47]\n"
      "ds_read_b64_tr_b16 v[52:53], v14 offset:28736\n"
      "ds_read_b64_tr_b16 v[54:55], v14 offset:29504\n"
      "v_lshlrev_b32 v4, 16, v146\n"
      "v_and_b32 v5, s71, v146\n"
      "v_fmac_f32 v2, v4, v4\n"
      "v_fmac_f32 v3, v5, v5\n"
      "v_lshlrev_b32 v6, 16, v147\n"
      "v_and_b32 v7, s71, v147\n"
      "v_fmac_f32 v2, v6, v6\n"
      "v_fmac_f32 v3, v7, v7\n"
      "v_mfma_f32_16x16x32_bf16 v[124:127], v[92:95], v[60:63], v[124:127]\n"
      "s_waitcnt lgkmcnt(5)\n"
      "v_mfma_f32_32x32x16_bf16 v[16:31], v[148:151], v[64:67], v[16:31]\n"
      "ds_read_b128 v[156:159], v11 offset:28672\n"
      "ds_read_b64_tr_b16 v[56:57], v15 offset:28672\n"
      "ds_read_b64_tr_b16 v[58:59], v15 offset:29440\n"
      "v_mfma_f32_32x32x16_bf16 v[32:47], v[148:151], v[68:71], v[32:47]\n"
      "ds_read_b64_tr_b16 v[60:61], v15 offset:28736\n"
      "ds_read_b64_tr_b16 v[62:63], v15 offset:29504\n"
      "s_waitcnt vmcnt(6)\n"
      "s_barrier\n"
      "s_waitcnt lgkmcnt(5)\n"
      "s_add_u32 s72, %[rot], 5\n"
      "s_and_b32 s72, s72, 7\n"
      "s_lshl_b32 s68, s72, 7\n"
      "v_mfma_f32_32x32x16_bf16 v[16:31], v[152:155], v[48:51], v[16:31]\n"
      "ds_read_b128 v[160:163], v72 offset:0\n"
      "ds_read_b64_tr_b16 v[64:65], v76 offset:0\n"
      "ds_read_b64_tr_b16 v[66:67], v76 offset:768\n"
      "v_lshlrev_b32 v4, 16, v152\n"
      "v_and_b32 v5, s71, v152\n"
      "v_fmac_f32 v2, v4, v4\n"
      "v_fmac_f32 v3, v5, v5\n"
      "v_lshlrev_b32 v6, 16, v153\n"
      "v_and_b32 v7, s71, v153\n"
      "v_fmac_f32 v2, v6, v6\n"
      "v_fmac_f32 v3, v7, v7\n"
      "s_mul_i32 s69, s72, 196608\n"
      "s_add_u32 m0, %[sx], 0\n"
      "v_mfma_f32_32x32x16_bf16 v[32:47], v[152:155], v[52:55], v[32:47]\n"
      "buffer_load_dwordx4 %[vx0], %[srdx], s68 offen lds\n"
      "ds_read_b64_tr_b16 v[68:69], v76 offset:64\n"
      "ds_read_b64_tr_b16 v[70:71], v76 offset:832\n"
      "v_lshlrev_b32 v4, 16, v154\n"
      "v_and_b32 v5, s71, v154\n"
      "v_fmac_f32 v2, v4, v4\n"
      "v_fmac_f32 v3, v5, v5\n"
      "v_lshlrev_b32 v6, 16, v155\n"
      "v_and_b32 v7, s71, v155\n"
      "v_fmac_f32 v2, v6, v6\n"
      "v_fmac_f32 v3, v7, v7\n"
      "s_waitcnt lgkmcnt(5)\n"
      "s_add_u32 m0, %[sx], 1024\n"
      "v_mfma_f32_32x32x16_bf16 v[16:31], v[156:159], v[56:59], v[16:31]\n"
      "buffer_load_dwordx4 %[vx1], %[srdx], s68 offen lds\n"
      "ds_read_b128 v[164:167], v73 offset:0\n"
      "ds_read_b64_tr_b16 v[48:49], v77 offset:0\n"
      "ds_read_b64_tr_b16 v[50:51], v77 offset:768\n"
      "v_mfma_f32_32x32x16_bf16 v[32:47], v[156:159], v[60:63], v[32:47]\n"
      "ds_read_b64_tr_b16 v[52:53], v77 offset:64\n"
      "ds_read_b64_tr_b16 v[54:55], v77 offset:832\n"
      "s_waitcnt lgkmcnt(5)\n"
      "s_add_u32 m0, %[sq], 0\n"
      "v_mfma_f32_32x32x16_bf16 v[16:31], v[160:163], v[64:67], v[16:31]\n"
      "buffer_load_dwordx4 %[vq0], %[srdq], s69 offen lds\n"
      "ds_read_b128 v[168:171], v74 offset:0\n"
      "ds_read_b64_tr_b16 v[56:57], v78 offset:0\n"
      "ds_read_b64_tr_b16 v[58:59], v78 offset:768\n"
      "v_lshlrev_b32 v4, 16, v160\n"
      "v_and_b32 v5, s71, v160\n"
      "v_fmac_f32 v2, v4, v4\n"
      "v_fmac_f32 v3, v5, v5\n"
      "v_lshlrev_b32 v6, 16, v161\n"
      "v_and_b32 v7, s71, v161\n"
      "v_fmac_f32 v2, v6, v6\n"
      "v_fmac_f32 v3, v7, v7\n"
      "v_mfma_f32_16x16x32_bf16 v[120:123], v[92:95], v[64:67], v[120:123]\n"
      "v_mfma_f32_32x32x16_bf16 v[32:47], v[160:163], v[68:71], v[32:47]\n"
      "ds_read_b64_tr_b16 v[60:61], v78 offset:64\n"
      "ds_read_b64_tr_b16 v[62:63], v78 offset:832\n"
      "v_lshlrev_b32 v4, 16, v162\n"
      "v_and_b32 v5, s71, v162\n"
      "v_fmac_f32 v2, v4, v4\n"
      "v_fmac_f32 v3, v5, v5\n"
      "v_lshlrev_b32 v6, 16, v163\n"
      "v_and_b32 v7, s71, v163\n"
      "v_fmac_f32 v2, v6, v6\n"
      "v_fmac_f32 v3, v7, v7\n"
      "v_mfma_f32_16x16x32_bf16 v[124:127], v[92:95], v[68:71], v[124:127]\n"
      "s_waitcnt lgkmcnt(5)\n"
      "v_mfma_f32_32x32x16_bf16 v[16:31], v[164:167], v[48:51], v[16:31]\n"
      "ds_read_b128 v[172:175], v75 offset:0\n"
      "ds_read_b64_tr_b16 v[64:65], v79 offset:0\n"
      "ds_read_b64_tr_b16 v[66:67], v79 offset:768\n"
      "v_mfma_f32_32x32x16_bf16 v[32:47], v[164:167], v[52:55], v[32:47]\n"
      "ds_read_b64_tr_b16 v[68:69], v79 offset:64\n"
      "ds_read_b64_tr_b16 v[70:71], v79 offset:832\n"
      "s_waitcnt vmcnt(6)\n"
      "s_barrier\n"
      "s_waitcnt lgkmcnt(5)\n"
      "s_add_u32 s72, %[rot], 6\n"
      "s_and_b32 s72, s72, 7\n"
      "s_lshl_b32 s68, s72, 7\n"
      "v_mfma_f32_32x32x16_bf16 v[16:31], v[168:171], v[56:59], v[16:31]\n"
      "ds_read_b128 v[176:179], v72 offset:28672\n"
      "ds_read_b64_tr_b16 v[48:49], v76 offset:28672\n"
      "ds_read_b64_tr_b16 v[50:51], v76 offset:29440\n"
      "v_lshlrev_b32 v4, 16, v168\n"
      "v_and_b32 v5, s71, v168\n"
      "v_fmac_f32 v2, v4, v4\n"
      "v_fmac_f32 v3, v5, v5\n"
      "v_lshlrev_b32 v6, 16, v169\n"
      "v_and_b32 v7, s71, v169\n"
      "v_fmac_f32 v2, v6, v6\n"
      "v_fmac_f32 v3, v7, v7\n"
      "s_mul_i32 s69, s72, 196608\n"
      "s_add_u32 m0, %[sx], 28672\n"
      "v_mfma_f32_32x32x16_bf16 v[32:47], v[168:171], v[60:63], v[32:47]\n"
      "buffer_load_dwordx4 %[vx0], %[srdx], s68 offen lds\n"
      "ds_read_b64_tr_b16 v[52:53], v76 offset:28736\n"
      "ds_read_b64_tr_b16 v[54:55], v76 offset:29504\n"
      "v_lshlrev_b32 v4, 16, v170\n"
      "v_and_b32 v5, s71, v170\n"
      "v_fmac_f32 v2, v4, v4\n"
      "v_fmac_f32 v3, v5, v5\n"
      "v_lshlrev_b32 v6, 16, v171\n"
      "v_and_b32 v7, s71, v171\n"
      "v_fmac_f32 v2, v6, v6\n"
      "v_fmac_f32 v3, v7, v7\n"
      "s_waitcnt lgkmcnt(5)\n"
      "s_add_u32 m0, %[sx], 29696\n"
      "v_mfma_f32_32x32x16_bf16 v[16:31], v[172:175], v[64:67], v[16:31]\n"
      "buffer_load_dwordx4 %[vx1], %[srdx], s68 offen lds\n"
      "ds_read_b128 v[180:183], v73 offset:28672\n"
      "ds_read_b64_tr_b16 v[56:57], v77 offset:28672\n"
      "ds_read_b64_tr_b16 v[58:59], v77 offset:29440\n"
      "v_mfma_f32_32x32x16_bf16 v[32:47], v[172:175], v[68:71], v[32:47]\n"
      "ds_read_b64_tr_b16 v[60:61], v77 offset:28736\n"
      "ds_read_b64_tr_b16 v[62:63], v77 offset:29504\n"
      "s_waitcnt lgkmcnt(5)\n"
      "s_add_u32 m0, %[sq], 28672\n"
      "v_mfma_f32_32x32x16_bf16 v[16:31], v[176:179], v[48:51], v[16:31]\n"
      "buffer_load_dwordx4 %[vq0], %[srdq], s69 offen lds\n"
      "ds_read_b128 v[184:187], v74 offset:28672\n"
      "ds_read_b64_tr_b16 v[64:65], v78 offset:28672\n"
      "ds_read_b64_tr_b16 v[66:67], v78 offset:29440\n"
      "v_lshlrev_b32 v4, 16, v176\n"
      "v_and_b32 v5, s71, v176\n"
      "v_fmac_f32 v2, v4, v4\n"
      "v_fmac_f32 v3, v5, v5\n"
      "v_lshlrev_b32 v6, 16, v177\n"
      "v_and_b32 v7, s71, v177\n"
      "v_fmac_f32 v2, v6, v6\n"
      "v_fmac_f32 v3, v7, v7\n"
      "v_mfma_f32_16x16x32_bf16 v[120:123], v[92:95], v[48:51], v[120:123]\n"
      "v_mfma_f32_32x32x16_bf16 v[32:47], v[176:179], v[52:55], v[32:47]\n"
      "ds_read_b64_tr_b16 v[68:69], v78 offset:28736\n"
      "ds_read_b64_tr_b16 v[70:71], v78 offset:29504\n"
      "v_lshlrev_b32 v4, 16, v178\n"
      "v_and_b32 v5, s71, v178\n"
      "v_fmac_f32 v2, v4, v4\n"
      "v_fmac_f32 v3, v5, v5\n"
      "v_lshlrev_b32 v6, 16, v179\n"
      "v_and_b32 v7, s71, v179\n"
      "v_fmac_f32 v2, v6, v6\n"
      "v_fmac_f32 v3, v7, v7\n"
      "v_mfma_f32_16x16x32_bf16 v[124:127], v[92:95], v[52:55], v[124:127]\n"
      "s_waitcnt lgkmcnt(5)\n"
      "v_mfma_f32_32x32x16_bf16 v[16:31], v[180:183], v[56:59], v[16:31]\n"
      "ds_read_b128 v[188:191], v75 offset:28672\n"
      "ds_read_b64_tr_b16 v[48:49], v79 offset:28672\n"
      "ds_read_b64_tr_b16 v[50:51], v79 offset:29440\n"
      "v_mfma_f32_32x32x16_bf16 v[32:47], v[180:183], v[60:63], v[32:47]\n"
      "ds_read_b64_tr_b16 v[52:53], v79 offset:28736\n"
      "ds_read_b64_tr_b16 v[54:55], v79 offset:29504\n"
      "s_waitcnt vmcnt(6)\n"
      "s_barrier\n"
      "s_waitcnt lgkmcnt(5)\n"
      "s_add_u32 s72, %[rot], 7\n"
      "s_and_b32 s72, s72, 7\n"
      "s_lshl_b32 s68, s72, 7\n"
      "v_mfma_f32_32x32x16_bf16 v[16:31], v[184:187], v[64:67], v[16:31]\n"
      "ds_read_b128 v[192:195], v80 offset:0\n"
      "ds_read_b64_tr_b16 v[56:57], v84 offset:0\n"
      "ds_read_b64_tr_b16 v[58:59], v84 offset:768\n"
      "v_lshlrev_b32 v4, 16, v184\n"
      "v_and_b32 v5, s71, v184\n"
      "v_fmac_f32 v2, v4, v4\n"
      "v_fmac_f32 v3, v5, v5\n"
      "v_lshlrev_b32 v6, 16, v185\n"
      "v_and_b32 v7, s71, v185\n"
      "v_fmac_f32 v2, v6, v6\n"
      "v_fmac_f32 v3, v7, v7\n"
      "s_mul_i32 s69, s72, 196608\n"
      "s_add_u32 m0, %[sx], 57344\n"
      "v_mfma_f32_32x32x16_bf16 v[32:47], v[184:187], v[68:71], v[32:47]\n"
      "buffer_load_dwordx4 %[vx0], %[srdx], s68 offen lds\n"
      "ds_read_b64_tr_b16 v[60:61], v84 offset:64\n"
      "ds_read_b64_tr_b16 v[62:63], v84 offset:832\n"
      "v_lshlrev_b32 v4, 16, v186\n"
      "v_and_b32 v5, s71, v186\n"
      "v_fmac_f32 v2, v4, v4\n"
      "v_fmac_f32 v3, v5, v5\n"
      "v_lshlrev_b32 v6, 16, v187\n"
      "v_and_b32 v7, s71, v187\n"
      "v_fmac_f32 v2, v6, v6\n"
      "v_fmac_f32 v3, v7, v7\n"
      "s_waitcnt lgkmcnt(5)\n"
      "s_add_u32 m0, %[sx], 58368\n"
      "v_mfma_f32_32x32x16_bf16 v[16:31], v[188:191], v[48:51], v[16:31]\n"
      "buffer_load_dwordx4 %[vx1], %[srdx], s68 offen lds\n"
      "ds_read_b128 v[196:199], v81 offset:0\n"
      "ds_read_b64_tr_b16 v[64:65], v85 offset:0\n"
      "ds_read_b64_tr_b16 v[66:67], v85 offset:768\n"
      "v_mfma_f32_32x32x16_bf16 v[32:47], v[188:191], v[52:55], v[32:47]\n"
      "ds_read_b64_tr_b16 v[68:69], v85 offset:64\n"
      "ds_read_b64_tr_b16 v[70:71], v85 offset:832\n"
      "s_waitcnt lgkmcnt(5)\n"
      "s_add_u32 m0, %[sq], 57344\n"
      "v_mfma_f32_32x32x16_bf16 v[16:31], v[192:195], v[56:59], v[16:31]\n"
      "buffer_load_dwordx4 %[vq0], %[srdq], s69 offen lds\n"
      "ds_read_b128 v[200:203], v82 offset:0\n"
      "ds_read_b64_tr_b16 v[48:49], v86 offset:0\n"
      "ds_read_b64_tr_b16 v[50:51], v86 offset:768\n"
      "v_lshlrev_b32 v4, 16, v192\n"
      "v_and_b32 v5, s71, v192\n"
      "v_fmac_f32 v2, v4, v4\n"
      "v_fmac_f32 v3, v5, v5\n"
      "v_lshlrev_b32 v6, 16, v193\n"
      "v_and_b32 v7, s71, v193\n"
      "v_fmac_f32 v2, v6, v6\n"
      "v_fmac_f32 v3, v7, v7\n"
      "v_mfma_f32_16x16x32_bf16 v[120:123], v[92:95], v[56:59], v[120:123]\n"
      "v_mfma_f32_32x32x16_bf16 v[32:47], v[192:195], v[60:63], v[32:47]\n"
      "ds_read_b64_tr_b16 v[52:53], v86 offset:64\n"
      "ds_read_b64_tr_b16 v[54:55], v86 offset:832\n"
      "v_lshlrev_b32 v4, 16, v194\n"
      "v_and_b32 v5, s71, v194\n"
      "v_fmac_f32 v2, v4, v4\n"
      "v_fmac_f32 v3, v5, v5\n"
      "v_lshlrev_b32 v6, 16, v195\n"
      "v_and_b32 v7, s71, v195\n"
      "v_fmac_f32 v2, v6, v6\n"
      "v_fmac_f32 v3, v7, v7\n"
      "v_mfma_f32_16x16x32_bf16 v[124:127], v[92:95], v[60:63], v[124:127]\n"
      "s_waitcnt lgkmcnt(5)\n"
      "v_mfma_f32_32x32x16_bf16 v[16:31], v[196:199], v[64:67], v[16:31]\n"
      "ds_read_b128 v[204:207], v83 offset:0\n"
      "ds_read_b64_tr_b16 v[56:57], v87 offset:0\n"
      "ds_read_b64_tr_b16 v[58:59], v87 offset:768\n"
      "v_mfma_f32_32x32x16_bf16 v[32:47], v[196:199], v[68:71], v[32:47]\n"
      "ds_read_b64_tr_b16 v[60:61], v87 offset:64\n"
      "ds_read_b64_tr_b16 v[62:63], v87 offset:832\n"
      "s_waitcnt vmcnt(6)\n"
      "s_barrier\n"
      "s_waitcnt lgkmcnt(5)\n"
      "s_add_u32 s72, %[rot], 0\n"
      "s_and_b32 s72, s72, 7\n"
      "v_mfma_f32_32x32x16_bf16 v[16:31], v[200:203], v[48:51], v[16:31]\n"
      "ds_read_b128 v[208:211], v8 offset:0\n"
      "ds_read_b64_tr_b16 v[64:65], v12 offset:0\n"
      "ds_read_b64_tr_b16 v[66:67], v12 offset:768\n"
      "v_lshlrev_b32 v4, 16, v200\n"
      "v_and_b32 v5, s71, v200\n"
      "v_fmac_f32 v2, v4, v4\n"
      "v_fmac_f32 v3, v5, v5\n"
      "v_lshlrev_b32 v6, 16, v201\n"
      "v_and_b32 v7, s71, v201\n"
      "v_fmac_f32 v2, v6, v6\n"
      "v_fmac_f32 v3, v7, v7\n"
      "s_mul_i32 s70, s72, 176128\n"
      "s_add_u32 m0, %[sg], 86016\n"
      "v_mfma_f32_32x32x16_bf16 v[32:47], v[200:203], v[52:55], v[32:47]\n"
      "buffer_load_dwordx4 v88, %[srdg], s70 offen lds\n"
      "ds_read_b64_tr_b16 v[68:69], v12 offset:64\n"
      "ds_read_b64_tr_b16 v[70:71], v12 offset:832\n"
      "v_lshlrev_b32 v4, 16, v202\n"
      "v_and_b32 v5, s71, v202\n"
      "v_fmac_f32 v2, v4, v4\n"
      "v_fmac_f32 v3, v5, v5\n"
      "v_lshlrev_b32 v6, 16, v203\n"
      "v_and_b32 v7, s71, v203\n"
      "v_fmac_f32 v2, v6, v6\n"
      "v_fmac_f32 v3, v7, v7\n"
      "s_waitcnt lgkmcnt(5)\n"
      "s_add_u32 m0, %[sg], 87040\n"
      "v_mfma_f32_32x32x16_bf16 v[16:31], v[204:207], v[56:59], v[16:31]\n"
      "buffer_load_dwordx4 v89, %[srdg], s70 offen lds\n"
      "ds_read_b128 v[212:215], v9 offset:0\n"
      "ds_read_b64_tr_b16 v[48:49], v13 offset:0\n"
      "ds_read_b64_tr_b16 v[50:51], v13 offset:768\n"
      "v_mfma_f32_32x32x16_bf16 v[32:47], v[204:207], v[60:63], v[32:47]\n"
      "ds_read_b64_tr_b16 v[52:53], v13 offset:64\n"
      "ds_read_b64_tr_b16 v[54:55], v13 offset:832\n"
      "s_waitcnt lgkmcnt(5)\n"
      "s_add_u32 m0, %[sg], 88064\n"
      "v_mfma_f32_32x32x16_bf16 v[16:31], v[208:211], v[64:67], v[16:31]\n"
      "buffer_load_dwordx4 v90, %[srdg], s70 offen lds\n"
      "ds_read_b128 v[216:219], v10 offset:0\n"
      "ds_read_b64_tr_b16 v[56:57], v14 offset:0\n"
      "ds_read_b64_tr_b16 v[58:59], v14 offset:768\n"
      "v_lshlrev_b32 v4, 16, v208\n"
      "v_and_b32 v5, s71, v208\n"
      "v_fmac_f32 v2, v4, v4\n"
      "v_fmac_f32 v3, v5, v5\n"
      "v_lshlrev_b32 v6, 16, v209\n"
      "v_and_b32 v7, s71, v209\n"
      "v_fmac_f32 v2, v6, v6\n"
      "v_fmac_f32 v3, v7, v7\n"
      "v_mfma_f32_16x16x32_bf16 v[120:123], v[92:95], v[64:67], v[120:123]\n"
      "v_mfma_f32_32x32x16_bf16 v[32:47], v[208:211], v[68:71], v[32:47]\n"
      "ds_read_b64_tr_b16 v[60:61], v14 offset:64\n"
      "ds_read_b64_tr_b16 v[62:63], v14 offset:832\n"
      "v_lshlrev_b32 v4, 16, v210\n"
      "v_and_b32 v5, s71, v210\n"
      "v_fmac_f32 v2, v4, v4\n"
      "v_fmac_f32 v3, v5, v5\n"
      "v_lshlrev_b32 v6, 16, v211\n"
      "v_and_b32 v7, s71, v211\n"
      "v_fmac_f32 v2, v6, v6\n"
      "v_fmac_f32 v3, v7, v7\n"
      "v_mfma_f32_16x16x32_bf16 v[124:127], v[92:95], v[68:71], v[124:127]\n"
      "s_waitcnt lgkmcnt(5)\n"
      "v_mfma_f32_32x32x16_bf16 v[16:31], v[212:215], v[48:51], v[16:31]\n"
      "ds_read_b128 v[220:223], v11 offset:0\n"
      "ds_read_b64_tr_b16 v[64:65], v15 offset:0\n"
      "ds_read_b64_tr_b16 v[66:67], v15 offset:768\n"
      "v_mfma_f32_32x32x16_bf16 v[32:47], v[212:215], v[52:55], v[32:47]\n"
      "ds_read_b64_tr_b16 v[68:69], v15 offset:64\n"
      "ds_read_b64_tr_b16 v[70:71], v15 offset:832\n"
      "s_waitcnt vmcnt(6)\n"
      "s_barrier\n"
      "s_waitcnt lgkmcnt(5)\n"
      "s_add_u32 s72, %[rot], 1\n"
      "s_and_b32 s72, s72, 7\n"
      "v_mfma_f32_32x32x16_bf16 v[16:31], v[216:219], v[56:59], v[16:31]\n"
      "ds_read_b128 v[224:227], v8 offset:28672\n"
      "ds_read_b64_tr_b16 v[48:49], v12 offset:28672\n"
      "ds_read_b64_tr_b16 v[50:51], v12 offset:29440\n"
      "v_lshlrev_b32 v4, 16, v216\n"
      "v_and_b32 v5, s71, v216\n"
      "v_fmac_f32 v2, v4, v4\n"
      "v_fmac_f32 v3, v5, v5\n"
      "v_lshlrev_b32 v6, 16, v217\n"
      "v_and_b32 v7, s71, v217\n"
      "v_fmac_f32 v2, v6, v6\n"
      "v_fmac_f32 v3, v7, v7\n"
      "s_mul_i32 s70, s72, 176128\n"
      "s_add_u32 m0, %[sg], 114688\n"
      "v_mfma_f32_32x32x16_bf16 v[32:47], v[216:219], v[60:63], v[32:47]\n"
      "buffer_load_dwordx4 v88, %[srdg], s70 offen lds\n"
      "ds_read_b64_tr_b16 v[52:53], v12 offset:28736\n"
      "ds_read_b64_tr_b16 v[54:55], v12 offset:29504\n"
      "v_lshlrev_b32 v4, 16, v218\n"
      "v_and_b32 v5, s71, v218\n"
      "v_fmac_f32 v2, v4, v4\n"
      "v_fmac_f32 v3, v5, v5\n"
      "v_lshlrev_b32 v6, 16, v219\n"
      "v_and_b32 v7, s71, v219\n"
      "v_fmac_f32 v2, v6, v6\n"
      "v_fmac_f32 v3, v7, v7\n"
      "s_waitcnt lgkmcnt(5)\n"
      "s_add_u32 m0, %[sg], 115712\n"
      "v_mfma_f32_32x32x16_bf16 v[16:31], v[220:223], v[64:67], v[16:31]\n"
      "buffer_load_dwordx4 v89, %[srdg], s70 offen lds\n"
      "ds_read_b128 v[228:231], v9 offset:28672\n"
      "ds_read_b64_tr_b16 v[56:57], v13 offset:28672\n"
      "ds_read_b64_tr_b16 v[58:59], v13 offset:29440\n"
      "v_mfma_f32_32x32x16_bf16 v[32:47], v[220:223], v[68:71], v[32:47]\n"
      "ds_read_b64_tr_b16 v[60:61], v13 offset:28736\n"
      "ds_read_b64_tr_b16 v[62:63], v13 offset:29504\n"
      "s_waitcnt lgkmcnt(5)\n"
      "s_add_u32 m0, %[sg], 116736\n"
      "v_mfma_f32_32x32x16_bf16 v[16:31], v[224:227], v[48:51], v[16:31]\n"
      "buffer_load_dwordx4 v90, %[srdg], s70 offen lds\n"
      "ds_read_b128 v[232:235], v10 offset:28672\n"
      "ds_read_b64_tr_b16 v[64:65], v14 offset:28672\n"
      "ds_read_b64_tr_b16 v[66:67], v14 offset:29440\n"
      "v_lshlrev_b32 v4, 16, v224\n"
      "v_and_b32 v5, s71, v224\n"
      "v_fmac_f32 v2, v4, v4\n"
      "v_fmac_f32 v3, v5, v5\n"
      "v_lshlrev_b32 v6, 16, v225\n"
      "v_and_b32 v7, s71, v225\n"
      "v_fmac_f32 v2, v6, v6\n"
      "v_fmac_f32 v3, v7, v7\n"
      "v_mfma_f32_16x16x32_bf16 v[120:123], v[92:95], v[48:51], v[120:123]\n"
      "v_mfma_f32_32x32x16_bf16 v[32:47], v[224:227], v[52:55], v[32:47]\n"
      "ds_read_b64_tr_b16 v[68:69], v14 offset:28736\n"
      "ds_read_b64_tr_b16 v[70:71], v14 offset:29504\n"
      "v_lshlrev_b32 v4, 16, v226\n"
      "v_and_b32 v5, s71, v226\n"
      "v_fmac_f32 v2, v4, v4\n"
      "v_fmac_f32 v3, v5, v5\n"
      "v_lshlrev_b32 v6, 16, v227\n"
      "v_and_b32 v7, s71, v227\n"
      "v_fmac_f32 v2, v6, v6\n"
      "v_fmac_f32 v3, v7, v7\n"
      "v_mfma_f32_16x16x32_bf16 v[124:127], v[92:95], v[52:55], v[124:127]\n"
      "s_waitcnt lgkmcnt(5)\n"
      "v_mfma_f32_32x32x16_bf16 v[16:31], v[228:231], v[56:59], v[16:31]\n"
      "ds_read_b128 v[236:239], v11 offset:28672\n"
      "ds_read_b64_tr_b16 v[48:49], v15 offset:28672\n"
      "ds_read_b64_tr_b16 v[50:51], v15 offset:29440\n"
      "v_mfma_f32_32x32x16_bf16 v[32:47], v[228:231], v[60:63], v[32:47]\n"
      "ds_read_b64_tr_b16 v[52:53], v15 offset:28736\n"
      "ds_read_b64_tr_b16 v[54:55], v15 offset:29504\n"
      "s_waitcnt vmcnt(6)\n"
      "s_barrier\n"
      "s_waitcnt lgkmcnt(5)\n"
      "s_add_u32 s72, %[rot], 2\n"
      "s_and_b32 s72, s72, 7\n"
      "v_mfma_f32_32x32x16_bf16 v[16:31], v[232:235], v[64:67], v[16:31]\n"
      "ds_read_b128 v[240:243], v72 offset:0\n"
      "ds_read_b64_tr_b16 v[56:57], v76 offset:0\n"
      "ds_read_b64_tr_b16 v[58:59], v76 offset:768\n"
      "v_lshlrev_b32 v4, 16, v232\n"
      "v_and_b32 v5, s71, v232\n"
      "v_fmac_f32 v2, v4, v4\n"
      "v_fmac_f32 v3, v5, v5\n"
      "v_lshlrev_b32 v6, 16, v233\n"
      "v_and_b32 v7, s71, v233\n"
      "v_fmac_f32 v2, v6, v6\n"
      "v_fmac_f32 v3, v7, v7\n"
      "s_mul_i32 s70, s72, 176128\n"
      "s_add_u32 m0, %[sg], 0\n"
      "v_mfma_f32_32x32x16_bf16 v[32:47], v[232:235], v[68:71], v[32:47]\n"
      "buffer_load_dwordx4 v88, %[srdg], s70 offen lds\n"
      "ds_read_b64_tr_b16 v[60:61], v76 offset:64\n"
      "ds_read_b64_tr_b16 v[62:63], v76 offset:832\n"
      "v_lshlrev_b32 v4, 16, v234\n"
      "v_and_b32 v5, s71, v234\n"
      "v_fmac_f32 v2, v4, v4\n"
      "v_fmac_f32 v3, v5, v5\n"
      "v_lshlrev_b32 v6, 16, v235\n"
      "v_and_b32 v7, s71, v235\n"
      "v_fmac_f32 v2, v6, v6\n"
      "v_fmac_f32 v3, v7, v7\n"
      "s_waitcnt lgkmcnt(5)\n"
      "s_add_u32 m0, %[sg], 1024\n"
      "v_mfma_f32_32x32x16_bf16 v[16:31], v[236:239], v[48:51], v[16:31]\n"
      "buffer_load_dwordx4 v89, %[srdg], s70 offen lds\n"
      "ds_read_b128 v[244:247], v73 offset:0\n"
      "ds_read_b64_tr_b16 v[64:65], v77 offset:0\n"
      "ds_read_b64_tr_b16 v[66:67], v77 offset:768\n"
      "v_mfma_f32_32x32x16_bf16 v[32:47], v[236:239], v[52:55], v[32:47]\n"
      "ds_read_b64_tr_b16 v[68:69], v77 offset:64\n"
      "ds_read_b64_tr_b16 v[70:71], v77 offset:832\n"
      "s_waitcnt lgkmcnt(5)\n"
      "s_add_u32 m0, %[sg], 2048\n"
      "v_mfma_f32_32x32x16_bf16 v[16:31], v[240:243], v[56:59], v[16:31]\n"
      "buffer_load_dwordx4 v90, %[srdg], s70 offen lds\n"
      "ds_read_b128 v[248:251], v74 offset:0\n"
      "ds_read_b64_tr_b16 v[48:49], v78 offset:0\n"
      "ds_read_b64_tr_b16 v[50:51], v78 offset:768\n"
      "v_lshlrev_b32 v4, 16, v240\n"
      "v_and_b32 v5, s71, v240\n"
      "v_fmac_f32 v2, v4, v4\n"
      "v_fmac_f32 v3, v5, v5\n"
      "v_lshlrev_b32 v6, 16, v241\n"
      "v_and_b32 v7, s71, v241\n"
      "v_fmac_f32 v2, v6, v6\n"
      "v_fmac_f32 v3, v7, v7\n"
      "v_mfma_f32_16x16x32_bf16 v[120:123], v[92:95], v[56:59], v[120:123]\n"
      "v_mfma_f32_32x32x16_bf16 v[32:47], v[240:243], v[60:63], v[32:47]\n"
      "ds_read_b64_tr_b16 v[52:53], v78 offset:64\n"
      "ds_read_b64_tr_b16 v[54:55], v78 offset:832\n"
      "v_lshlrev_b32 v4, 16, v242\n"
      "v_and_b32 v5, s71, v242\n"
      "v_fmac_f32 v2, v4, v4\n"
      "v_fmac_f32 v3, v5, v5\n"
      "v_lshlrev_b32 v6, 16, v243\n"
      "v_and_b32 v7, s71, v243\n"
      "v_fmac_f32 v2, v6, v6\n"
      "v_fmac_f32 v3, v7, v7\n"
      "v_mfma_f32_16x16x32_bf16 v[124:127], v[92:95], v[60:63], v[124:127]\n"
      "s_waitcnt lgkmcnt(5)\n"
      "v_mfma_f32_32x32x16_bf16 v[16:31], v[244:247], v[64:67], v[16:31]\n"
      "ds_read_b128 v[252:255], v75 offset:0\n"
      "ds_read_b64_tr_b16 v[56:57], v79 offset:0\n"
      "ds_read_b64_tr_b16 v[58:59], v79 offset:768\n"
      "v_mfma_f32_32x32x16_bf16 v[32:47], v[244:247], v[68:71], v[32:47]\n"
      "ds_read_b64_tr_b16 v[60:61], v79 offset:64\n"
      "ds_read_b64_tr_b16 v[62:63], v79 offset:832\n"
      "s_barrier\n"
      "s_waitcnt lgkmcnt(5)\n"
      "s_add_u32 s72, %[rot], 3\n"
      "s_and_b32 s72, s72, 7\n"
      "v_mfma_f32_32x32x16_bf16 v[16:31], v[248:251], v[48:51], v[16:31]\n"
      "v_lshlrev_b32 v4, 16, v248\n"
      "v_and_b32 v5, s71, v248\n"
      "v_fmac_f32 v2, v4, v4\n"
      "v_fmac_f32 v3, v5, v5\n"
      "v_lshlrev_b32 v6, 16, v249\n"
      "v_and_b32 v7, s71, v249\n"
      "v_fmac_f32 v2, v6, v6\n"
      "v_fmac_f32 v3, v7, v7\n"
      "s_mul_i32 s70, s72, 176128\n"
      "s_add_u32 m0, %[sg], 28672\n"
      "v_mfma_f32_32x32x16_bf16 v[32:47], v[248:251], v[52:55], v[32:47]\n"
      "buffer_load_dwordx4 v88, %[srdg], s70 offen lds\n"
      "v_lshlrev_b32 v4, 16, v250\n"
      "v_and_b32 v5, s71, v250\n"
      "v_fmac_f32 v2, v4, v4\n"
      "v_fmac_f32 v3, v5, v5\n"
      "v_lshlrev_b32 v6, 16, v251\n"
      "v_and_b32 v7, s71, v251\n"
      "v_fmac_f32 v2, v6, v6\n"
      "v_fmac_f32 v3, v7, v7\n"
      "s_waitcnt lgkmcnt(0)\n"
      "s_add_u32 m0, %[sg], 29696\n"
      "v_mfma_f32_32x32x16_bf16 v[16:31], v[252:255], v[56:59], v[16:31]\n"
      "buffer_load_dwordx4 v89, %[srdg], s70 offen lds\n"
      "s_add_u32 m0, %[sg], 30720\n"
      "v_mfma_f32_32x32x16_bf16 v[32:47], v[252:255], v[60:63], v[32:47]\n"
      "buffer_load_dwordx4 v90, %[srdg], s70 offen lds\n"
      "v_add_f32 v2, v2, v3\n"
      "s_lshl_b32 s72, %[ti], 2\n"
      "v_mov_b32 v4, v2\n"
      "v_add_u32 v5, s72, v105\n"
      "s_mul_i32 s68, %[ji], 393216\n"
      "v_permlane32_swap_b32 v4, v2\n"
      "s_lshl_b32 s69, %[gi], 12\n"
      "s_add_u32 s68, s68, s69\n"
      "v_add_f32 v2, v2, v4\n"
      "s_lshl_b32 s69, %[ti], 10\n"
      "s_add_u32 s68, s68, s69\n"
      "s_nop 1\n"
      "ds_write_b32 v5, v120 offset:0\n"
      "ds_write_b32 v5, v124 offset:512\n"
      "ds_write_b32 v106, v2 offset:0\n"
      "v_readfirstlane_b32 %[ep0], v116\n"
      "v_readfirstlane_b32 %[ep1], v117\n"
      "s_add_u32 s68, s68, 1048576\n"
      "s_waitcnt vmcnt(6) lgkmcnt(0)\n"
      "s_barrier\n"
      "ds_read_b32 v4, v106 offset:256\n"
      "ds_read_b32 v5, v106\n"
      "ds_read_b32 v6, v106 offset:128\n"
      "ds_read_b128 v[48:51], v105 offset:0\n"
      "ds_read_b128 v[52:55], v105 offset:512\n"
      "s_add_u32 s72, %[rot], 4\n"
      "s_and_b32 s72, s72, 7\n"
      "s_mul_i32 s70, s72, 176128\n"
      "s_add_u32 m0, %[sg], 57344\n"
      "s_nop 0\n"
      "buffer_load_dwordx4 v88, %[srdg], s70 offen lds\n"
      "s_add_u32 m0, %[sg], 58368\n"
      "s_nop 0\n"
      "buffer_load_dwordx4 v89, %[srdg], s70 offen lds\n"
      "s_add_u32 m0, %[sg], 59392\n"
      "s_nop 0\n"
      "buffer_load_dwordx4 v90, %[srdg], s70 offen lds\n"
      "v_mov_b32 v122, 0\n"
      "v_mov_b32 v123, 0\n"
      "v_mov_b32 v58, 0\n"
      "v_mov_b32 v59, 0\n"
      "v_mov_b32 v62, 0\n"
      "v_mov_b32 v63, 0\n"
      "s_mov_b32 s70, 0x3b000000\n"
      "s_waitcnt lgkmcnt(2)\n"
      "v_mul_f32 v4, 0x3b000000, v4\n"
      "v_add_f32 v5, v5, v6\n"
      "v_mul_f32 v6, v4, v4\n"
      "v_mul_f32 v4, -0.5, v4\n"
      "v_fma_f32 v5, v5, s70, -v6\n"
      "v_cvt_pk_bf16_f32 v7, v4, v4\n"
      "v_max_f32 v5, 0, v5\n"
      "v_and_b32 v7, s71, v7\n"
      "v_add_f32 v5, 0x3727c5ac, v5\n"
      "v_sub_f32 v7, v4, v7\n"
      "v_rsq_f32 v124, v5\n"
      "v_cvt_pk_bf16_f32 v120, v4, v7\n"
      "s_waitcnt lgkmcnt(0)\n"
      "v_mov_b32 v121, v120\n"
      "s_lshl_b32 s72, %[wid], 7\n"
      "s_add_u32 s72, s72, 146432\n"
      "v_bfe_u32 v5, v104, 2, 7\n"
      "v_add_u32 v5, s72, v5\n"
      "v_add_u32 v108, s72, v108\n"
      "ds_write_b32 v5, v124\n"
      "ds_read_b128 v[72:75], v108 offset:0\n"
      "ds_read_b128 v[76:79], v108 offset:32\n"
      "ds_read_b128 v[80:83], v108 offset:64\n"
      "ds_read_b128 v[84:87], v108 offset:96\n"
      "v_add_f32 v48, v48, v49\n"
      "v_add_f32 v50, v50, v51\n"
      "v_add_f32 v48, v48, v50\n"
      "v_cvt_pk_bf16_f32 v56, v48, v48\n"
      "v_and_b32 v49, s71, v56\n"
      "v_sub_f32 v48, v48, v49\n"
      "v_cvt_pk_bf16_f32 v57, v48, v48\n"
      "v_add_f32 v52, v52, v53\n"
      "v_add_f32 v54, v54, v55\n"
      "v_add_f32 v52, v52, v54\n"
      "v_cvt_pk_bf16_f32 v60, v52, v52\n"
      "v_and_b32 v53, s71, v60\n"
      "v_sub_f32 v52, v52, v53\n"
      "v_cvt_pk_bf16_f32 v61, v52, v52\n"
      "s_nop 1\n"
      "v_mfma_f32_32x32x16_bf16 v[16:31], v[120:123], v[56:59], v[16:31]\n"
      "v_mfma_f32_32x32x16_bf16 v[32:47], v[120:123], v[60:63], v[32:47]\n"
      "v_add_u32 v107, s68, v107\n"
      "s_waitcnt lgkmcnt(0)\n"
      "s_nop 9\n"
      "v_mul_f32 v16, v16, v72\n"
      "v_mul_f32 v17, v17, v73\n"
      "v_mul_f32 v18, v18, v74\n"
      "v_mul_f32 v19, v19, v75\n"
      "v_mul_f32 v20, v20, v76\n"
      "v_mul_f32 v21, v21, v77\n"
      "v_mul_f32 v22, v22, v78\n"
      "v_mul_f32 v23, v23, v79\n"
      "v_cvt_pk_bf16_f32 v64, v16, v17\n"
      "v_cvt_pk_bf16_f32 v65, v18, v19\n"
      "v_cvt_pk_bf16_f32 v66, v20, v21\n"
      "v_cvt_pk_bf16_f32 v67, v22, v23\n"
      "buffer_store_dwordx4 v[64:67], v107, %[srd], 0 offen offset:0 sc1\n"
      "v_mul_f32 v24, v24, v80\n"
      "v_mul_f32 v25, v25, v81\n"
      "v_mul_f32 v26, v26, v82\n"
      "v_mul_f32 v27, v27, v83\n"
      "v_mul_f32 v28, v28, v84\n"
      "v_mul_f32 v29, v29, v85\n"
      "v_mul_f32 v30, v30, v86\n"
      "v_mul_f32 v31, v31, v87\n"
      "v_cvt_pk_bf16_f32 v68, v24, v25\n"
      "v_cvt_pk_bf16_f32 v69, v26, v27\n"
      "v_cvt_pk_bf16_f32 v70, v28, v29\n"
      "v_cvt_pk_bf16_f32 v71, v30, v31\n"
      "buffer_store_dwordx4 v[68:71], v107, %[srd], 0 offen offset:512 sc1\n"
      "v_mul_f32 v32, v32, v72\n"
      "v_mul_f32 v33, v33, v73\n"
      "v_mul_f32 v34, v34, v74\n"
      "v_mul_f32 v35, v35, v75\n"
      "v_mul_f32 v36, v36, v76\n"
      "v_mul_f32 v37, v37, v77\n"
      "v_mul_f32 v38, v38, v78\n"
      "v_mul_f32 v39, v39, v79\n"
      "v_cvt_pk_bf16_f32 v64, v32, v33\n"
      "v_cvt_pk_bf16_f32 v65, v34, v35\n"
      "v_cvt_pk_bf16_f32 v66, v36, v37\n"
      "v_cvt_pk_bf16_f32 v67, v38, v39\n"
      "v_add_u32 v107, 0x20000, v107\n"
      "buffer_store_dwordx4 v[64:67], v107, %[srd], 0 offen offset:0 sc1\n"
      "v_mul_f32 v40, v40, v80\n"
      "v_mul_f32 v41, v41, v81\n"
      "v_mul_f32 v42, v42, v82\n"
      "v_mul_f32 v43, v43, v83\n"
      "v_mul_f32 v44, v44, v84\n"
      "v_mul_f32 v45, v45, v85\n"
      "v_mul_f32 v46, v46, v86\n"
      "v_mul_f32 v47, v47, v87\n"
      "v_cvt_pk_bf16_f32 v68, v40, v41\n"
      "v_cvt_pk_bf16_f32 v69, v42, v43\n"
      "v_cvt_pk_bf16_f32 v70, v44, v45\n"
      "v_cvt_pk_bf16_f32 v71, v46, v47\n"
      "buffer_store_dwordx4 v[68:71], v107, %[srd], 0 offen offset:512 sc1\n"
      "v_mov_b32 v112, -1\n"
      "v_mov_b32 v113, -1\n"
      "v_mov_b32 v114, -1\n"
      "v_mov_b32 v115, -1\n"
      "buffer_store_dwordx4 v[112:115], %[vsn], %[srd], 0 offen sc1\n"
      "v_bfe_u32 v6, v104, 8, 1\n"
      "v_mul_u32_u24 v6, 12288, v6\n"
      "v_add_u32 v6, 69632, v6\n"
      "v_add_u32 v12, v12, v6\n"
      "v_mov_b32 v8, v88\n"
      "v_mov_b32 v28, v92\n"
      "v_add_u32 v13, v13, v6\n"
      "v_mov_b32 v9, v89\n"
      "v_mov_b32 v29, v92\n"
      "v_add_u32 v14, v14, v6\n"
      "v_mov_b32 v10, v90\n"
      "v_mov_b32 v30, v92\n"
      "v_add_u32 v15, v15, v6\n"
      "v_mov_b32 v11, v91\n"
      "v_mov_b32 v31, v92\n"
      "s_branch 49f\n"
      "42:\n"
      "v_mov_b32 v2, 0\n"
      "v_mov_b32 v3, 0\n"
      "s_mov_b32 s71, 0xffff0000\n"
      "v_mov_b32 v116, 0\n"
      "buffer_load_dwordx2 v[116:117], v116, %[srd], 0 offen sc1\n"
      "s_movk_i32 s73, 2560\n"
      "v_mov_b32 v104, 163776\n"
      "ds_write_b64 v104, v[2:3]\n"
      "v_add_u32 v72, 57344, v8\n"
      "v_add_u32 v73, 57344, v9\n"
      "v_add_u32 v74, 57344, v10\n"
      "v_add_u32 v75, 57344, v11\n"
      "v_add_u32 v76, 57344, v12\n"
      "v_add_u32 v77, 57344, v13\n"
      "v_add_u32 v78, 57344, v14\n"
      "v_add_u32 v79, 57344, v15\n"
      "v_add_u32 v80, 114688, v8\n"
      "v_add_u32 v81, 114688, v9\n"
      "v_add_u32 v82, 114688, v10\n"
      "v_add_u32 v83, 114688, v11\n"
      "v_add_u32 v84, 114688, v12\n"
      "v_add_u32 v85, 114688, v13\n"
      "v_add_u32 v86, 114688, v14\n"
      "v_add_u32 v87, 114688, v15\n"
      "v_mbcnt_lo_u32_b32 v91, -1, 0\n"
      "v_mbcnt_hi_u32_b32 v91, -1, v91\n"
      "v_add3_u32 v91, v91, %[n0], 0\n"
      "v_mul_u32_u24 v88, 0xaaab, v91\n"
      "v_lshrrev_b32 v88, 19, v88\n"
      "v_lshlrev_b32 v91, 4, v91\n"
      "v_mad_u32_u24 v88, v88, s73, v91\n"
      "v_mbcnt_lo_u32_b32 v91, -1, 0\n"
      "v_mbcnt_hi_u32_b32 v91, -1, v91\n"
      "v_add3_u32 v91, v91, %[n0], 64\n"
      "v_mul_u32_u24 v89, 0xaaab, v91\n"
      "v_lshrrev_b32 v89, 19, v89\n"
      "v_lshlrev_b32 v91, 4, v91\n"
      "v_mad_u32_u24 v89, v89, s73, v91\n"
      "v_mbcnt_lo_u32_b32 v91, -1, 0\n"
      "v_mbcnt_hi_u32_b32 v91, -1, v91\n"
      "v_add3_u32 v91, v91, %[n0], 64\n"
      "v_add_u32 v91, 64, v91\n"
      "v_mul_u32_u24 v90, 0xaaab, v91\n"
      "v_lshrrev_b32 v90, 19, v90\n"
      "v_lshlrev_b32 v91, 4, v91\n"
      "v_mad_u32_u24 v90, v90, s73, v91\n"
      "v_mbcnt_lo_u32_b32 v104, -1, 0\n"
      "v_mbcnt_hi_u32_b32 v104, -1, v104\n"
      "v_lshlrev_b32 v104, 4, v104\n"
      "v_and_b32 v105, 0x1f0, v104\n"
      "v_lshrrev_b32 v106, 2, v105\n"
      "v_add_u32 v105, 144384, v105\n"
      "v_lshl_add_u32 v106, %[n0], 1, v106\n"
      "v_add_u32 v106, 144896, v106\n"
      "v_and_b32 v108, 0x330, v104\n"
      "v_and_b32 v107, 0x40, v104\n"
      "v_lshl_or_b32 v108, v107, 1, v108\n"
      "v_and_b32 v107, 0x80, v104\n"
      "v_lshrrev_b32 v107, 1, v107\n"
      "v_or_b32 v108, v108, v107\n"
      "s_add_u32 s73, %[ji], 16\n"
      "s_mul_i32 s73, s73, 86\n"
      "s_lshr_b32 s73, s73, 8\n"
      "v_mov_b32 v109, s73\n"
      "v_mov_b32 v91, s73\n"
      "s_add_u32 s73, %[ji], 32\n"
      "s_mul_i32 s73, s73, 86\n"
      "s_lshr_b32 s73, s73, 8\n"
      "v_mov_b32 v110, s73\n"
      "v_cmp_lt_u32 vcc, 63, v104\n"
      "s_mul_i32 s73, %[ji], 86\n"
      "s_lshr_b32 s73, s73, 8\n"
      "v_cndmask_b32 v109, v109, v110, vcc\n"
      "v_mov_b32 v110, s73\n"
      "v_cmp_lt_u32 vcc, 255, v104\n"
      "s_lshl_b32 s73, %[ti], 6\n"
      "v_add_u32 v109, s73, v109\n"
      "v_cndmask_b32 v91, v91, v110, vcc\n"
      "v_and_b32 v110, 0x30, v104\n"
      "s_lshl_b32 s73, %[gi], 4\n"
      "v_add_u32 v109, v109, v110\n"
      "v_cmp_lt_u32 vcc, 127, v104\n"
      "v_add_u32 v91, s73, v91\n"
      "s_nop 0\n"
      "v_cndmask_b32 v91, v109, v91, vcc\n"
      "v_lshlrev_b32 v91, 7, v91\n"
      "v_add_u32 v91, 4096, v91\n"
      "s_waitcnt vmcnt(5) lgkmcnt(0)\n"
      "s_barrier\n"
      "ds_read_b128 v[128:131], v8 offset:0\n"
      "ds_read_b64_tr_b16 v[48:49], v12 offset:0\n"
      "ds_read_b64_tr_b16 v[50:51], v12 offset:768\n"
      "ds_read_b128 v[132:135], v9 offset:0\n"
      "ds_read_b64_tr_b16 v[56:57], v13 offset:0\n"
      "ds_read_b64_tr_b16 v[58:59], v13 offset:768\n"
      "s_waitcnt lgkmcnt(3)\n"
      "s_add_u32 s72, %[rot], 2\n"
      "s_and_b32 s72, s72, 7\n"
      "s_lshl_b32 s68, s72, 7\n"
      "s_mul_i32 s69, s72, 196608\n"
      "s_add_u32 m0, %[sx], 57344\n"
      "v_mfma_f32_32x32x16_bf16 v[16:31], v[48:51], v[128:131], 0\n"
      "buffer_load_dwordx4 %[vx0], %[srdx], s68 offen lds\n"
      "ds_read_b128 v[136:139], v10 offset:0\n"
      "ds_read_b64_tr_b16 v[64:65], v14 offset:0\n"
      "ds_read_b64_tr_b16 v[66:67], v14 offset:768\n"
      "s_add_u32 m0, %[sx], 58368\n"
      "s_nop 0\n"
      "buffer_load_dwordx4 %[vx1], %[srdx], s68 offen lds\n"
      "s_add_u32 m0, %[sq], 57344\n"
      "s_nop 0\n"
      "buffer_load_dwordx4 %[vq0], %[srdq], s69 offen lds\n"
      "s_add_u32 m0, %[sq], 61440\n"
      "s_nop 0\n"
      "buffer_load_dwordx4 %[vq1], %[srdq], s69 offen lds\n"
      "v_mfma_f32_16x16x32_bf16 v[120:123], v[92:95], v[48:51], 0\n"
      "v_mfma_f32_16x16x32_bf16 v[32:35], v[92:95], v[128:131], 0\n"
      "s_waitcnt lgkmcnt(3)\n"
      "s_add_u32 s72, %[rot], 3\n"
      "s_and_b32 s72, s72, 7\n"
      "s_lshl_b32 s68, s72, 7\n"
      "s_mul_i32 s69, s72, 196608\n"
      "s_add_u32 m0, %[sx], 86016\n"
      "v_mfma_f32_32x32x16_bf16 v[16:31], v[56:59], v[132:135], v[16:31]\n"
      "buffer_load_dwordx4 %[vx0], %[srdx], s68 offen lds\n"
      "ds_read_b128 v[140:143], v11 offset:0\n"
      "ds_read_b64_tr_b16 v[48:49], v15 offset:0\n"
      "ds_read_b64_tr_b16 v[50:51], v15 offset:768\n"
      "s_add_u32 m0, %[sx], 87040\n"
      "s_nop 0\n"
      "buffer_load_dwordx4 %[vx1], %[srdx], s68 offen lds\n"
      "s_add_u32 m0, %[sq], 86016\n"
      "s_nop 0\n"
      "buffer_load_dwordx4 %[vq0], %[srdq], s69 offen lds\n"
      "s_add_u32 m0, %[sq], 90112\n"
      "s_nop 0\n"
      "buffer_load_dwordx4 %[vq1], %[srdq], s69 offen lds\n"
      "v_lshlrev_b32 v4, 16, v132\n"
      "v_and_b32 v5, s71, v132\n"
      "v_fmac_f32 v2, v4, v4\n"
      "v_fmac_f32 v3, v5, v5\n"
      "v_lshlrev_b32 v6, 16, v133\n"
      "v_and_b32 v7, s71, v133\n"
      "v_fmac_f32 v2, v6, v6\n"
      "v_fmac_f32 v3, v7, v7\n"
      "v_lshlrev_b32 v4, 16, v134\n"
      "v_and_b32 v5, s71, v134\n"
      "v_fmac_f32 v2, v4, v4\n"
      "v_fmac_f32 v3, v5, v5\n"
      "v_lshlrev_b32 v6, 16, v135\n"
      "v_and_b32 v7, s71, v135\n"
      "v_fmac_f32 v2, v6, v6\n"
      "v_fmac_f32 v3, v7, v7\n"
      "v_mfma_f32_16x16x32_bf16 v[32:35], v[92:95], v[132:135], v[32:35]\n"
      "s_waitcnt vmcnt(9)\n"
      "s_barrier\n"
      "s_waitcnt lgkmcnt(3)\n"
      "s_add_u32 s72, %[rot], 4\n"
      "s_and_b32 s72, s72, 7\n"
      "s_lshl_b32 s68, s72, 7\n"
      "s_mul_i32 s69, s72, 196608\n"
      "s_add_u32 m0, %[sx], 114688\n"
      "v_mfma_f32_32x32x16_bf16 v[16:31], v[64:67], v[136:139], v[16:31]\n"
      "buffer_load_dwordx4 %[vx0], %[srdx], s68 offen lds\n"
      "ds_read_b128 v[144:147], v8 offset:28672\n"
      "ds_read_b64_tr_b16 v[56:57], v12 offset:28672\n"
      "ds_read_b64_tr_b16 v[58:59], v12 offset:29440\n"
      "v_mfma_f32_16x16x32_bf16 v[32:35], v[92:95], v[136:139], v[32:35]\n"
      "s_waitcnt lgkmcnt(3)\n"
      "s_add_u32 m0, %[sx], 115712\n"
      "v_mfma_f32_32x32x16_bf16 v[16:31], v[48:51], v[140:143], v[16:31]\n"
      "buffer_load_dwordx4 %[vx1], %[srdx], s68 offen lds\n"
      "ds_read_b128 v[148:151], v9 offset:28672\n"
      "ds_read_b64_tr_b16 v[64:65], v13 offset:28672\n"
      "ds_read_b64_tr_b16 v[66:67], v13 offset:29440\n"
      "v_lshlrev_b32 v4, 16, v140\n"
      "v_and_b32 v5, s71, v140\n"
      "v_fmac_f32 v2, v4, v4\n"
      "v_fmac_f32 v3, v5, v5\n"
      "v_lshlrev_b32 v6, 16, v141\n"
      "v_and_b32 v7, s71, v141\n"
      "v_fmac_f32 v2, v6, v6\n"
      "v_fmac_f32 v3, v7, v7\n"
      "v_lshlrev_b32 v4, 16, v142\n"
      "v_and_b32 v5, s71, v142\n"
      "v_fmac_f32 v2, v4, v4\n"
      "v_fmac_f32 v3, v5, v5\n"
      "v_lshlrev_b32 v6, 16, v143\n"
      "v_and_b32 v7, s71, v143\n"
      "v_fmac_f32 v2, v6, v6\n"
      "v_fmac_f32 v3, v7, v7\n"
      "v_mfma_f32_16x16x32_bf16 v[32:35], v[92:95], v[140:143], v[32:35]\n"
      "s_waitcnt lgkmcnt(3)\n"
      "s_add_u32 m0, %[sq], 114688\n"
      "v_mfma_f32_32x32x16_bf16 v[16:31], v[56:59], v[144:147], v[16:31]\n"
      "buffer_load_dwordx4 %[vq0], %[srdq], s69 offen lds\n"
      "ds_read_b128 v[152:155], v10 offset:28672\n"
      "ds_read_b64_tr_b16 v[48:49], v14 offset:28672\n"
      "ds_read_b64_tr_b16 v[50:51], v14 offset:29440\n"
      "v_mfma_f32_16x16x32_bf16 v[120:123], v[92:95], v[56:59], v[120:123]\n"
      "v_mfma_f32_16x16x32_bf16 v[32:35], v[92:95], v[144:147], v[32:35]\n"
      "s_waitcnt lgkmcnt(3)\n"
      "s_add_u32 m0, %[sq], 118784\n"
      "v_mfma_f32_32x32x16_bf16 v[16:31], v[64:67], v[148:151], v[16:31]\n"
      "buffer_load_dwordx4 %[vq1], %[srdq], s69 offen lds\n"
      "ds_read_b128 v[156:159], v11 offset:28672\n"
      "ds_read_b64_tr_b16 v[56:57], v15 offset:28672\n"
      "ds_read_b64_tr_b16 v[58:59], v15 offset:29440\n"
      "v_lshlrev_b32 v4, 16, v148\n"
      "v_and_b32 v5, s71, v148\n"
      "v_fmac_f32 v2, v4, v4\n"
      "v_fmac_f32 v3, v5, v5\n"
      "v_lshlrev_b32 v6, 16, v149\n"
      "v_and_b32 v7, s71, v149\n"
      "v_fmac_f32 v2, v6, v6\n"
      "v_fmac_f32 v3, v7, v7\n"
      "v_lshlrev_b32 v4, 16, v150\n"
      "v_and_b32 v5, s71, v150\n"
      "v_fmac_f32 v2, v4, v4\n"
      "v_fmac_f32 v3, v5, v5\n"
      "v_lshlrev_b32 v6, 16, v151\n"
      "v_and_b32 v7, s71, v151\n"
      "v_fmac_f32 v2, v6, v6\n"
      "v_fmac_f32 v3, v7, v7\n"
      "v_mfma_f32_16x16x32_bf16 v[32:35], v[92:95], v[148:151], v[32:35]\n"
      "s_waitcnt vmcnt(8)\n"
      "s_barrier\n"
      "s_waitcnt lgkmcnt(3)\n"
      "s_add_u32 s72, %[rot], 5\n"
      "s_and_b32 s72, s72, 7\n"
      "s_lshl_b32 s68, s72, 7\n"
      "s_mul_i32 s69, s72, 196608\n"
      "s_add_u32 m0, %[sx], 0\n"
      "v_mfma_f32_32x32x16_bf16 v[16:31], v[48:51], v[152:155], v[16:31]\n"
      "buffer_load_dwordx4 %[vx0], %[srdx], s68 offen lds\n"
      "ds_read_b128 v[160:163], v72 offset:0\n"
      "ds_read_b64_tr_b16 v[64:65], v76 offset:0\n"
      "ds_read_b64_tr_b16 v[66:67], v76 offset:768\n"
      "v_mfma_f32_16x16x32_bf16 v[32:35], v[92:95], v[152:155], v[32:35]\n"
      "s_waitcnt lgkmcnt(3)\n"
      "s_add_u32 m0, %[sx], 1024\n"
      "v_mfma_f32_32x32x16_bf16 v[16:31], v[56:59], v[156:159], v[16:31]\n"
      "buffer_load_dwordx4 %[vx1], %[srdx], s68 offen lds\n"
      "ds_read_b128 v[164:167], v73 offset:0\n"
      "ds_read_b64_tr_b16 v[48:49], v77 offset:0\n"
      "ds_read_b64_tr_b16 v[50:51], v77 offset:768\n"
      "v_lshlrev_b32 v4, 16, v156\n"
      "v_and_b32 v5, s71, v156\n"
      "v_fmac_f32 v2, v4, v4\n"
      "v_fmac_f32 v3, v5, v5\n"
      "v_lshlrev_b32 v6, 16, v157\n"
      "v_and_b32 v7, s71, v157\n"
      "v_fmac_f32 v2, v6, v6\n"
      "v_fmac_f32 v3, v7, v7\n"
      "v_lshlrev_b32 v4, 16, v158\n"
      "v_and_b32 v5, s71, v158\n"
      "v_fmac_f32 v2, v4, v4\n"
      "v_fmac_f32 v3, v5, v5\n"
      "v_lshlrev_b32 v6, 16, v159\n"
      "v_and_b32 v7, s71, v159\n"
      "v_fmac_f32 v2, v6, v6\n"
      "v_fmac_f32 v3, v7, v7\n"
      "v_mfma_f32_16x16x32_bf16 v[32:35], v[92:95], v[156:159], v[32:35]\n"
      "s_waitcnt lgkmcnt(3)\n"
      "s_add_u32 m0, %[sq], 0\n"
      "v_mfma_f32_32x32x16_bf16 v[16:31], v[64:67], v[160:163], v[16:31]\n"
      "buffer_load_dwordx4 %[vq0], %[srdq], s69 offen lds\n"
      "ds_read_b128 v[168:171], v74 offset:0\n"
      "ds_read_b64_tr_b16 v[56:57], v78 offset:0\n"
      "ds_read_b64_tr_b16 v[58:59], v78 offset:768\n"
      "v_mfma_f32_16x16x32_bf16 v[120:123], v[92:95], v[64:67], v[120:123]\n"
      "v_mfma_f32_16x16x32_bf16 v[32:35], v[92:95], v[160:163], v[32:35]\n"
      "s_waitcnt lgkmcnt(3)\n"
      "s_add_u32 m0, %[sq], 4096\n"
      "v_mfma_f32_32x32x16_bf16 v[16:31], v[48:51], v[164:167], v[16:31]\n"
      "buffer_load_dwordx4 %[vq1], %[srdq], s69 offen lds\n"
      "ds_read_b128 v[172:175], v75 offset:0\n"
      "ds_read_b64_tr_b16 v[64:65], v79 offset:0\n"
      "ds_read_b64_tr_b16 v[66:67], v79 offset:768\n"
      "v_lshlrev_b32 v4, 16, v164\n"
      "v_and_b32 v5, s71, v164\n"
      "v_fmac_f32 v2, v4, v4\n"
      "v_fmac_f32 v3, v5, v5\n"
      "v_lshlrev_b32 v6, 16, v165\n"
      "v_and_b32 v7, s71, v165\n"
      "v_fmac_f32 v2, v6, v6\n"
      "v_fmac_f32 v3, v7, v7\n"
      "v_lshlrev_b32 v4, 16, v166\n"
      "v_and_b32 v5, s71, v166\n"
      "v_fmac_f32 v2, v4, v4\n"
      "v_fmac_f32 v3, v5, v5\n"
      "v_lshlrev_b32 v6, 16, v167\n"
      "v_and_b32 v7, s71, v167\n"
      "v_fmac_f32 v2, v6, v6\n"
      "v_fmac_f32 v3, v7, v7\n"
      "v_mfma_f32_16x16x32_bf16 v[32:35], v[92:95], v[164:167], v[32:35]\n"
      "s_waitcnt vmcnt(8)\n"
      "s_barrier\n"
      "s_waitcnt lgkmcnt(3)\n"
      "s_add_u32 s72, %[rot], 6\n"
      "s_and_b32 s72, s72, 7\n"
      "s_lshl_b32 s68, s72, 7\n"
      "s_mul_i32 s69, s72, 196608\n"
      "s_add_u32 m0, %[sx], 28672\n"
      "v_mfma_f32_32x32x16_bf16 v[16:31], v[56:59], v[168:171], v[16:31]\n"
      "buffer_load_dwordx4 %[vx0], %[srdx], s68 offen lds\n"
      "ds_read_b128 v[176:179], v72 offset:28672\n"
      "ds_read_b64_tr_b16 v[48:49], v76 offset:28672\n"
      "ds_read_b64_tr_b16 v[50:51], v76 offset:29440\n"
      "v_mfma_f32_16x16x32_bf16 v[32:35], v[92:95], v[168:171], v[32:35]\n"
      "s_waitcnt lgkmcnt(3)\n"
      "s_add_u32 m0, %[sx], 29696\n"
      "v_mfma_f32_32x32x16_bf16 v[16:31], v[64:67], v[172:175], v[16:31]\n"
      "buffer_load_dwordx4 %[vx1], %[srdx], s68 offen lds\n"
      "ds_read_b128 v[180:183], v73 offset:28672\n"
      "ds_read_b64_tr_b16 v[56:57], v77 offset:28672\n"
      "ds_read_b64_tr_b16 v[58:59], v77 offset:29440\n"
      "v_lshlrev_b32 v4, 16, v172\n"
      "v_and_b32 v5, s71, v172\n"
      "v_fmac_f32 v2, v4, v4\n"
      "v_fmac_f32 v3, v5, v5\n"
      "v_lshlrev_b32 v6, 16, v173\n"
      "v_and_b32 v7, s71, v173\n"
      "v_fmac_f32 v2, v6, v6\n"
      "v_fmac_f32 v3, v7, v7\n"
      "v_lshlrev_b32 v4, 16, v174\n"
      "v_and_b32 v5, s71, v174\n"
      "v_fmac_f32 v2, v4, v4\n"
      "v_fmac_f32 v3, v5, v5\n"
      "v_lshlrev_b32 v6, 16, v175\n"
      "v_and_b32 v7, s71, v175\n"
      "v_fmac_f32 v2, v6, v6\n"
      "v_fmac_f32 v3, v7, v7\n"
      "v_mfma_f32_16x16x32_bf16 v[32:35], v[92:95], v[172:175], v[32:35]\n"
      "s_waitcnt lgkmcnt(3)\n"
      "s_add_u32 m0, %[sq], 28672\n"
      "v_mfma_f32_32x32x16_bf16 v[16:31], v[48:51], v[176:179], v[16:31]\n"
      "buffer_load_dwordx4 %[vq0], %[srdq], s69 offen lds\n"
      "ds_read_b128 v[184:187], v74 offset:28672\n"
      "ds_read_b64_tr_b16 v[64:65], v78 offset:28672\n"
      "ds_read_b64_tr_b16 v[66:67], v78 offset:29440\n"
      "v_mfma_f32_16x16x32_bf16 v[120:123], v[92:95], v[48:51], v[120:123]\n"
      "v_mfma_f32_16x16x32_bf16 v[32:35], v[92:95], v[176:179], v[32:35]\n"
      "s_waitcnt lgkmcnt(3)\n"
      "s_add_u32 m0, %[sq], 32768\n"
      "v_mfma_f32_32x32x16_bf16 v[16:31], v[56:59], v[180:183], v[16:31]\n"
      "buffer_load_dwordx4 %[vq1], %[srdq], s69 offen lds\n"
      "ds_read_b128 v[188:191], v75 offset:28672\n"
      "ds_read_b64_tr_b16 v[48:49], v79 offset:28672\n"
      "ds_read_b64_tr_b16 v[50:51], v79 offset:29440\n"
      "v_lshlrev_b32 v4, 16, v180\n"
      "v_and_b32 v5, s71, v180\n"
      "v_fmac_f32 v2, v4, v4\n"
      "v_fmac_f32 v3, v5, v5\n"
      "v_lshlrev_b32 v6, 16, v181\n"
      "v_and_b32 v7, s71, v181\n"
      "v_fmac_f32 v2, v6, v6\n"
      "v_fmac_f32 v3, v7, v7\n"
      "v_lshlrev_b32 v4, 16, v182\n"
      "v_and_b32 v5, s71, v182\n"
      "v_fmac_f32 v2, v4, v4\n"
      "v_fmac_f32 v3, v5, v5\n"
      "v_lshlrev_b32 v6, 16, v183\n"
      "v_and_b32 v7, s71, v183\n"
      "v_fmac_f32 v2, v6, v6\n"
      "v_fmac_f32 v3, v7, v7\n"
      "v_mfma_f32_16x16x32_bf16 v[32:35], v[92:95], v[180:183], v[32:35]\n"
      "s_waitcnt vmcnt(8)\n"
      "s_barrier\n"
      "s_waitcnt lgkmcnt(3)\n"
      "s_add_u32 s72, %[rot], 7\n"
      "s_and_b32 s72, s72, 7\n"
      "s_lshl_b32 s68, s72, 7\n"
      "s_mul_i32 s69, s72, 196608\n"
      "s_add_u32 m0, %[sx], 57344\n"
      "v_mfma_f32_32x32x16_bf16 v[16:31], v[64:67], v[184:187], v[16:31]\n"
      "buffer_load_dwordx4 %[vx0], %[srdx], s68 offen lds\n"
      "ds_read_b128 v[192:195], v80 offset:0\n"
      "ds_read_b64_tr_b16 v[56:57], v84 offset:0\n"
      "ds_read_b64_tr_b16 v[58:59], v84 offset:768\n"
      "v_mfma_f32_16x16x32_bf16 v[32:35], v[92:95], v[184:187], v[32:35]\n"
      "s_waitcnt lgkmcnt(3)\n"
      "s_add_u32 m0, %[sx], 58368\n"
      "v_mfma_f32_32x32x16_bf16 v[16:31], v[48:51], v[188:191], v[16:31]\n"
      "buffer_load_dwordx4 %[vx1], %[srdx], s68 offen lds\n"
      "ds_read_b128 v[196:199], v81 offset:0\n"
      "ds_read_b64_tr_b16 v[64:65], v85 offset:0\n"
      "ds_read_b64_tr_b16 v[66:67], v85 offset:768\n"
      "v_lshlrev_b32 v4, 16, v188\n"
      "v_and_b32 v5, s71, v188\n"
      "v_fmac_f32 v2, v4, v4\n"
      "v_fmac_f32 v3, v5, v5\n"
      "v_lshlrev_b32 v6, 16, v189\n"
      "v_and_b32 v7, s71, v189\n"
      "v_fmac_f32 v2, v6, v6\n"
      "v_fmac_f32 v3, v7, v7\n"
      "v_lshlrev_b32 v4, 16, v190\n"
      "v_and_b32 v5, s71, v190\n"
      "v_fmac_f32 v2, v4, v4\n"
      "v_fmac_f32 v3, v5, v5\n"
      "v_lshlrev_b32 v6, 16, v191\n"
      "v_and_b32 v7, s71, v191\n"
      "v_fmac_f32 v2, v6, v6\n"
      "v_fmac_f32 v3, v7, v7\n"
      "v_mfma_f32_16x16x32_bf16 v[32:35], v[92:95], v[188:191], v[32:35]\n"
      "s_waitcnt lgkmcnt(3)\n"
      "s_add_u32 m0, %[sq], 57344\n"
      "v_mfma_f32_32x32x16_bf16 v[16:31], v[56:59], v[192:195], v[16:31]\n"
      "buffer_load_dwordx4 %[vq0], %[srdq], s69 offen lds\n"
      "ds_read_b128 v[200:203], v82 offset:0\n"
      "ds_read_b64_tr_b16 v[48:49], v86 offset:0\n"
      "ds_read_b64_tr_b16 v[50:51], v86 offset:768\n"
      "v_mfma_f32_16x16x32_bf16 v[120:123], v[92:95], v[56:59], v[120:123]\n"
      "v_mfma_f32_16x16x32_bf16 v[32:35], v[92:95], v[192:195], v[32:35]\n"
      "s_waitcnt lgkmcnt(3)\n"
      "s_add_u32 m0, %[sq], 61440\n"
      "v_mfma_f32_32x32x16_bf16 v[16:31], v[64:67], v[196:199], v[16:31]\n"
      "buffer_load_dwordx4 %[vq1], %[srdq], s69 offen lds\n"
      "ds_read_b128 v[204:207], v83 offset:0\n"
      "ds_read_b64_tr_b16 v[56:57], v87 offset:0\n"
      "ds_read_b64_tr_b16 v[58:59], v87 offset:768\n"
      "v_lshlrev_b32 v4, 16, v196\n"
      "v_and_b32 v5, s71, v196\n"
      "v_fmac_f32 v2, v4, v4\n"
      "v_fmac_f32 v3, v5, v5\n"
      "v_lshlrev_b32 v6, 16, v197\n"
      "v_and_b32 v7, s71, v197\n"
      "v_fmac_f32 v2, v6, v6\n"
      "v_fmac_f32 v3, v7, v7\n"
      "v_lshlrev_b32 v4, 16, v198\n"
      "v_and_b32 v5, s71, v198\n"
      "v_fmac_f32 v2, v4, v4\n"
      "v_fmac_f32 v3, v5, v5\n"
      "v_lshlrev_b32 v6, 16, v199\n"
      "v_and_b32 v7, s71, v199\n"
      "v_fmac_f32 v2, v6, v6\n"
      "v_fmac_f32 v3, v7, v7\n"
      "v_mfma_f32_16x16x32_bf16 v[32:35], v[92:95], v[196:199], v[32:35]\n"
      "s_waitcnt vmcnt(8)\n"
      "s_barrier\n"
      "s_waitcnt lgkmcnt(3)\n"
      "s_add_u32 s72, %[rot], 0\n"
      "s_and_b32 s72, s72, 7\n"
      "s_mul_i32 s70, s72, 176128\n"
      "s_add_u32 m0, %[sg], 86016\n"
      "v_mfma_f32_32x32x16_bf16 v[16:31], v[48:51], v[200:203], v[16:31]\n"
      "buffer_load_dwordx4 v88, %[srdg], s70 offen lds\n"
      "ds_read_b128 v[208:211], v8 offset:0\n"
      "ds_read_b64_tr_b16 v[64:65], v12 offset:0\n"
      "ds_read_b64_tr_b16 v[66:67], v12 offset:768\n"
      "v_mfma_f32_16x16x32_bf16 v[32:35], v[92:95], v[200:203], v[32:35]\n"
      "s_waitcnt lgkmcnt(3)\n"
      "s_add_u32 m0, %[sg], 87040\n"
      "v_mfma_f32_32x32x16_bf16 v[16:31], v[56:59], v[204:207], v[16:31]\n"
      "buffer_load_dwordx4 v89, %[srdg], s70 offen lds\n"
      "ds_read_b128 v[212:215], v9 offset:0\n"
      "ds_read_b64_tr_b16 v[48:49], v13 offset:0\n"
      "ds_read_b64_tr_b16 v[50:51], v13 offset:768\n"
      "v_lshlrev_b32 v4, 16, v204\n"
      "v_and_b32 v5, s71, v204\n"
      "v_fmac_f32 v2, v4, v4\n"
      "v_fmac_f32 v3, v5, v5\n"
      "v_lshlrev_b32 v6, 16, v205\n"
      "v_and_b32 v7, s71, v205\n"
      "v_fmac_f32 v2, v6, v6\n"
      "v_fmac_f32 v3, v7, v7\n"
      "v_lshlrev_b32 v4, 16, v206\n"
      "v_and_b32 v5, s71, v206\n"
      "v_fmac_f32 v2, v4, v4\n"
      "v_fmac_f32 v3, v5, v5\n"
      "v_lshlrev_b32 v6, 16, v207\n"
      "v_and_b32 v7, s71, v207\n"
      "v_fmac_f32 v2, v6, v6\n"
      "v_fmac_f32 v3, v7, v7\n"
      "v_mfma_f32_16x16x32_bf16 v[32:35], v[92:95], v[204:207], v[32:35]\n"
      "s_waitcnt lgkmcnt(3)\n"
      "s_add_u32 m0, %[sg], 88064\n"
      "v_mfma_f32_32x32x16_bf16 v[16:31], v[64:67], v[208:211], v[16:31]\n"
      "buffer_load_dwordx4 v90, %[srdg], s70 offen lds\n"
      "ds_read_b128 v[216:219], v10 offset:0\n"
      "ds_read_b64_tr_b16 v[56:57], v14 offset:0\n"
      "ds_read_b64_tr_b16 v[58:59], v14 offset:768\n"
      "v_mfma_f32_16x16x32_bf16 v[120:123], v[92:95], v[64:67], v[120:123]\n"
      "v_mfma_f32_16x16x32_bf16 v[32:35], v[92:95], v[208:211], v[32:35]\n"
      "s_waitcnt lgkmcnt(3)\n"
      "v_mfma_f32_32x32x16_bf16 v[16:31], v[48:51], v[212:215], v[16:31]\n"
      "ds_read_b128 v[220:223], v11 offset:0\n"
      "ds_read_b64_tr_b16 v[64:65], v15 offset:0\n"
      "ds_read_b64_tr_b16 v[66:67], v15 offset:768\n"
      "v_lshlrev_b32 v4, 16, v212\n"
      "v_and_b32 v5, s71, v212\n"
      "v_fmac_f32 v2, v4, v4\n"
      "v_fmac_f32 v3, v5, v5\n"
      "v_lshlrev_b32 v6, 16, v213\n"
      "v_and_b32 v7, s71, v213\n"
      "v_fmac_f32 v2, v6, v6\n"
      "v_fmac_f32 v3, v7, v7\n"
      "v_lshlrev_b32 v4, 16, v214\n"
      "v_and_b32 v5, s71, v214\n"
      "v_fmac_f32 v2, v4, v4\n"
      "v_fmac_f32 v3, v5, v5\n"
      "v_lshlrev_b32 v6, 16, v215\n"
      "v_and_b32 v7, s71, v215\n"
      "v_fmac_f32 v2, v6, v6\n"
      "v_fmac_f32 v3, v7, v7\n"
      "v_mfma_f32_16x16x32_bf16 v[32:35], v[92:95], v[212:215], v[32:35]\n"
      "s_waitcnt vmcnt(7)\n"
      "s_barrier\n"
      "s_waitcnt lgkmcnt(3)\n"
      "s_add_u32 s72, %[rot], 1\n"
      "s_and_b32 s72, s72, 7\n"
      "s_mul_i32 s70, s72, 176128\n"
      "s_add_u32 m0, %[sg], 114688\n"
      "v_mfma_f32_32x32x16_bf16 v[16:31], v[56:59], v[216:219], v[16:31]\n"
      "buffer_load_dwordx4 v88, %[srdg], s70 offen lds\n"
      "ds_read_b128 v[224:227], v8 offset:28672\n"
      "ds_read_b64_tr_b16 v[48:49], v12 offset:28672\n"
      "ds_read_b64_tr_b16 v[50:51], v12 offset:29440\n"
      "v_mfma_f32_16x16x32_bf16 v[32:35], v[92:95], v[216:219], v[32:35]\n"
      "s_waitcnt lgkmcnt(3)\n"
      "s_add_u32 m0, %[sg], 115712\n"
      "v_mfma_f32_32x32x16_bf16 v[16:31], v[64:67], v[220:223], v[16:31]\n"
      "buffer_load_dwordx4 v89, %[srdg], s70 offen lds\n"
      "ds_read_b128 v[228:231], v9 offset:28672\n"
      "ds_read_b64_tr_b16 v[56:57], v13 offset:28672\n"
      "ds_read_b64_tr_b16 v[58:59], v13 offset:29440\n"
      "v_lshlrev_b32 v4, 16, v220\n"
      "v_and_b32 v5, s71, v220\n"
      "v_fmac_f32 v2, v4, v4\n"
      "v_fmac_f32 v3, v5, v5\n"
      "v_lshlrev_b32 v6, 16, v221\n"
      "v_and_b32 v7, s71, v221\n"
      "v_fmac_f32 v2, v6, v6\n"
      "v_fmac_f32 v3, v7, v7\n"
      "v_lshlrev_b32 v4, 16, v222\n"
      "v_and_b32 v5, s71, v222\n"
      "v_fmac_f32 v2, v4, v4\n"
      "v_fmac_f32 v3, v5, v5\n"
      "v_lshlrev_b32 v6, 16, v223\n"
      "v_and_b32 v7, s71, v223\n"
      "v_fmac_f32 v2, v6, v6\n"
      "v_fmac_f32 v3, v7, v7\n"
      "v_mfma_f32_16x16x32_bf16 v[32:35], v[92:95], v[220:223], v[32:35]\n"
      "s_waitcnt lgkmcnt(3)\n"
      "s_add_u32 m0, %[sg], 116736\n"
      "v_mfma_f32_32x32x16_bf16 v[16:31], v[48:51], v[224:227], v[16:31]\n"
      "buffer_load_dwordx4 v90, %[srdg], s70 offen lds\n"
      "ds_read_b128 v[232:235], v10 offset:28672\n"
      "ds_read_b64_tr_b16 v[64:65], v14 offset:28672\n"
      "ds_read_b64_tr_b16 v[66:67], v14 offset:29440\n"
      "v_mfma_f32_16x16x32_bf16 v[120:123], v[92:95], v[48:51], v[120:123]\n"
      "v_mfma_f32_16x16x32_bf16 v[32:35], v[92:95], v[224:227], v[32:35]\n"
      "s_waitcnt lgkmcnt(3)\n"
      "v_mfma_f32_32x32x16_bf16 v[16:31], v[56:59], v[228:231], v[16:31]\n"
      "ds_read_b128 v[236:239], v11 offset:28672\n"
      "ds_read_b64_tr_b16 v[48:49], v15 offset:28672\n"
      "ds_read_b64_tr_b16 v[50:51], v15 offset:29440\n"
      "v_lshlrev_b32 v4, 16, v228\n"
      "v_and_b32 v5, s71, v228\n"
      "v_fmac_f32 v2, v4, v4\n"
      "v_fmac_f32 v3, v5, v5\n"
      "v_lshlrev_b32 v6, 16, v229\n"
      "v_and_b32 v7, s71, v229\n"
      "v_fmac_f32 v2, v6, v6\n"
      "v_fmac_f32 v3, v7, v7\n"
      "v_lshlrev_b32 v4, 16, v230\n"
      "v_and_b32 v5, s71, v230\n"
      "v_fmac_f32 v2, v4, v4\n"
      "v_fmac_f32 v3, v5, v5\n"
      "v_lshlrev_b32 v6, 16, v231\n"
      "v_and_b32 v7, s71, v231\n"
      "v_fmac_f32 v2, v6, v6\n"
      "v_fmac_f32 v3, v7, v7\n"
      "v_mfma_f32_16x16x32_bf16 v[32:35], v[92:95], v[228:231], v[32:35]\n"
      "s_waitcnt vmcnt(6)\n"
      "s_barrier\n"
      "s_waitcnt lgkmcnt(3)\n"
      "s_add_u32 s72, %[rot], 2\n"
      "s_and_b32 s72, s72, 7\n"
      "s_mul_i32 s70, s72, 176128\n"
      "s_add_u32 m0, %[sg], 0\n"
      "v_mfma_f32_32x32x16_bf16 v[16:31], v[64:67], v[232:235], v[16:31]\n"
      "buffer_load_dwordx4 v88, %[srdg], s70 offen lds\n"
      "ds_read_b128 v[240:243], v72 offset:0\n"
      "ds_read_b64_tr_b16 v[56:57], v76 offset:0\n"
      "ds_read_b64_tr_b16 v[58:59], v76 offset:768\n"
      "v_mfma_f32_16x16x32_bf16 v[32:35], v[92:95], v[232:235], v[32:35]\n"
      "s_waitcnt lgkmcnt(3)\n"
      "s_add_u32 m0, %[sg], 1024\n"
      "v_mfma_f32_32x32x16_bf16 v[16:31], v[48:51], v[236:239], v[16:31]\n"
      "buffer_load_dwordx4 v89, %[srdg], s70 offen lds\n"
      "ds_read_b128 v[244:247], v73 offset:0\n"
      "ds_read_b64_tr_b16 v[64:65], v77 offset:0\n"
      "ds_read_b64_tr_b16 v[66:67], v77 offset:768\n"
      "v_lshlrev_b32 v4, 16, v236\n"
      "v_and_b32 v5, s71, v236\n"
      "v_fmac_f32 v2, v4, v4\n"
      "v_fmac_f32 v3, v5, v5\n"
      "v_lshlrev_b32 v6, 16, v237\n"
      "v_and_b32 v7, s71, v237\n"
      "v_fmac_f32 v2, v6, v6\n"
      "v_fmac_f32 v3, v7, v7\n"
      "v_lshlrev_b32 v4, 16, v238\n"
      "v_and_b32 v5, s71, v238\n"
      "v_fmac_f32 v2, v4, v4\n"
      "v_fmac_f32 v3, v5, v5\n"
      "v_lshlrev_b32 v6, 16, v239\n"
      "v_and_b32 v7, s71, v239\n"
      "v_fmac_f32 v2, v6, v6\n"
      "v_fmac_f32 v3, v7, v7\n"
      "v_mfma_f32_16x16x32_bf16 v[32:35], v[92:95], v[236:239], v[32:35]\n"
      "s_waitcnt lgkmcnt(3)\n"
      "s_add_u32 m0, %[sg], 2048\n"
      "v_mfma_f32_32x32x16_bf16 v[16:31], v[56:59], v[240:243], v[16:31]\n"
      "buffer_load_dwordx4 v90, %[srdg], s70 offen lds\n"
      "ds_read_b128 v[248:251], v74 offset:0\n"
      "ds_read_b64_tr_b16 v[48:49], v78 offset:0\n"
      "ds_read_b64_tr_b16 v[50:51], v78 offset:768\n"
      "v_mfma_f32_16x16x32_bf16 v[120:123], v[92:95], v[56:59], v[120:123]\n"
      "v_mfma_f32_16x16x32_bf16 v[32:35], v[92:95], v[240:243], v[32:35]\n"
      "s_waitcnt lgkmcnt(3)\n"
      "v_mfma_f32_32x32x16_bf16 v[16:31], v[64:67], v[244:247], v[16:31]\n"
      "ds_read_b128 v[252:255], v75 offset:0\n"
      "ds_read_b64_tr_b16 v[56:57], v79 offset:0\n"
      "ds_read_b64_tr_b16 v[58:59], v79 offset:768\n"
      "v_lshlrev_b32 v4, 16, v244\n"
      "v_and_b32 v5, s71, v244\n"
      "v_fmac_f32 v2, v4, v4\n"
      "v_fmac_f32 v3, v5, v5\n"
      "v_lshlrev_b32 v6, 16, v245\n"
      "v_and_b32 v7, s71, v245\n"
      "v_fmac_f32 v2, v6, v6\n"
      "v_fmac_f32 v3, v7, v7\n"
      "v_lshlrev_b32 v4, 16, v246\n"
      "v_and_b32 v5, s71, v246\n"
      "v_fmac_f32 v2, v4, v4\n"
      "v_fmac_f32 v3, v5, v5\n"
      "v_lshlrev_b32 v6, 16, v247\n"
      "v_and_b32 v7, s71, v247\n"
      "v_fmac_f32 v2, v6, v6\n"
      "v_fmac_f32 v3, v7, v7\n"
      "v_mfma_f32_16x16x32_bf16 v[32:35], v[92:95], v[244:247], v[32:35]\n"
      "s_barrier\n"
      "s_waitcnt lgkmcnt(3)\n"
      "s_add_u32 s72, %[rot], 3\n"
      "s_and_b32 s72, s72, 7\n"
      "s_mul_i32 s70, s72, 176128\n"
      "s_add_u32 m0, %[sg], 28672\n"
      "v_mfma_f32_32x32x16_bf16 v[16:31], v[48:51], v[248:251], v[16:31]\n"
      "buffer_load_dwordx4 v88, %[srdg], s70 offen lds\n"
      "v_mfma_f32_16x16x32_bf16 v[32:35], v[92:95], v[248:251], v[32:35]\n"
      "s_waitcnt lgkmcnt(0)\n"
      "s_add_u32 m0, %[sg], 29696\n"
      "v_mfma_f32_32x32x16_bf16 v[16:31], v[56:59], v[252:255], v[16:31]\n"
      "buffer_load_dwordx4 v89, %[srdg], s70 offen lds\n"
      "s_add_u32 m0, %[sg], 30720\n"
      "s_nop 0\n"
      "buffer_load_dwordx4 v90, %[srdg], s70 offen lds\n"
      "v_lshlrev_b32 v4, 16, v252\n"
      "v_and_b32 v5, s71, v252\n"
      "v_fmac_f32 v2, v4, v4\n"
      "v_fmac_f32 v3, v5, v5\n"
      "v_lshlrev_b32 v6, 16, v253\n"
      "v_and_b32 v7, s71, v253\n"
      "v_fmac_f32 v2, v6, v6\n"
      "v_fmac_f32 v3, v7, v7\n"
      "v_lshlrev_b32 v4, 16, v254\n"
      "v_and_b32 v5, s71, v254\n"
      "v_fmac_f32 v2, v4, v4\n"
      "v_fmac_f32 v3, v5, v5\n"
      "v_lshlrev_b32 v6, 16, v255\n"
      "v_and_b32 v7, s71, v255\n"
      "v_fmac_f32 v2, v6, v6\n"
      "v_fmac_f32 v3, v7, v7\n"
      "v_mfma_f32_16x16x32_bf16 v[32:35], v[92:95], v[252:255], v[32:35]\n"
      "v_add_f32 v2, v2, v3\n"
      "s_lshl_b32 s72, %[ti], 2\n"
      "v_mov_b32 v4, v2\n"
      "v_add_u32 v5, s72, v105\n"
      "s_mul_i32 s68, %[ji], 393216\n"
      "v_permlane32_swap_b32 v4, v2\n"
      "s_lshl_b32 s69, %[gi], 12\n"
      "s_add_u32 s68, s68, s69\n"
      "v_add_f32 v2, v2, v4\n"
      "s_lshl_b32 s69, %[ti], 10\n"
      "s_add_u32 s68, s68, s69\n"
      "s_nop 1\n"
      "ds_write_b32 v5, v120 offset:0\n"
      "ds_write_b32 v106, v2 offset:128\n"
      "ds_write_b32 v106, v32 offset:256\n"
      "v_readfirstlane_b32 %[ep0], v116\n"
      "v_readfirstlane_b32 %[ep1], v117\n"
      "s_add_u32 s68, s68, 1310720\n"
      "s_setprio 1\n"
      "s_waitcnt vmcnt(6) lgkmcnt(0)\n"
      "s_barrier\n"
      "ds_read_b32 v4, v106 offset:256\n"
      "ds_read_b32 v5, v106\n"
      "ds_read_b32 v6, v106 offset:128\n"
      "ds_read_b128 v[48:51], v105 offset:0\n"
      "s_add_u32 s72, %[rot], 4\n"
      "s_and_b32 s72, s72, 7\n"
      "s_mul_i32 s70, s72, 176128\n"
      "s_add_u32 m0, %[sg], 57344\n"
      "s_nop 0\n"
      "buffer_load_dwordx4 v88, %[srdg], s70 offen lds\n"
      "s_add_u32 m0, %[sg], 58368\n"
      "s_nop 0\n"
      "buffer_load_dwordx4 v89, %[srdg], s70 offen lds\n"
      "s_add_u32 m0, %[sg], 59392\n"
      "s_nop 0\n"
      "buffer_load_dwordx4 v90, %[srdg], s70 offen lds\n"
      "v_mov_b32 v122, 0\n"
      "v_mov_b32 v123, 0\n"
      "v_mov_b32 v58, 0\n"
      "v_mov_b32 v59, 0\n"
      "s_mov_b32 s70, 0x3b000000\n"
      "s_waitcnt lgkmcnt(1)\n"
      "v_mul_f32 v4, 0x3b000000, v4\n"
      "v_add_f32 v5, v5, v6\n"
      "v_mul_f32 v6, v4, v4\n"
      "v_mul_f32 v4, -0.5, v4\n"
      "v_fma_f32 v5, v5, s70, -v6\n"
      "v_cvt_pk_bf16_f32 v7, v4, v4\n"
      "v_max_f32 v5, 0, v5\n"
      "v_and_b32 v7, s71, v7\n"
      "v_add_f32 v5, 0x3727c5ac, v5\n"
      "v_sub_f32 v7, v4, v7\n"
      "v_rsq_f32 v124, v5\n"
      "v_cvt_pk_bf16_f32 v120, v4, v7\n"
      "s_waitcnt lgkmcnt(0)\n"
      "v_mov_b32 v121, v120\n"
      "v_add_f32 v48, v48, v49\n"
      "v_add_f32 v50, v50, v51\n"
      "v_add_f32 v48, v48, v50\n"
      "v_cvt_pk_bf16_f32 v56, v48, v48\n"
      "v_and_b32 v49, s71, v56\n"
      "v_sub_f32 v48, v48, v49\n"
      "v_cvt_pk_bf16_f32 v57, v48, v48\n"
      "s_nop 1\n"
      "v_mfma_f32_32x32x16_bf16 v[16:31], v[56:59], v[120:123], v[16:31]\n"
      "s_cmp_lt_u32 %[ji], 5\n"
      "s_cselect_b64 vcc, -1, 0\n"
      "s_cselect_b32 s72, 0x3eb8aa3b, 1.0\n"
      "v_cndmask_b32 v4, v108, v104, vcc\n"
      "v_mul_f32 v6, s72, v124\n"
      "v_add_u32 v4, s68, v4\n"
      "s_nop 7\n"
      "v_mul_f32 v16, v16, v6\n"
      "v_mul_f32 v17, v17, v6\n"
      "v_mul_f32 v18, v18, v6\n"
      "v_mul_f32 v19, v19, v6\n"
      "v_mul_f32 v20, v20, v6\n"
      "v_mul_f32 v21, v21, v6\n"
      "v_mul_f32 v22, v22, v6\n"
      "v_mul_f32 v23, v23, v6\n"
      "v_cvt_pk_bf16_f32 v64, v16, v17\n"
      "v_cvt_pk_bf16_f32 v65, v18, v19\n"
      "v_cvt_pk_bf16_f32 v66, v20, v21\n"
      "v_cvt_pk_bf16_f32 v67, v22, v23\n"
      "buffer_store_dwordx4 v[64:67], v4, %[srd], 0 offen sc1\n"
      "v_mul_f32 v24, v24, v6\n"
      "v_mul_f32 v25, v25, v6\n"
      "v_mul_f32 v26, v26, v6\n"
      "v_mul_f32 v27, v27, v6\n"
      "v_mul_f32 v28, v28, v6\n"
      "v_mul_f32 v29, v29, v6\n"
      "v_mul_f32 v30, v30, v6\n"
      "v_mul_f32 v31, v31, v6\n"
      "v_cvt_pk_bf16_f32 v68, v24, v25\n"
      "v_cvt_pk_bf16_f32 v69, v26, v27\n"
      "v_cvt_pk_bf16_f32 v70, v28, v29\n"
      "v_cvt_pk_bf16_f32 v71, v30, v31\n"
      "v_add_u32 v4, 65536, v4\n"
      "buffer_store_dwordx4 v[68:71], v4, %[srd], 0 offen sc1\n"
      "v_mov_b32 v112, -1\n"
      "v_mov_b32 v113, -1\n"
      "v_mov_b32 v114, -1\n"
      "v_mov_b32 v115, -1\n"
      "buffer_store_dwordx4 v[112:115], %[vsn], %[srd], 0 offen sc1\n"
      "v_bfe_u32 v6, v104, 8, 1\n"
      "v_mul_u32_u24 v6, 12288, v6\n"
      "v_add_u32 v6, 69600, v6\n"
      "v_add_u32 v12, v12, v6\n"
      "v_mov_b32 v8, v88\n"
      "v_mov_b32 v28, v92\n"
      "v_add_u32 v13, v13, v6\n"
      "v_mov_b32 v9, v89\n"
      "v_mov_b32 v29, v92\n"
      "v_add_u32 v14, v14, v6\n"
      "v_mov_b32 v10, v90\n"
      "v_mov_b32 v30, v92\n"
      "v_add_u32 v15, v15, v6\n"
      "v_mov_b32 v11, v91\n"
      "v_mov_b32 v31, v92\n"
      "s_branch 49f\n"
      "43:\n"
      "v_mov_b32 v2, 0\n"
      "v_mov_b32 v3, 0\n"
      "s_mov_b32 s71, 0xffff0000\n"
      "v_mov_b32 v116, 0\n"
      "buffer_load_dwordx2 v[116:117], v116, %[srd], 0 offen sc1\n"
      "s_movk_i32 s73, 2560\n"
      "v_mov_b32 v104, 163776\n"
      "ds_write_b64 v104, v[2:3]\n"
      "v_add_u32 v72, 57344, v8\n"
      "v_add_u32 v73, 57344, v9\n"
      "v_add_u32 v74, 57344, v10\n"
      "v_add_u32 v75, 57344, v11\n"
      "v_add_u32 v76, 57344, v12\n"
      "v_add_u32 v77, 57344, v13\n"
      "v_add_u32 v78, 57344, v14\n"
      "v_add_u32 v79, 57344, v15\n"
      "v_add_u32 v80, 114688, v8\n"
      "v_add_u32 v81, 114688, v9\n"
      "v_add_u32 v82, 114688, v10\n"
      "v_add_u32 v83, 114688, v11\n"
      "v_add_u32 v84, 114688, v12\n"
      "v_add_u32 v85, 114688, v13\n"
      "v_add_u32 v86, 114688, v14\n"
      "v_add_u32 v87, 114688, v15\n"
      "v_mbcnt_lo_u32_b32 v91, -1, 0\n"
      "v_mbcnt_hi_u32_b32 v91, -1, v91\n"
      "v_add3_u32 v91, v91, %[n0], 0\n"
      "v_mul_u32_u24 v88, 0xaaab, v91\n"
      "v_lshrrev_b32 v88, 19, v88\n"
      "v_lshlrev_b32 v91, 4, v91\n"
      "v_mad_u32_u24 v88, v88, s73, v91\n"
      "v_mbcnt_lo_u32_b32 v91, -1, 0\n"
      "v_mbcnt_hi_u32_b32 v91, -1, v91\n"
      "v_add3_u32 v91, v91, %[n0], 64\n"
      "v_mul_u32_u24 v89, 0xaaab, v91\n"
      "v_lshrrev_b32 v89, 19, v89\n"
      "v_lshlrev_b32 v91, 4, v91\n"
      "v_mad_u32_u24 v89, v89, s73, v91\n"
      "v_mbcnt_lo_u32_b32 v91, -1, 0\n"
      "v_mbcnt_hi_u32_b32 v91, -1, v91\n"
      "v_add3_u32 v91, v91, %[n0], 64\n"
      "v_add_u32 v91, 64, v91\n"
      "v_mul_u32_u24 v90, 0xaaab, v91\n"
      "v_lshrrev_b32 v90, 19, v90\n"
      "v_lshlrev_b32 v91, 4, v91\n"
      "v_mad_u32_u24 v90, v90, s73, v91\n"
      "v_mbcnt_lo_u32_b32 v104, -1, 0\n"
      "v_mbcnt_hi_u32_b32 v104, -1, v104\n"
      "v_lshlrev_b32 v104, 4, v104\n"
      "v_and_b32 v105, 0x1f0, v104\n"
      "v_lshrrev_b32 v106, 2, v105\n"
      "v_add_u32 v105, 144384, v105\n"
      "v_lshl_add_u32 v106, %[n0], 1, v106\n"
      "v_add_u32 v106, 144896, v106\n"
      "v_and_b32 v107, 0xf0, v104\n"
      "v_lshrrev_b32 v108, 9, v104\n"
      "v_lshl_or_b32 v107, v108, 8, v107\n"
      "v_bfe_u32 v108, v104, 8, 1\n"
      "v_lshl_or_b32 v107, v108, 16, v107\n"
      "v_lshrrev_b32 v108, 5, v104\n"
      "v_and_b32 v108, 16, v108\n"
      "s_add_u32 s73, %[ji], 16\n"
      "s_mul_i32 s73, s73, 86\n"
      "s_lshr_b32 s73, s73, 8\n"
      "v_mov_b32 v109, s73\n"
      "v_mov_b32 v91, s73\n"
      "s_add_u32 s73, %[ji], 32\n"
      "s_mul_i32 s73, s73, 86\n"
      "s_lshr_b32 s73, s73, 8\n"
      "v_mov_b32 v110, s73\n"
      "v_cmp_lt_u32 vcc, 63, v104\n"
      "s_mul_i32 s73, %[ji], 86\n"
      "s_lshr_b32 s73, s73, 8\n"
      "v_cndmask_b32 v109, v109, v110, vcc\n"
      "v_mov_b32 v110, s73\n"
      "v_cmp_lt_u32 vcc, 255, v104\n"
      "s_lshl_b32 s73, %[ti], 6\n"
      "v_add_u32 v109, s73, v109\n"
      "v_cndmask_b32 v91, v91, v110, vcc\n"
      "v_and_b32 v110, 0x30, v104\n"
      "s_lshl_b32 s73, %[gi], 4\n"
      "v_add_u32 v109, v109, v110\n"
      "v_cmp_lt_u32 vcc, 127, v104\n"
      "v_add_u32 v91, s73, v91\n"
      "s_nop 0\n"
      "v_cndmask_b32 v91, v109, v91, vcc\n"
      "v_lshlrev_b32 v91, 7, v91\n"
      "v_add_u32 v91, 4096, v91\n"
      "s_waitcnt vmcnt(5) lgkmcnt(0)\n"
      "s_barrier\n"
      "ds_read_b128 v[128:131], v8 offset:0\n"
      "ds_read_b64_tr_b16 v[48:49], v12 offset:0\n"
      "ds_read_b64_tr_b16 v[50:51], v12 offset:768\n"
      "ds_read_b128 v[132:135], v9 offset:0\n"
      "ds_read_b64_tr_b16 v[56:57], v13 offset:0\n"
      "ds_read_b64_tr_b16 v[58:59], v13 offset:768\n"
      "s_waitcnt lgkmcnt(3)\n"
      "s_add_u32 s72, %[rot], 2\n"
      "s_and_b32 s72, s72, 7\n"
      "s_lshl_b32 s68, s72, 7\n"
      "s_mul_i32 s69, s72, 196608\n"
      "s_add_u32 m0, %[sx], 57344\n"
      "v_mfma_f32_32x32x16_bf16 v[16:31], v[128:131], v[48:51], 0\n"
      "buffer_load_dwordx4 %[vx0], %[srdx], s68 offen lds\n"
      "ds_read_b128 v[136:139], v10 offset:0\n"
      "ds_read_b64_tr_b16 v[64:65], v14 offset:0\n"
      "ds_read_b64_tr_b16 v[66:67], v14 offset:768\n"
      "s_add_u32 m0, %[sx], 58368\n"
      "s_nop 0\n"
      "buffer_load_dwordx4 %[vx1], %[srdx], s68 offen lds\n"
      "s_add_u32 m0, %[sq], 57344\n"
      "s_nop 0\n"
      "buffer_load_dwordx4 %[vq0], %[srdq], s69 offen lds\n"
      "s_add_u32 m0, %[sq], 61440\n"
      "s_nop 0\n"
      "buffer_load_dwordx4 %[vq1], %[srdq], s69 offen lds\n"
      "v_mfma_f32_16x16x32_bf16 v[120:123], v[92:95], v[48:51], 0\n"
      "v_mfma_f32_16x16x32_bf16 v[32:35], v[92:95], v[128:131], 0\n"
      "s_waitcnt lgkmcnt(3)\n"
      "s_add_u32 s72, %[rot], 3\n"
      "s_and_b32 s72, s72, 7\n"
      "s_lshl_b32 s68, s72, 7\n"
      "s_mul_i32 s69, s72, 196608\n"
      "s_add_u32 m0, %[sx], 86016\n"
      "v_mfma_f32_32x32x16_bf16 v[16:31], v[132:135], v[56:59], v[16:31]\n"
      "buffer_load_dwordx4 %[vx0], %[srdx], s68 offen lds\n"
      "ds_read_b128 v[140:143], v11 offset:0\n"
      "ds_read_b64_tr_b16 v[48:49], v15 offset:0\n"
      "ds_read_b64_tr_b16 v[50:51], v15 offset:768\n"
      "s_add_u32 m0, %[sx], 87040\n"
      "s_nop 0\n"
      "buffer_load_dwordx4 %[vx1], %[srdx], s68 offen lds\n"
      "s_add_u32 m0, %[sq], 86016\n"
      "s_nop 0\n"
      "buffer_load_dwordx4 %[vq0], %[srdq], s69 offen lds\n"
      "s_add_u32 m0, %[sq], 90112\n"
      "s_nop 0\n"
      "buffer_load_dwordx4 %[vq1], %[srdq], s69 offen lds\n"
      "v_lshlrev_b32 v4, 16, v132\n"
      "v_and_b32 v5, s71, v132\n"
      "v_fmac_f32 v2, v4, v4\n"
      "v_fmac_f32 v3, v5, v5\n"
      "v_lshlrev_b32 v6, 16, v133\n"
      "v_and_b32 v7, s71, v133\n"
      "v_fmac_f32 v2, v6, v6\n"
      "v_fmac_f32 v3, v7, v7\n"
      "v_lshlrev_b32 v4, 16, v134\n"
      "v_and_b32 v5, s71, v134\n"
      "v_fmac_f32 v2, v4, v4\n"
      "v_fmac_f32 v3, v5, v5\n"
      "v_lshlrev_b32 v6, 16, v135\n"
      "v_and_b32 v7, s71, v135\n"
      "v_fmac_f32 v2, v6, v6\n"
      "v_fmac_f32 v3, v7, v7\n"
      "v_mfma_f32_16x16x32_bf16 v[32:35], v[92:95], v[132:135], v[32:35]\n"
      "s_waitcnt vmcnt(9)\n"
      "s_barrier\n"
      "s_waitcnt lgkmcnt(3)\n"
      "s_add_u32 s72, %[rot], 4\n"
      "s_and_b32 s72, s72, 7\n"
      "s_lshl_b32 s68, s72, 7\n"
      "s_mul_i32 s69, s72, 196608\n"
      "s_add_u32 m0, %[sx], 114688\n"
      "v_mfma_f32_32x32x16_bf16 v[16:31], v[136:139], v[64:67], v[16:31]\n"
      "buffer_load_dwordx4 %[vx0], %[srdx], s68 offen lds\n"
      "ds_read_b128 v[144:147], v8 offset:28672\n"
      "ds_read_b64_tr_b16 v[56:57], v12 offset:28672\n"
      "ds_read_b64_tr_b16 v[58:59], v12 offset:29440\n"
      "v_mfma_f32_16x16x32_bf16 v[32:35], v[92:95], v[136:139], v[32:35]\n"
      "s_waitcnt lgkmcnt(3)\n"
      "s_add_u32 m0, %[sx], 115712\n"
      "v_mfma_f32_32x32x16_bf16 v[16:31], v[140:143], v[48:51], v[16:31]\n"
      "buffer_load_dwordx4 %[vx1], %[srdx], s68 offen lds\n"
      "ds_read_b128 v[148:151], v9 offset:28672\n"
      "ds_read_b64_tr_b16 v[64:65], v13 offset:28672\n"
      "ds_read_b64_tr_b16 v[66:67], v13 offset:29440\n"
      "v_lshlrev_b32 v4, 16, v140\n"
      "v_and_b32 v5, s71, v140\n"
      "v_fmac_f32 v2, v4, v4\n"
      "v_fmac_f32 v3, v5, v5\n"
      "v_lshlrev_b32 v6, 16, v141\n"
      "v_and_b32 v7, s71, v141\n"
      "v_fmac_f32 v2, v6, v6\n"
      "v_fmac_f32 v3, v7, v7\n"
      "v_lshlrev_b32 v4, 16, v142\n"
      "v_and_b32 v5, s71, v142\n"
      "v_fmac_f32 v2, v4, v4\n"
      "v_fmac_f32 v3, v5, v5\n"
      "v_lshlrev_b32 v6, 16, v143\n"
      "v_and_b32 v7, s71, v143\n"
      "v_fmac_f32 v2, v6, v6\n"
      "v_fmac_f32 v3, v7, v7\n"
      "v_mfma_f32_16x16x32_bf16 v[32:35], v[92:95], v[140:143], v[32:35]\n"
      "s_waitcnt lgkmcnt(3)\n"
      "s_add_u32 m0, %[sq], 114688\n"
      "v_mfma_f32_32x32x16_bf16 v[16:31], v[144:147], v[56:59], v[16:31]\n"
      "buffer_load_dwordx4 %[vq0], %[srdq], s69 offen lds\n"
      "ds_read_b128 v[152:155], v10 offset:28672\n"
      "ds_read_b64_tr_b16 v[48:49], v14 offset:28672\n"
      "ds_read_b64_tr_b16 v[50:51], v14 offset:29440\n"
      "v_mfma_f32_16x16x32_bf16 v[120:123], v[92:95], v[56:59], v[120:123]\n"
      "v_mfma_f32_16x16x32_bf16 v[32:35], v[92:95], v[144:147], v[32:35]\n"
      "s_waitcnt lgkmcnt(3)\n"
      "s_add_u32 m0, %[sq], 118784\n"
      "v_mfma_f32_32x32x16_bf16 v[16:31], v[148:151], v[64:67], v[16:31]\n"
      "buffer_load_dwordx4 %[vq1], %[srdq], s69 offen lds\n"
      "ds_read_b128 v[156:159], v11 offset:28672\n"
      "ds_read_b64_tr_b16 v[56:57], v15 offset:28672\n"
      "ds_read_b64_tr_b16 v[58:59], v15 offset:29440\n"
      "v_lshlrev_b32 v4, 16, v148\n"
      "v_and_b32 v5, s71, v148\n"
      "v_fmac_f32 v2, v4, v4\n"
      "v_fmac_f32 v3, v5, v5\n"
      "v_lshlrev_b32 v6, 16, v149\n"
      "v_and_b32 v7, s71, v149\n"
      "v_fmac_f32 v2, v6, v6\n"
      "v_fmac_f32 v3, v7, v7\n"
      "v_lshlrev_b32 v4, 16, v150\n"
      "v_and_b32 v5, s71, v150\n"
      "v_fmac_f32 v2, v4, v4\n"
      "v_fmac_f32 v3, v5, v5\n"
      "v_lshlrev_b32 v6, 16, v151\n"
      "v_and_b32 v7, s71, v151\n"
      "v_fmac_f32 v2, v6, v6\n"
      "v_fmac_f32 v3, v7, v7\n"
      "v_mfma_f32_16x16x32_bf16 v[32:35], v[92:95], v[148:151], v[32:35]\n"
      "s_waitcnt vmcnt(8)\n"
      "s_barrier\n"
      "s_waitcnt lgkmcnt(3)\n"
      "s_add_u32 s72, %[rot], 5\n"
      "s_and_b32 s72, s72, 7\n"
      "s_lshl_b32 s68, s72, 7\n"
      "s_mul_i32 s69, s72, 196608\n"
      "s_add_u32 m0, %[sx], 0\n"
      "v_mfma_f32_32x32x16_bf16 v[16:31], v[152:155], v[48:51], v[16:31]\n"
      "buffer_load_dwordx4 %[vx0], %[srdx], s68 offen lds\n"
      "ds_read_b128 v[160:163], v72 offset:0\n"
      "ds_read_b64_tr_b16 v[64:65], v76 offset:0\n"
      "ds_read_b64_tr_b16 v[66:67], v76 offset:768\n"
      "v_mfma_f32_16x16x32_bf16 v[32:35], v[92:95], v[152:155], v[32:35]\n"
      "s_waitcnt lgkmcnt(3)\n"
      "s_add_u32 m0, %[sx], 1024\n"
      "v_mfma_f32_32x32x16_bf16 v[16:31], v[156:159], v[56:59], v[16:31]\n"
      "buffer_load_dwordx4 %[vx1], %[srdx], s68 offen lds\n"
      "ds_read_b128 v[164:167], v73 offset:0\n"
      "ds_read_b64_tr_b16 v[48:49], v77 offset:0\n"
      "ds_read_b64_tr_b16 v[50:51], v77 offset:768\n"
      "v_lshlrev_b32 v4, 16, v156\n"
      "v_and_b32 v5, s71, v156\n"
      "v_fmac_f32 v2, v4, v4\n"
      "v_fmac_f32 v3, v5, v5\n"
      "v_lshlrev_b32 v6, 16, v157\n"
      "v_and_b32 v7, s71, v157\n"
      "v_fmac_f32 v2, v6, v6\n"
      "v_fmac_f32 v3, v7, v7\n"
      "v_lshlrev_b32 v4, 16, v158\n"
      "v_and_b32 v5, s71, v158\n"
      "v_fmac_f32 v2, v4, v4\n"
      "v_fmac_f32 v3, v5, v5\n"
      "v_lshlrev_b32 v6, 16, v159\n"
      "v_and_b32 v7, s71, v159\n"
      "v_fmac_f32 v2, v6, v6\n"
      "v_fmac_f32 v3, v7, v7\n"
      "v_mfma_f32_16x16x32_bf16 v[32:35], v[92:95], v[156:159], v[32:35]\n"
      "s_waitcnt lgkmcnt(3)\n"
      "s_add_u32 m0, %[sq], 0\n"
      "v_mfma_f32_32x32x16_bf16 v[16:31], v[160:163], v[64:67], v[16:31]\n"
      "buffer_load_dwordx4 %[vq0], %[srdq], s69 offen lds\n"
      "ds_read_b128 v[168:171], v74 offset:0\n"
      "ds_read_b64_tr_b16 v[56:57], v78 offset:0\n"
      "ds_read_b64_tr_b16 v[58:59], v78 offset:768\n"
      "v_mfma_f32_16x16x32_bf16 v[120:123], v[92:95], v[64:67], v[120:123]\n"
      "v_mfma_f32_16x16x32_bf16 v[32:35], v[92:95], v[160:163], v[32:35]\n"
      "s_waitcnt lgkmcnt(3)\n"
      "s_add_u32 m0, %[sq], 4096\n"
      "v_mfma_f32_32x32x16_bf16 v[16:31], v[164:167], v[48:51], v[16:31]\n"
      "buffer_load_dwordx4 %[vq1], %[srdq], s69 offen lds\n"
      "ds_read_b128 v[172:175], v75 offset:0\n"
      "ds_read_b64_tr_b16 v[64:65], v79 offset:0\n"
      "ds_read_b64_tr_b16 v[66:67], v79 offset:768\n"
      "v_lshlrev_b32 v4, 16, v164\n"
      "v_and_b32 v5, s71, v164\n"
      "v_fmac_f32 v2, v4, v4\n"
      "v_fmac_f32 v3, v5, v5\n"
      "v_lshlrev_b32 v6, 16, v165\n"
      "v_and_b32 v7, s71, v165\n"
      "v_fmac_f32 v2, v6, v6\n"
      "v_fmac_f32 v3, v7, v7\n"
      "v_lshlrev_b32 v4, 16, v166\n"
      "v_and_b32 v5, s71, v166\n"
      "v_fmac_f32 v2, v4, v4\n"
      "v_fmac_f32 v3, v5, v5\n"
      "v_lshlrev_b32 v6, 16, v167\n"
      "v_and_b32 v7, s71, v167\n"
      "v_fmac_f32 v2, v6, v6\n"
      "v_fmac_f32 v3, v7, v7\n"
      "v_mfma_f32_16x16x32_bf16 v[32:35], v[92:95], v[164:167], v[32:35]\n"
      "s_waitcnt vmcnt(8)\n"
      "s_barrier\n"
      "s_waitcnt lgkmcnt(3)\n"
      "s_add_u32 s72, %[rot], 6\n"
      "s_and_b32 s72, s72, 7\n"
      "s_lshl_b32 s68, s72, 7\n"
      "s_mul_i32 s69, s72, 196608\n"
      "s_add_u32 m0, %[sx], 28672\n"
      "v_mfma_f32_32x32x16_bf16 v[16:31], v[168:171], v[56:59], v[16:31]\n"
      "buffer_load_dwordx4 %[vx0], %[srdx], s68 offen lds\n"
      "ds_read_b128 v[176:179], v72 offset:28672\n"
      "ds_read_b64_tr_b16 v[48:49], v76 offset:28672\n"
      "ds_read_b64_tr_b16 v[50:51], v76 offset:29440\n"
      "v_mfma_f32_16x16x32_bf16 v[32:35], v[92:95], v[168:171], v[32:35]\n"
      "s_waitcnt lgkmcnt(3)\n"
      "s_add_u32 m0, %[sx], 29696\n"
      "v_mfma_f32_32x32x16_bf16 v[16:31], v[172:175], v[64:67], v[16:31]\n"
      "buffer_load_dwordx4 %[vx1], %[srdx], s68 offen lds\n"
      "ds_read_b128 v[180:183], v73 offset:28672\n"
      "ds_read_b64_tr_b16 v[56:57], v77 offset:28672\n"
      "ds_read_b64_tr_b16 v[58:59], v77 offset:29440\n"
      "v_lshlrev_b32 v4, 16, v172\n"
      "v_and_b32 v5, s71, v172\n"
      "v_fmac_f32 v2, v4, v4\n"
      "v_fmac_f32 v3, v5, v5\n"
      "v_lshlrev_b32 v6, 16, v173\n"
      "v_and_b32 v7, s71, v173\n"
      "v_fmac_f32 v2, v6, v6\n"
      "v_fmac_f32 v3, v7, v7\n"
      "v_lshlrev_b32 v4, 16, v174\n"
      "v_and_b32 v5, s71, v174\n"
      "v_fmac_f32 v2, v4, v4\n"
      "v_fmac_f32 v3, v5, v5\n"
      "v_lshlrev_b32 v6, 16, v175\n"
      "v_and_b32 v7, s71, v175\n"
      "v_fmac_f32 v2, v6, v6\n"
      "v_fmac_f32 v3, v7, v7\n"
      "v_mfma_f32_16x16x32_bf16 v[32:35], v[92:95], v[172:175], v[32:35]\n"
      "s_waitcnt lgkmcnt(3)\n"
      "s_add_u32 m0, %[sq], 28672\n"
      "v_mfma_f32_32x32x16_bf16 v[16:31], v[176:179], v[48:51], v[16:31]\n"
      "buffer_load_dwordx4 %[vq0], %[srdq], s69 offen lds\n"
      "ds_read_b128 v[184:187], v74 offset:28672\n"
      "ds_read_b64_tr_b16 v[64:65], v78 offset:28672\n"
      "ds_read_b64_tr_b16 v[66:67], v78 offset:29440\n"
      "v_mfma_f32_16x16x32_bf16 v[120:123], v[92:95], v[48:51], v[120:123]\n"
      "v_mfma_f32_16x16x32_bf16 v[32:35], v[92:95], v[176:179], v[32:35]\n"
      "s_waitcnt lgkmcnt(3)\n"
      "s_add_u32 m0, %[sq], 32768\n"
      "v_mfma_f32_32x32x16_bf16 v[16:31], v[180:183], v[56:59], v[16:31]\n"
      "buffer_load_dwordx4 %[vq1], %[srdq], s69 offen lds\n"
      "ds_read_b128 v[188:191], v75 offset:28672\n"
      "ds_read_b64_tr_b16 v[48:49], v79 offset:28672\n"
      "ds_read_b64_tr_b16 v[50:51], v79 offset:29440\n"
      "v_lshlrev_b32 v4, 16, v180\n"
      "v_and_b32 v5, s71, v180\n"
      "v_fmac_f32 v2, v4, v4\n"
      "v_fmac_f32 v3, v5, v5\n"
      "v_lshlrev_b32 v6, 16, v181\n"
      "v_and_b32 v7, s71, v181\n"
      "v_fmac_f32 v2, v6, v6\n"
      "v_fmac_f32 v3, v7, v7\n"
      "v_lshlrev_b32 v4, 16, v182\n"
      "v_and_b32 v5, s71, v182\n"
      "v_fmac_f32 v2, v4, v4\n"
      "v_fmac_f32 v3, v5, v5\n"
      "v_lshlrev_b32 v6, 16, v183\n"
      "v_and_b32 v7, s71, v183\n"
      "v_fmac_f32 v2, v6, v6\n"
      "v_fmac_f32 v3, v7, v7\n"
      "v_mfma_f32_16x16x32_bf16 v[32:35], v[92:95], v[180:183], v[32:35]\n"
      "s_waitcnt vmcnt(8)\n"
      "s_barrier\n"
      "s_waitcnt lgkmcnt(3)\n"
      "s_add_u32 s72, %[rot], 7\n"
      "s_and_b32 s72, s72, 7\n"
      "s_lshl_b32 s68, s72, 7\n"
      "s_mul_i32 s69, s72, 196608\n"
      "s_add_u32 m0, %[sx], 57344\n"
      "v_mfma_f32_32x32x16_bf16 v[16:31], v[184:187], v[64:67], v[16:31]\n"
      "buffer_load_dwordx4 %[vx0], %[srdx], s68 offen lds\n"
      "ds_read_b128 v[192:195], v80 offset:0\n"
      "ds_read_b64_tr_b16 v[56:57], v84 offset:0\n"
      "ds_read_b64_tr_b16 v[58:59], v84 offset:768\n"
      "v_mfma_f32_16x16x32_bf16 v[32:35], v[92:95], v[184:187], v[32:35]\n"
      "s_waitcnt lgkmcnt(3)\n"
      "s_add_u32 m0, %[sx], 58368\n"
      "v_mfma_f32_32x32x16_bf16 v[16:31], v[188:191], v[48:51], v[16:31]\n"
      "buffer_load_dwordx4 %[vx1], %[srdx], s68 offen lds\n"
      "ds_read_b128 v[196:199], v81 offset:0\n"
      "ds_read_b64_tr_b16 v[64:65], v85 offset:0\n"
      "ds_read_b64_tr_b16 v[66:67], v85 offset:768\n"
      "v_lshlrev_b32 v4, 16, v188\n"
      "v_and_b32 v5, s71, v188\n"
      "v_fmac_f32 v2, v4, v4\n"
      "v_fmac_f32 v3, v5, v5\n"
      "v_lshlrev_b32 v6, 16, v189\n"
      "v_and_b32 v7, s71, v189\n"
      "v_fmac_f32 v2, v6, v6\n"
      "v_fmac_f32 v3, v7, v7\n"
      "v_lshlrev_b32 v4, 16, v190\n"
      "v_and_b32 v5, s71, v190\n"
      "v_fmac_f32 v2, v4, v4\n"
      "v_fmac_f32 v3, v5, v5\n"
      "v_lshlrev_b32 v6, 16, v191\n"
      "v_and_b32 v7, s71, v191\n"
      "v_fmac_f32 v2, v6, v6\n"
      "v_fmac_f32 v3, v7, v7\n"
      "v_mfma_f32_16x16x32_bf16 v[32:35], v[92:95], v[188:191], v[32:35]\n"
      "s_waitcnt lgkmcnt(3)\n"
      "s_add_u32 m0, %[sq], 57344\n"
      "v_mfma_f32_32x32x16_bf16 v[16:31], v[192:195], v[56:59], v[16:31]\n"
      "buffer_load_dwordx4 %[vq0], %[srdq], s69 offen lds\n"
      "ds_read_b128 v[200:203], v82 offset:0\n"
      "ds_read_b64_tr_b16 v[48:49], v86 offset:0\n"
      "ds_read_b64_tr_b16 v[50:51], v86 offset:768\n"
      "v_mfma_f32_16x16x32_bf16 v[120:123], v[92:95], v[56:59], v[120:123]\n"
      "v_mfma_f32_16x16x32_bf16 v[32:35], v[92:95], v[192:195], v[32:35]\n"
      "s_waitcnt lgkmcnt(3)\n"
      "s_add_u32 m0, %[sq], 61440\n"
      "v_mfma_f32_32x32x16_bf16 v[16:31], v[196:199], v[64:67], v[16:31]\n"
      "buffer_load_dwordx4 %[vq1], %[srdq], s69 offen lds\n"
      "ds_read_b128 v[204:207], v83 offset:0\n"
      "ds_read_b64_tr_b16 v[56:57], v87 offset:0\n"
      "ds_read_b64_tr_b16 v[58:59], v87 offset:768\n"
      "v_lshlrev_b32 v4, 16, v196\n"
      "v_and_b32 v5, s71, v196\n"
      "v_fmac_f32 v2, v4, v4\n"
      "v_fmac_f32 v3, v5, v5\n"
      "v_lshlrev_b32 v6, 16, v197\n"
      "v_and_b32 v7, s71, v197\n"
      "v_fmac_f32 v2, v6, v6\n"
      "v_fmac_f32 v3, v7, v7\n"
      "v_lshlrev_b32 v4, 16, v198\n"
      "v_and_b32 v5, s71, v198\n"
      "v_fmac_f32 v2, v4, v4\n"
      "v_fmac_f32 v3, v5, v5\n"
      "v_lshlrev_b32 v6, 16, v199\n"
      "v_and_b32 v7, s71, v199\n"
      "v_fmac_f32 v2, v6, v6\n"
      "v_fmac_f32 v3, v7, v7\n"
      "v_mfma_f32_16x16x32_bf16 v[32:35], v[92:95], v[196:199], v[32:35]\n"
      "s_waitcnt vmcnt(8)\n"
      "s_barrier\n"
      "s_waitcnt lgkmcnt(3)\n"
      "s_add_u32 s72, %[rot], 0\n"
      "s_and_b32 s72, s72, 7\n"
      "s_mul_i32 s70, s72, 176128\n"
      "s_add_u32 m0, %[sg], 86016\n"
      "v_mfma_f32_32x32x16_bf16 v[16:31], v[200:203], v[48:51], v[16:31]\n"
      "buffer_load_dwordx4 v88, %[srdg], s70 offen lds\n"
      "ds_read_b128 v[208:211], v8 offset:0\n"
      "ds_read_b64_tr_b16 v[64:65], v12 offset:0\n"
      "ds_read_b64_tr_b16 v[66:67], v12 offset:768\n"
      "v_mfma_f32_16x16x32_bf16 v[32:35], v[92:95], v[200:203], v[32:35]\n"
      "s_waitcnt lgkmcnt(3)\n"
      "s_add_u32 m0, %[sg], 87040\n"
      "v_mfma_f32_32x32x16_bf16 v[16:31], v[204:207], v[56:59], v[16:31]\n"
      "buffer_load_dwordx4 v89, %[srdg], s70 offen lds\n"
      "ds_read_b128 v[212:215], v9 offset:0\n"
      "ds_read_b64_tr_b16 v[48:49], v13 offset:0\n"
      "ds_read_b64_tr_b16 v[50:51], v13 offset:768\n"
      "v_lshlrev_b32 v4, 16, v204\n"
      "v_and_b32 v5, s71, v204\n"
      "v_fmac_f32 v2, v4, v4\n"
      "v_fmac_f32 v3, v5, v5\n"
      "v_lshlrev_b32 v6, 16, v205\n"
      "v_and_b32 v7, s71, v205\n"
      "v_fmac_f32 v2, v6, v6\n"
      "v_fmac_f32 v3, v7, v7\n"
      "v_lshlrev_b32 v4, 16, v206\n"
      "v_and_b32 v5, s71, v206\n"
      "v_fmac_f32 v2, v4, v4\n"
      "v_fmac_f32 v3, v5, v5\n"
      "v_lshlrev_b32 v6, 16, v207\n"
      "v_and_b32 v7, s71, v207\n"
      "v_fmac_f32 v2, v6, v6\n"
      "v_fmac_f32 v3, v7, v7\n"
      "v_mfma_f32_16x16x32_bf16 v[32:35], v[92:95], v[204:207], v[32:35]\n"
      "s_waitcnt lgkmcnt(3)\n"
      "s_add_u32 m0, %[sg], 88064\n"
      "v_mfma_f32_32x32x16_bf16 v[16:31], v[208:211], v[64:67], v[16:31]\n"
      "buffer_load_dwordx4 v90, %[srdg], s70 offen lds\n"
      "ds_read_b128 v[216:219], v10 offset:0\n"
      "ds_read_b64_tr_b16 v[56:57], v14 offset:0\n"
      "ds_read_b64_tr_b16 v[58:59], v14 offset:768\n"
      "v_mfma_f32_16x16x32_bf16 v[120:123], v[92:95], v[64:67], v[120:123]\n"
      "v_mfma_f32_16x16x32_bf16 v[32:35], v[92:95], v[208:211], v[32:35]\n"
      "s_waitcnt lgkmcnt(3)\n"
      "v_mfma_f32_32x32x16_bf16 v[16:31], v[212:215], v[48:51], v[16:31]\n"
      "ds_read_b128 v[220:223], v11 offset:0\n"
      "ds_read_b64_tr_b16 v[64:65], v15 offset:0\n"
      "ds_read_b64_tr_b16 v[66:67], v15 offset:768\n"
      "v_lshlrev_b32 v4, 16, v212\n"
      "v_and_b32 v5, s71, v212\n"
      "v_fmac_f32 v2, v4, v4\n"
      "v_fmac_f32 v3, v5, v5\n"
      "v_lshlrev_b32 v6, 16, v213\n"
      "v_and_b32 v7, s71, v213\n"
      "v_fmac_f32 v2, v6, v6\n"
      "v_fmac_f32 v3, v7, v7\n"
      "v_lshlrev_b32 v4, 16, v214\n"
      "v_and_b32 v5, s71, v214\n"
      "v_fmac_f32 v2, v4, v4\n"
      "v_fmac_f32 v3, v5, v5\n"
      "v_lshlrev_b32 v6, 16, v215\n"
      "v_and_b32 v7, s71, v215\n"
      "v_fmac_f32 v2, v6, v6\n"
      "v_fmac_f32 v3, v7, v7\n"
      "v_mfma_f32_16x16x32_bf16 v[32:35], v[92:95], v[212:215], v[32:35]\n"
      "s_waitcnt vmcnt(7)\n"
      "s_barrier\n"
      "s_waitcnt lgkmcnt(3)\n"
      "s_add_u32 s72, %[rot], 1\n"
      "s_and_b32 s72, s72, 7\n"
      "s_mul_i32 s70, s72, 176128\n"
      "s_add_u32 m0, %[sg], 114688\n"
      "v_mfma_f32_32x32x16_bf16 v[16:31], v[216:219], v[56:59], v[16:31]\n"
      "buffer_load_dwordx4 v88, %[srdg], s70 offen lds\n"
      "ds_read_b128 v[224:227], v8 offset:28672\n"
      "ds_read_b64_tr_b16 v[48:49], v12 offset:28672\n"
      "ds_read_b64_tr_b16 v[50:51], v12 offset:29440\n"
      "v_mfma_f32_16x16x32_bf16 v[32:35], v[92:95], v[216:219], v[32:35]\n"
      "s_waitcnt lgkmcnt(3)\n"
      "s_add_u32 m0, %[sg], 115712\n"
      "v_mfma_f32_32x32x16_bf16 v[16:31], v[220:223], v[64:67], v[16:31]\n"
      "buffer_load_dwordx4 v89, %[srdg], s70 offen lds\n"
      "ds_read_b128 v[228:231], v9 offset:28672\n"
      "ds_read_b64_tr_b16 v[56:57], v13 offset:28672\n"
      "ds_read_b64_tr_b16 v[58:59], v13 offset:29440\n"
      "v_lshlrev_b32 v4, 16, v220\n"
      "v_and_b32 v5, s71, v220\n"
      "v_fmac_f32 v2, v4, v4\n"
      "v_fmac_f32 v3, v5, v5\n"
      "v_lshlrev_b32 v6, 16, v221\n"
      "v_and_b32 v7, s71, v221\n"
      "v_fmac_f32 v2, v6, v6\n"
      "v_fmac_f32 v3, v7, v7\n"
      "v_lshlrev_b32 v4, 16, v222\n"
      "v_and_b32 v5, s71, v222\n"
      "v_fmac_f32 v2, v4, v4\n"
      "v_fmac_f32 v3, v5, v5\n"
      "v_lshlrev_b32 v6, 16, v223\n"
      "v_and_b32 v7, s71, v223\n"
      "v_fmac_f32 v2, v6, v6\n"
      "v_fmac_f32 v3, v7, v7\n"
      "v_mfma_f32_16x16x32_bf16 v[32:35], v[92:95], v[220:223], v[32:35]\n"
      "s_waitcnt lgkmcnt(3)\n"
      "s_add_u32 m0, %[sg], 116736\n"
      "v_mfma_f32_32x32x16_bf16 v[16:31], v[224:227], v[48:51], v[16:31]\n"
      "buffer_load_dwordx4 v90, %[srdg], s70 offen lds\n"
      "ds_read_b128 v[232:235], v10 offset:28672\n"
      "ds_read_b64_tr_b16 v[64:65], v14 offset:28672\n"
      "ds_read_b64_tr_b16 v[66:67], v14 offset:29440\n"
      "v_mfma_f32_16x16x32_bf16 v[120:123], v[92:95], v[48:51], v[120:123]\n"
      "v_mfma_f32_16x16x32_bf16 v[32:35], v[92:95], v[224:227], v[32:35]\n"
      "s_waitcnt lgkmcnt(3)\n"
      "v_mfma_f32_32x32x16_bf16 v[16:31], v[228:231], v[56:59], v[16:31]\n"
      "ds_read_b128 v[236:239], v11 offset:28672\n"
      "ds_read_b64_tr_b16 v[48:49], v15 offset:28672\n"
      "ds_read_b64_tr_b16 v[50:51], v15 offset:29440\n"
      "v_lshlrev_b32 v4, 16, v228\n"
      "v_and_b32 v5, s71, v228\n"
      "v_fmac_f32 v2, v4, v4\n"
      "v_fmac_f32 v3, v5, v5\n"
      "v_lshlrev_b32 v6, 16, v229\n"
      "v_and_b32 v7, s71, v229\n"
      "v_fmac_f32 v2, v6, v6\n"
      "v_fmac_f32 v3, v7, v7\n"
      "v_lshlrev_b32 v4, 16, v230\n"
      "v_and_b32 v5, s71, v230\n"
      "v_fmac_f32 v2, v4, v4\n"
      "v_fmac_f32 v3, v5, v5\n"
      "v_lshlrev_b32 v6, 16, v231\n"
      "v_and_b32 v7, s71, v231\n"
      "v_fmac_f32 v2, v6, v6\n"
      "v_fmac_f32 v3, v7, v7\n"
      "v_mfma_f32_16x16x32_bf16 v[32:35], v[92:95], v[228:231], v[32:35]\n"
      "s_waitcnt vmcnt(6)\n"
      "s_barrier\n"
      "s_waitcnt lgkmcnt(3)\n"
      "s_add_u32 s72, %[rot], 2\n"
      "s_and_b32 s72, s72, 7\n"
      "s_mul_i32 s70, s72, 176128\n"
      "s_add_u32 m0, %[sg], 0\n"
      "v_mfma_f32_32x32x16_bf16 v[16:31], v[232:235], v[64:67], v[16:31]\n"
      "buffer_load_dwordx4 v88, %[srdg], s70 offen lds\n"
      "ds_read_b128 v[240:243], v72 offset:0\n"
      "ds_read_b64_tr_b16 v[56:57], v76 offset:0\n"
      "ds_read_b64_tr_b16 v[58:59], v76 offset:768\n"
      "v_mfma_f32_16x16x32_bf16 v[32:35], v[92:95], v[232:235], v[32:35]\n"
      "s_waitcnt lgkmcnt(3)\n"
      "s_add_u32 m0, %[sg], 1024\n"
      "v_mfma_f32_32x32x16_bf16 v[16:31], v[236:239], v[48:51], v[16:31]\n"
      "buffer_load_dwordx4 v89, %[srdg], s70 offen lds\n"
      "ds_read_b128 v[244:247], v73 offset:0\n"
      "ds_read_b64_tr_b16 v[64:65], v77 offset:0\n"
      "ds_read_b64_tr_b16 v[66:67], v77 offset:768\n"
      "v_lshlrev_b32 v4, 16, v236\n"
      "v_and_b32 v5, s71, v236\n"
      "v_fmac_f32 v2, v4, v4\n"
      "v_fmac_f32 v3, v5, v5\n"
      "v_lshlrev_b32 v6, 16, v237\n"
      "v_and_b32 v7, s71, v237\n"
      "v_fmac_f32 v2, v6, v6\n"
      "v_fmac_f32 v3, v7, v7\n"
      "v_lshlrev_b32 v4, 16, v238\n"
      "v_and_b32 v5, s71, v238\n"
      "v_fmac_f32 v2, v4, v4\n"
      "v_fmac_f32 v3, v5, v5\n"
      "v_lshlrev_b32 v6, 16, v239\n"
      "v_and_b32 v7, s71, v239\n"
      "v_fmac_f32 v2, v6, v6\n"
      "v_fmac_f32 v3, v7, v7\n"
      "v_mfma_f32_16x16x32_bf16 v[32:35], v[92:95], v[236:239], v[32:35]\n"
      "s_waitcnt lgkmcnt(3)\n"
      "s_add_u32 m0, %[sg], 2048\n"
      "v_mfma_f32_32x32x16_bf16 v[16:31], v[240:243], v[56:59], v[16:31]\n"
      "buffer_load_dwordx4 v90, %[srdg], s70 offen lds\n"
      "ds_read_b128 v[248:251], v74 offset:0\n"
      "ds_read_b64_tr_b16 v[48:49], v78 offset:0\n"
      "ds_read_b64_tr_b16 v[50:51], v78 offset:768\n"
      "v_mfma_f32_16x16x32_bf16 v[120:123], v[92:95], v[56:59], v[120:123]\n"
      "v_mfma_f32_16x16x32_bf16 v[32:35], v[92:95], v[240:243], v[32:35]\n"
      "s_waitcnt lgkmcnt(3)\n"
      "v_mfma_f32_32x32x16_bf16 v[16:31], v[244:247], v[64:67], v[16:31]\n"
      "ds_read_b128 v[252:255], v75 offset:0\n"
      "ds_read_b64_tr_b16 v[56:57], v79 offset:0\n"
      "ds_read_b64_tr_b16 v[58:59], v79 offset:768\n"
      "v_lshlrev_b32 v4, 16, v244\n"
      "v_and_b32 v5, s71, v244\n"
      "v_fmac_f32 v2, v4, v4\n"
      "v_fmac_f32 v3, v5, v5\n"
      "v_lshlrev_b32 v6, 16, v245\n"
      "v_and_b32 v7, s71, v245\n"
      "v_fmac_f32 v2, v6, v6\n"
      "v_fmac_f32 v3, v7, v7\n"
      "v_lshlrev_b32 v4, 16, v246\n"
      "v_and_b32 v5, s71, v246\n"
      "v_fmac_f32 v2, v4, v4\n"
      "v_fmac_f32 v3, v5, v5\n"
      "v_lshlrev_b32 v6, 16, v247\n"
      "v_and_b32 v7, s71, v247\n"
      "v_fmac_f32 v2, v6, v6\n"
      "v_fmac_f32 v3, v7, v7\n"
      "v_mfma_f32_16x16x32_bf16 v[32:35], v[92:95], v[244:247], v[32:35]\n"
      "s_barrier\n"
      "s_waitcnt lgkmcnt(3)\n"
      "s_add_u32 s72, %[rot], 3\n"
      "s_and_b32 s72, s72, 7\n"
      "s_mul_i32 s70, s72, 176128\n"
      "s_add_u32 m0, %[sg], 28672\n"
      "v_mfma_f32_32x32x16_bf16 v[16:31], v[248:251], v[48:51], v[16:31]\n"
      "buffer_load_dwordx4 v88, %[srdg], s70 offen lds\n"
      "v_mfma_f32_16x16x32_bf16 v[32:35], v[92:95], v[248:251], v[32:35]\n"
      "s_waitcnt lgkmcnt(0)\n"
      "s_add_u32 m0, %[sg], 29696\n"
      "v_mfma_f32_32x32x16_bf16 v[16:31], v[252:255], v[56:59], v[16:31]\n"
      "buffer_load_dwordx4 v89, %[srdg], s70 offen lds\n"
      "s_add_u32 m0, %[sg], 30720\n"
      "s_nop 0\n"
      "buffer_load_dwordx4 v90, %[srdg], s70 offen lds\n"
      "v_lshlrev_b32 v4, 16, v252\n"
      "v_and_b32 v5, s71, v252\n"
      "v_fmac_f32 v2, v4, v4\n"
      "v_fmac_f32 v3, v5, v5\n"
      "v_lshlrev_b32 v6, 16, v253\n"
      "v_and_b32 v7, s71, v253\n"
      "v_fmac_f32 v2, v6, v6\n"
      "v_fmac_f32 v3, v7, v7\n"
      "v_lshlrev_b32 v4, 16, v254\n"
      "v_and_b32 v5, s71, v254\n"
      "v_fmac_f32 v2, v4, v4\n"
      "v_fmac_f32 v3, v5, v5\n"
      "v_lshlrev_b32 v6, 16, v255\n"
      "v_and_b32 v7, s71, v255\n"
      "v_fmac_f32 v2, v6, v6\n"
      "v_fmac_f32 v3, v7, v7\n"
      "v_mfma_f32_16x16x32_bf16 v[32:35], v[92:95], v[252:255], v[32:35]\n"
      "v_add_f32 v2, v2, v3\n"
      "s_lshl_b32 s72, %[ti], 2\n"
      "v_mov_b32 v4, v2\n"
      "v_add_u32 v5, s72, v105\n"
      "s_mul_i32 s68, %[ji], 393216\n"
      "v_permlane32_swap_b32 v4, v2\n"
      "s_lshl_b32 s69, %[gi], 12\n"
      "s_add_u32 s68, s68, s69\n"
      "v_add_f32 v2, v2, v4\n"
      "s_lshl_b32 s69, %[ti], 10\n"
      "s_add_u32 s68, s68, s69\n"
      "s_nop 1\n"
      "ds_write_b32 v5, v120 offset:0\n"
      "ds_write_b32 v106, v2 offset:128\n"
      "ds_write_b32 v106, v32 offset:256\n"
      "v_readfirstlane_b32 %[ep0], v116\n"
      "v_readfirstlane_b32 %[ep1], v117\n"
      "s_add_u32 s68, s68, 1310720\n"
      "s_setprio 1\n"
      "s_waitcnt vmcnt(6) lgkmcnt(0)\n"
      "s_barrier\n"
      "ds_read_b32 v4, v106 offset:256\n"
      "ds_read_b32 v5, v106\n"
      "ds_read_b32 v6, v106 offset:128\n"
      "ds_read_b128 v[48:51], v105 offset:0\n"
      "s_add_u32 s72, %[rot], 4\n"
      "s_and_b32 s72, s72, 7\n"
      "s_mul_i32 s70, s72, 176128\n"
      "s_add_u32 m0, %[sg], 57344\n"
      "s_nop 0\n"
      "buffer_load_dwordx4 v88, %[srdg], s70 offen lds\n"
      "s_add_u32 m0, %[sg], 58368\n"
      "s_nop 0\n"
      "buffer_load_dwordx4 v89, %[srdg], s70 offen lds\n"
      "s_add_u32 m0, %[sg], 59392\n"
      "s_nop 0\n"
      "buffer_load_dwordx4 v90, %[srdg], s70 offen lds\n"
      "v_mov_b32 v122, 0\n"
      "v_mov_b32 v123, 0\n"
      "v_mov_b32 v58, 0\n"
      "v_mov_b32 v59, 0\n"
      "s_mov_b32 s70, 0x3b000000\n"
      "s_waitcnt lgkmcnt(1)\n"
      "v_mul_f32 v4, 0x3b000000, v4\n"
      "v_add_f32 v5, v5, v6\n"
      "v_mul_f32 v6, v4, v4\n"
      "v_mul_f32 v4, -0.5, v4\n"
      "v_fma_f32 v5, v5, s70, -v6\n"
      "v_cvt_pk_bf16_f32 v7, v4, v4\n"
      "v_max_f32 v5, 0, v5\n"
      "v_and_b32 v7, s71, v7\n"
      "v_add_f32 v5, 0x3727c5ac, v5\n"
      "v_sub_f32 v7, v4, v7\n"
      "v_rsq_f32 v124, v5\n"
      "v_cvt_pk_bf16_f32 v120, v4, v7\n"
      "s_waitcnt lgkmcnt(0)\n"
      "v_mov_b32 v121, v120\n"
      "s_lshl_b32 s72, %[wid], 7\n"
      "s_add_u32 s72, s72, 146432\n"
      "v_bfe_u32 v5, v104, 2, 7\n"
      "v_add_u32 v5, s72, v5\n"
      "v_add_u32 v108, s72, v108\n"
      "ds_write_b32 v5, v124\n"
      "ds_read_b128 v[72:75], v108 offset:0\n"
      "ds_read_b128 v[76:79], v108 offset:32\n"
      "ds_read_b128 v[80:83], v108 offset:64\n"
      "ds_read_b128 v[84:87], v108 offset:96\n"
      "v_add_f32 v48, v48, v49\n"
      "v_add_f32 v50, v50, v51\n"
      "v_add_f32 v48, v48, v50\n"
      "v_cvt_pk_bf16_f32 v56, v48, v48\n"
      "v_and_b32 v49, s71, v56\n"
      "v_sub_f32 v48, v48, v49\n"
      "v_cvt_pk_bf16_f32 v57, v48, v48\n"
      "s_nop 1\n"
      "v_mfma_f32_32x32x16_bf16 v[16:31], v[120:123], v[56:59], v[16:31]\n"
      "v_add_u32 v107, s68, v107\n"
      "s_waitcnt lgkmcnt(0)\n"
      "s_nop 10\n"
      "v_mul_f32 v16, v16, v72\n"
      "v_mul_f32 v17, v17, v73\n"
      "v_mul_f32 v18, v18, v74\n"
      "v_mul_f32 v19, v19, v75\n"
      "v_mul_f32 v20, v20, v76\n"
      "v_mul_f32 v21, v21, v77\n"
      "v_mul_f32 v22, v22, v78\n"
      "v_mul_f32 v23, v23, v79\n"
      "v_cvt_pk_bf16_f32 v64, v16, v17\n"
      "v_cvt_pk_bf16_f32 v65, v18, v19\n"
      "v_cvt_pk_bf16_f32 v66, v20, v21\n"
      "v_cvt_pk_bf16_f32 v67, v22, v23\n"
      "buffer_store_dwordx4 v[64:67], v107, %[srd], 0 offen offset:0 sc1\n"
      "v_mul_f32 v24, v24, v80\n"
      "v_mul_f32 v25, v25, v81\n"
      "v_mul_f32 v26, v26, v82\n"
      "v_mul_f32 v27, v27, v83\n"
      "v_mul_f32 v28, v28, v84\n"
      "v_mul_f32 v29, v29, v85\n"
      "v_mul_f32 v30, v30, v86\n"
      "v_mul_f32 v31, v31, v87\n"
      "v_cvt_pk_bf16_f32 v68, v24, v25\n"
      "v_cvt_pk_bf16_f32 v69, v26, v27\n"
      "v_cvt_pk_bf16_f32 v70, v28, v29\n"
      "v_cvt_pk_bf16_f32 v71, v30, v31\n"
      "buffer_store_dwordx4 v[68:71], v107, %[srd], 0 offen offset:512 sc1\n"
      "v_mov_b32 v112, -1\n"
      "v_mov_b32 v113, -1\n"
      "v_mov_b32 v114, -1\n"
      "v_mov_b32 v115, -1\n"
      "buffer_store_dwordx4 v[112:115], %[vsn], %[srd], 0 offen sc1\n"
      "v_bfe_u32 v6, v104, 8, 1\n"
      "v_mul_u32_u24 v6, 12288, v6\n"
      "v_add_u32 v6, 69600, v6\n"
      "v_add_u32 v12, v12, v6\n"
      "v_mov_b32 v8, v88\n"
      "v_mov_b32 v28, v92\n"
      "v_add_u32 v13, v13, v6\n"
      "v_mov_b32 v9, v89\n"
      "v_mov_b32 v29, v92\n"
      "v_add_u32 v14, v14, v6\n"
      "v_mov_b32 v10, v90\n"
      "v_mov_b32 v30, v92\n"
      "v_add_u32 v15, v15, v6\n"
      "v_mov_b32 v11, v91\n"
      "v_mov_b32 v31, v92\n"
      "49:\n"
      : QKV_OUT : QKV_IN : "memory", "scc", "v2", "v3", "v4", "v5", "v6", "v7", "v16", "v17", "v18", "v19", "v20", "v21", "v22", "v23", "v24", "v25", "v26", "v27", "v32", "v33", "v34", "v35", "v36", "v37", "v38", "v39", "v40", "v41", "v42", "v43", "v44", "v45", "v46", "v47", "v48", "v49", "v50", "v51", "v52", "v53", "v54", "v55", "v56", "v57", "v58", "v59", "v60", "v61", "v62", "v63", "v64", "v65", "v66", "v67", "v68", "v69", "v70", "v71", "v72", "v73", "v74", "v75", "v76", "v77", "v78", "v79", "v80", "v81", "v82", "v83", "v84", "v85", "v86", "v87", "v88", "v89", "v90", "v91", "v104", "v105", "v106", "v107", "v108", "v109", "v110", "v112", "v113", "v114", "v115", "v116", "v117", "v125", "v126", "v127", "vcc", "s98", "s99", "s68", "s69", "s70", "s71", "s72", "s73");
  }
  STAMP(1); RSTAMP(1);
  STAMP(2);
  {
    f32x16 a2, a3, a4;
    f32x8 gcs;
    f32x4 gcs2;
    const u32 wrow0 = (g & 7) * 236 + t * 59;
    const rsrc_t wrr = __builtin_amdgcn_make_buffer_rsrc((void*)(wrow0 < 1376 ? wd : wo), 0, wrow0 < 1376 ? 1376 * 1024 : 512 * 1024, 0x00020000);
    asm volatile(
      "ds_read_b64_tr_b16 v[96:97], v12 offset:0\n"
      "ds_read_b64_tr_b16 v[98:99], v12 offset:768\n"
      "ds_read_b64_tr_b16 v[100:101], v12 offset:32\n"
      "ds_read_b64_tr_b16 v[102:103], v12 offset:800\n"
      "ds_read_b64_tr_b16 v[104:105], v12 offset:64\n"
      "ds_read_b64_tr_b16 v[106:107], v12 offset:832\n"
      "v_add_u32 v12, 28672, v12\n"
      "ds_read_b64_tr_b16 v[108:109], v13 offset:0\n"
      "ds_read_b64_tr_b16 v[110:111], v13 offset:768\n"
      "ds_read_b64_tr_b16 v[112:113], v13 offset:32\n"
      "ds_read_b64_tr_b16 v[114:115], v13 offset:800\n"
      "ds_read_b64_tr_b16 v[116:117], v13 offset:64\n"
      "ds_read_b64_tr_b16 v[118:119], v13 offset:832\n"
      "v_add_u32 v13, 28672, v13\n"
      "s_waitcnt lgkmcnt(6)\n"
      "v_mfma_f32_32x32x16_bf16 v[48:63], v[96:99], v[128:131], 0\n"
      "ds_read_b64_tr_b16 v[32:33], v14 offset:0\n"
      "ds_read_b64_tr_b16 v[34:35], v14 offset:768\n"
      "v_mfma_f32_16x16x32_bf16 v[16:19], v[28:31], v[96:99], 0\n"
      "v_mfma_f32_32x32x16_bf16 v[64:79], v[100:103], v[128:131], 0\n"
      "ds_read_b64_tr_b16 v[36:37], v14 offset:32\n"
      "ds_read_b64_tr_b16 v[38:39], v14 offset:800\n"
      "v_mfma_f32_16x16x32_bf16 v[20:23], v[28:31], v[100:103], 0\n"
      "v_mfma_f32_32x32x16_bf16 v[80:95], v[104:107], v[128:131], 0\n"
      "ds_read_b64_tr_b16 v[40:41], v14 offset:64\n"
      "ds_read_b64_tr_b16 v[42:43], v14 offset:832\n"
      "v_add_u32 v14, 28672, v14\n"
      "v_mfma_f32_16x16x32_bf16 v[24:27], v[28:31], v[104:107], 0\n"
      "s_waitcnt lgkmcnt(6)\n"
      "v_mfma_f32_32x32x16_bf16 v[48:63], v[108:111], v[132:135], v[48:63]\n"
      "ds_read_b64_tr_b16 v[96:97], v15 offset:0\n"
      "ds_read_b64_tr_b16 v[98:99], v15 offset:768\n"
      "s_lshl_b32 s65, %[ji], 7\n"
      "s_and_b32 s65, s65, 0x380\n"
      "s_and_b32 s66, %[gi], 7\n"
      "v_mfma_f32_32x32x16_bf16 v[64:79], v[112:115], v[132:135], v[64:79]\n"
      "ds_read_b64_tr_b16 v[100:101], v15 offset:32\n"
      "ds_read_b64_tr_b16 v[102:103], v15 offset:800\n"
      "s_mul_i32 s66, s66, 236\n"
      "s_mul_i32 s67, %[ti], 59\n"
      "s_add_u32 s66, s66, s67\n"
      "v_mfma_f32_32x32x16_bf16 v[80:95], v[116:119], v[132:135], v[80:95]\n"
      "ds_read_b64_tr_b16 v[104:105], v15 offset:64\n"
      "ds_read_b64_tr_b16 v[106:107], v15 offset:832\n"
      "v_add_u32 v15, 28672, v15\n"
      "s_sub_u32 s67, s66, 1376\n"
      "s_cselect_b32 s66, s66, s67\n"
      "s_waitcnt lgkmcnt(6)\n"
      "v_mfma_f32_32x32x16_bf16 v[48:63], v[32:35], v[136:139], v[48:63]\n"
      "ds_read_b64_tr_b16 v[108:109], v12 offset:0\n"
      "ds_read_b64_tr_b16 v[110:111], v12 offset:768\n"
      "s_lshl_b32 s66, s66, 10\n"
      "s_add_u32 s47, s66, s65\n"
      "s_add_u32 s42, %[ep0], 2135587861\n"
      "s_addc_u32 s43, %[ep1], 2654435769\n"
      "v_mfma_f32_32x32x16_bf16 v[64:79], v[36:39], v[136:139], v[64:79]\n"
      "ds_read_b64_tr_b16 v[112:113], v12 offset:32\n"
      "ds_read_b64_tr_b16 v[114:115], v12 offset:800\n"
      "s_mov_b32 s41, %[ri]\n"
      "s_mov_b32 s40, 2097152\n"
      "s_mov_b32 s44, 0xffff\n"
      "v_mfma_f32_32x32x16_bf16 v[80:95], v[40:43], v[136:139], v[80:95]\n"
      "ds_read_b64_tr_b16 v[116:117], v12 offset:64\n"
      "ds_read_b64_tr_b16 v[118:119], v12 offset:832\n"
      "v_add_u32 v12, 4294852608, v12\n"
      "s_mov_b32 s45, 0xffff\n"
      "s_waitcnt lgkmcnt(6)\n"
      "v_mfma_f32_32x32x16_bf16 v[48:63], v[96:99], v[140:143], v[48:63]\n"
      "ds_read_b64_tr_b16 v[32:33], v13 offset:0\n"
      "ds_read_b64_tr_b16 v[34:35], v13 offset:768\n"
      "s_lshl_b32 s66, %[ji], 1\n"
      "s_add_u32 s66, s66, %[ri]\n"
      "s_lshl_b32 s67, s66, 16\n"
      "v_mfma_f32_32x32x16_bf16 v[64:79], v[100:103], v[140:143], v[64:79]\n"
      "ds_read_b64_tr_b16 v[36:37], v13 offset:32\n"
      "ds_read_b64_tr_b16 v[38:39], v13 offset:800\n"
      "s_lshl_b32 s36, %[gi], 12\n"
      "s_add_u32 s36, s36, s67\n"
      "s_add_u32 s38, s36, 3145728\n"
      "v_mfma_f32_32x32x16_bf16 v[80:95], v[104:107], v[140:143], v[80:95]\n"
      "ds_read_b64_tr_b16 v[40:41], v13 offset:64\n"
      "ds_read_b64_tr_b16 v[42:43], v13 offset:832\n"
      "v_add_u32 v13, 4294852608, v13\n"
      "s_add_u32 s36, s36, 1048576\n"
      "s_waitcnt lgkmcnt(6)\n"
      "v_mfma_f32_32x32x16_bf16 v[48:63], v[108:111], v[144:147], v[48:63]\n"
      "ds_read_b64_tr_b16 v[96:97], v14 offset:0\n"
      "ds_read_b64_tr_b16 v[98:99], v14 offset:768\n"
      "s_lshl_b32 s37, %[ti], 10\n"
      "s_lshl_b32 s39, %[ti], 14\n"
      "s_add_u32 s39, s39, s67\n"
      "v_mfma_f32_16x16x32_bf16 v[16:19], v[28:31], v[108:111], v[16:19]\n"
      "v_mfma_f32_32x32x16_bf16 v[64:79], v[112:115], v[144:147], v[64:79]\n"
      "ds_read_b64_tr_b16 v[100:101], v14 offset:32\n"
      "ds_read_b64_tr_b16 v[102:103], v14 offset:800\n"
      "s_add_u32 s39, s39, 3145728\n"
      "s_lshl_b32 s66, s66, 10\n"
      "s_lshl_b32 s60, %[gi], 17\n"
      "v_mfma_f32_16x16x32_bf16 v[20:23], v[28:31], v[112:115], v[20:23]\n"
      "v_mfma_f32_32x32x16_bf16 v[80:95], v[116:119], v[144:147], v[80:95]\n"
      "ds_read_b64_tr_b16 v[104:105], v14 offset:64\n"
      "ds_read_b64_tr_b16 v[106:107], v14 offset:832\n"
      "v_add_u32 v14, 4294852608, v14\n"
      "s_add_u32 s60, s60, s66\n"
      "v_mfma_f32_16x16x32_bf16 v[24:27], v[28:31], v[116:119], v[24:27]\n"
      "s_waitcnt lgkmcnt(6)\n"
      "v_mfma_f32_32x32x16_bf16 v[48:63], v[32:35], v[148:151], v[48:63]\n"
      "ds_read_b64_tr_b16 v[108:109], v15 offset:0\n"
      "ds_read_b64_tr_b16 v[110:111], v15 offset:768\n"
      "s_lshl_b32 s66, %[ti], 15\n"
      "s_add_u32 s60, s60, s66\n"
      "s_add_u32 s60, s60, 7340032\n"
      "v_mfma_f32_32x32x16_bf16 v[64:79], v[36:39], v[148:151], v[64:79]\n"
      "ds_read_b64_tr_b16 v[112:113], v15 offset:32\n"
      "ds_read_b64_tr_b16 v[114:115], v15 offset:800\n"
      "s_lshl_b32 s62, %[gi], 11\n"
      "s_add_u32 s62, s62, 36864\n"
      "s_lshl_b32 s66, %[ji], 7\n"
      "v_mfma_f32_32x32x16_bf16 v[80:95], v[40:43], v[148:151], v[80:95]\n"
      "ds_read_b64_tr_b16 v[116:117], v15 offset:64\n"
      "ds_read_b64_tr_b16 v[118:119], v15 offset:832\n"
      "v_add_u32 v15, 4294852608, v15\n"
      "s_add_u32 s46, s62, s66\n"
      "s_cmp_eq_u32 %[ri], 0\n"
      "s_cbranch_scc1 5f\n"
      "s_waitcnt vmcnt(9)\n"
      "5:\n"
      "s_setprio 0\n"
      "s_waitcnt vmcnt(11)\n"
      "s_barrier\n"
      "s_waitcnt lgkmcnt(6)\n"
      "v_mfma_f32_32x32x16_bf16 v[48:63], v[96:99], v[152:155], v[48:63]\n"
      "ds_read_b64_tr_b16 v[32:33], v12 offset:0\n"
      "ds_read_b64_tr_b16 v[34:35], v12 offset:768\n"
      "s_add_u32 s66, %[ti], 1\n"
      "s_and_b32 s66, s66, 3\n"
      "s_mul_i32 s66, s66, 40960\n"
      "v_mfma_f32_32x32x16_bf16 v[64:79], v[100:103], v[152:155], v[64:79]\n"
      "ds_read_b64_tr_b16 v[36:37], v12 offset:32\n"
      "ds_read_b64_tr_b16 v[38:39], v12 offset:800\n"
      "s_mul_i32 s67, %[ri], 7168\n"
      "s_add_u32 s66, s66, s67\n"
      "s_add_u32 s48, s66, 31232\n"
      "v_mfma_f32_32x32x16_bf16 v[80:95], v[104:107], v[152:155], v[80:95]\n"
      "ds_read_b64_tr_b16 v[40:41], v12 offset:64\n"
      "ds_read_b64_tr_b16 v[42:43], v12 offset:832\n"
      "v_add_u32 v12, 28672, v12\n"
      "s_add_u32 s66, %[ti], 2\n"
      "s_waitcnt lgkmcnt(6)\n"
      "v_mfma_f32_32x32x16_bf16 v[48:63], v[108:111], v[156:159], v[48:63]\n"
      "ds_read_b64_tr_b16 v[96:97], v13 offset:0\n"
      "ds_read_b64_tr_b16 v[98:99], v13 offset:768\n"
      "s_and_b32 s66, s66, 3\n"
      "s_mul_i32 s66, s66, 40960\n"
      "s_mul_i32 s67, %[ri], 7168\n"
      "v_mfma_f32_32x32x16_bf16 v[64:79], v[112:115], v[156:159], v[64:79]\n"
      "ds_read_b64_tr_b16 v[100:101], v13 offset:32\n"
      "ds_read_b64_tr_b16 v[102:103], v13 offset:800\n"
      "s_add_u32 s66, s66, s67\n"
      "s_add_u32 s49, s66, 28928\n"
      "s_add_u32 s66, %[ti], 3\n"
      "v_mfma_f32_32x32x16_bf16 v[80:95], v[116:119], v[156:159], v[80:95]\n"
      "ds_read_b64_tr_b16 v[104:105], v13 offset:64\n"
      "ds_read_b64_tr_b16 v[106:107], v13 offset:832\n"
      "v_add_u32 v13, 28672, v13\n"
      "s_and_b32 s66, s66, 3\n"
      "s_waitcnt lgkmcnt(6)\n"
      "v_mfma_f32_32x32x16_bf16 v[48:63], v[32:35], v[160:163], v[48:63]\n"
      "ds_read_b64_tr_b16 v[108:109], v14 offset:0\n"
      "ds_read_b64_tr_b16 v[110:111], v14 offset:768\n"
      "s_mul_i32 s66, s66, 40960\n"
      "s_mul_i32 s67, %[ri], 7168\n"
      "s_add_u32 s66, s66, s67\n"
      "v_mfma_f32_16x16x32_bf16 v[16:19], v[28:31], v[32:35], v[16:19]\n"
      "v_mfma_f32_32x32x16_bf16 v[64:79], v[36:39], v[160:163], v[64:79]\n"
      "ds_read_b64_tr_b16 v[112:113], v14 offset:32\n"
      "ds_read_b64_tr_b16 v[114:115], v14 offset:800\n"
      "s_add_u32 s50, s66, 26624\n"
      "s_add_u32 s66, %[ti], 0\n"
      "s_and_b32 s66, s66, 3\n"
      "v_mfma_f32_16x16x32_bf16 v[20:23], v[28:31], v[36:39], v[20:23]\n"
      "v_mfma_f32_32x32x16_bf16 v[80:95], v[40:43], v[160:163], v[80:95]\n"
      "ds_read_b64_tr_b16 v[116:117], v14 offset:64\n"
      "ds_read_b64_tr_b16 v[118:119], v14 offset:832\n"
      "v_add_u32 v14, 28672, v14\n"
      "s_mul_i32 s66, s66, 40960\n"
      "v_mfma_f32_16x16x32_bf16 v[24:27], v[28:31], v[40:43], v[24:27]\n"
      "s_cmp_lg_u32 %[ri], 0\n"
      "s_cbranch_scc1 4f\n"
      "v_mbcnt_lo_u32_b32 v7, -1, 0\n"
      "v_mbcnt_hi_u32_b32 v7, -1, v7\n"
      "v_lshlrev_b32 v7, 10, v7\n"
      "buffer_load_dword v7, v7, %[srdwarm], s47 offen\n"
      "4:\n"
      "s_waitcnt lgkmcnt(6)\n"
      "v_mfma_f32_32x32x16_bf16 v[48:63], v[96:99], v[164:167], v[48:63]\n"
      "ds_read_b64_tr_b16 v[32:33], v15 offset:0\n"
      "ds_read_b64_tr_b16 v[34:35], v15 offset:768\n"
      "s_mul_i32 s67, %[ri], 7168\n"
      "s_add_u32 s66, s66, s67\n"
      "s_add_u32 s51, s66, 26624\n"
      "v_mfma_f32_32x32x16_bf16 v[64:79], v[100:103], v[164:167], v[64:79]\n"
      "ds_read_b64_tr_b16 v[36:37], v15 offset:32\n"
      "ds_read_b64_tr_b16 v[38:39], v15 offset:800\n"
      "s_lshl_b32 s52, %[wid], 2\n"
      "s_add_u32 s52, s52, 163808\n"
      "s_mul_i32 s58, %[ti], 40960\n"
      "v_mfma_f32_32x32x16_bf16 v[80:95], v[104:107], v[164:167], v[80:95]\n"
      "ds_read_b64_tr_b16 v[40:41], v15 offset:64\n"
      "ds_read_b64_tr_b16 v[42:43], v15 offset:832\n"
      "v_add_u32 v15, 28672, v15\n"
      "s_lshl_b32 s57, %[ti], 17\n"
      "s_cmp_eq_u32 %[ri], 0\n"
      "s_cbranch_scc1 5f\n"
      "s_waitcnt vmcnt(6)\n"
      "5:\n"
      "s_waitcnt vmcnt(9)\n"
      "s_barrier\n"
      "s_add_u32 s72, %[rot], 5\n"
      "s_and_b32 s72, s72, 7\n"
      "s_mul_i32 s70, s72, 176128\n"
      "s_waitcnt lgkmcnt(6)\n"
      "v_mfma_f32_32x32x16_bf16 v[48:63], v[108:111], v[168:171], v[48:63]\n"
      "ds_read_b64_tr_b16 v[96:97], v12 offset:0\n"
      "ds_read_b64_tr_b16 v[98:99], v12 offset:768\n"
      "s_add_u32 s57, s57, s65\n"
      "s_mul_i32 s66, %[ti], 22\n"
      "s_cmp_eq_u32 %[ti], 3\n"
      "s_subb_u32 s66, s66, 0\n"
      "s_add_u32 m0, %[sg], 86016\n"
      "v_mfma_f32_32x32x16_bf16 v[64:79], v[112:115], v[168:171], v[64:79]\n"
      "buffer_load_dwordx4 v8, %[srdg], s70 offen lds\n"
      "ds_read_b64_tr_b16 v[100:101], v12 offset:32\n"
      "ds_read_b64_tr_b16 v[102:103], v12 offset:800\n"
      "s_cmp_lt_u32 %[ti], 2\n"
      "s_cselect_b32 s59, 1, 0\n"
      "s_lshl_b32 s55, s66, 14\n"
      "s_add_u32 s55, s55, s65\n"
      "v_mfma_f32_32x32x16_bf16 v[80:95], v[116:119], v[168:171], v[80:95]\n"
      "ds_read_b64_tr_b16 v[104:105], v12 offset:64\n"
      "ds_read_b64_tr_b16 v[106:107], v12 offset:832\n"
      "v_add_u32 v12, 28672, v12\n"
      "s_add_u32 s56, s59, 20\n"
      "s_waitcnt lgkmcnt(6)\n"
      "v_mfma_f32_32x32x16_bf16 v[48:63], v[32:35], v[172:175], v[48:63]\n"
      "ds_read_b64_tr_b16 v[108:109], v13 offset:0\n"
      "ds_read_b64_tr_b16 v[110:111], v13 offset:768\n"
      "s_lshl_b32 s54, s56, 10\n"
      "s_lshl_b32 s56, s56, 14\n"
      "s_add_u32 s56, s56, s55\n"
      "s_add_u32 m0, %[sg], 87040\n"
      "v_mfma_f32_32x32x16_bf16 v[64:79], v[36:39], v[172:175], v[64:79]\n"
      "buffer_load_dwordx4 v9, %[srdg], s70 offen lds\n"
      "ds_read_b64_tr_b16 v[112:113], v13 offset:32\n"
      "ds_read_b64_tr_b16 v[114:115], v13 offset:800\n"
      "s_lshl_b32 s53, s66, 10\n"
      "s_lshr_b32 s66, %[ji], 3\n"
      "s_lshl_b32 s66, s66, 1\n"
      "v_mfma_f32_32x32x16_bf16 v[80:95], v[40:43], v[172:175], v[80:95]\n"
      "ds_read_b64_tr_b16 v[116:117], v13 offset:64\n"
      "ds_read_b64_tr_b16 v[118:119], v13 offset:832\n"
      "v_add_u32 v13, 28672, v13\n"
      "s_lshl_b32 s67, %[gi], 2\n"
      "s_waitcnt lgkmcnt(6)\n"
      "v_mfma_f32_32x32x16_bf16 v[48:63], v[96:99], v[176:179], v[48:63]\n"
      "ds_read_b64_tr_b16 v[32:33], v14 offset:0\n"
      "ds_read_b64_tr_b16 v[34:35], v14 offset:768\n"
      "s_add_u32 s67, s67, s66\n"
      "s_mul_i32 s64, s67, 88064\n"
      "s_add_u32 s64, s64, 9437184\n"
      "v_mfma_f32_16x16x32_bf16 v[16:19], v[28:31], v[96:99], v[16:19]\n"
      "s_add_u32 m0, %[sg], 88064\n"
      "v_mfma_f32_32x32x16_bf16 v[64:79], v[100:103], v[176:179], v[64:79]\n"
      "buffer_load_dwordx4 v10, %[srdg], s70 offen lds\n"
      "ds_read_b64_tr_b16 v[36:37], v14 offset:32\n"
      "ds_read_b64_tr_b16 v[38:39], v14 offset:800\n"
      "s_add_u32 s53, s53, s64\n"
      "s_add_u32 s54, s54, s53\n"
      "s_lshl_b32 s63, s67, 15\n"
      "v_mfma_f32_16x16x32_bf16 v[20:23], v[28:31], v[100:103], v[20:23]\n"
      "v_mfma_f32_32x32x16_bf16 v[80:95], v[104:107], v[176:179], v[80:95]\n"
      "ds_read_b64_tr_b16 v[40:41], v14 offset:64\n"
      "ds_read_b64_tr_b16 v[42:43], v14 offset:832\n"
      "v_add_u32 v14, 28672, v14\n"
      "s_add_u32 s63, s63, s65\n"
      "v_mfma_f32_16x16x32_bf16 v[24:27], v[28:31], v[104:107], v[24:27]\n"
      "s_add_u32 s72, %[rot], 6\n"
      "s_and_b32 s72, s72, 7\n"
      "s_mul_i32 s70, s72, 176128\n"
      "s_waitcnt lgkmcnt(6)\n"
      "v_mfma_f32_32x32x16_bf16 v[48:63], v[108:111], v[180:183], v[48:63]\n"
      "ds_read_b64_tr_b16 v[96:97], v15 offset:0\n"
      "ds_read_b64_tr_b16 v[98:99], v15 offset:768\n"
      "s_add_u32 s67, s67, %[ri]\n"
      "s_lshl_b32 s61, s67, 15\n"
      "s_lshl_b32 s67, %[ti], 13\n"
      "s_add_u32 m0, %[sg], 114688\n"
      "v_mfma_f32_32x32x16_bf16 v[64:79], v[112:115], v[180:183], v[64:79]\n"
      "buffer_load_dwordx4 v8, %[srdg], s70 offen lds\n"
      "ds_read_b64_tr_b16 v[100:101], v15 offset:32\n"
      "ds_read_b64_tr_b16 v[102:103], v15 offset:800\n"
      "s_add_u32 s61, s61, s67\n"
      "s_add_u32 s61, s61, 7340032\n"
      "s_lshl_b32 s67, %[wid], 13\n"
      "v_mfma_f32_32x32x16_bf16 v[80:95], v[116:119], v[180:183], v[80:95]\n"
      "ds_read_b64_tr_b16 v[104:105], v15 offset:64\n"
      "ds_read_b64_tr_b16 v[106:107], v15 offset:832\n"
      "v_add_u32 v15, 28672, v15\n"
      "s_add_u32 s63, s63, s67\n"
      "s_cmp_eq_u32 %[ri], 0\n"
      "s_cbranch_scc1 5f\n"
      "s_waitcnt vmcnt(7)\n"
      "5:\n"
      "s_waitcnt vmcnt(10)\n"
      "s_barrier\n"
      "s_waitcnt lgkmcnt(6)\n"
      "v_mfma_f32_32x32x16_bf16 v[48:63], v[32:35], v[184:187], v[48:63]\n"
      "ds_read_b64_tr_b16 v[108:109], v12 offset:0\n"
      "ds_read_b64_tr_b16 v[110:111], v12 offset:768\n"
      "s_lshl_b32 s66, %[ji], 1\n"
      "s_add_u32 s66, s66, 2\n"
      "s_and_b32 s66, s66, 7\n"
      "s_add_u32 m0, %[sg], 115712\n"
      "v_mfma_f32_32x32x16_bf16 v[64:79], v[36:39], v[184:187], v[64:79]\n"
      "buffer_load_dwordx4 v9, %[srdg], s70 offen lds\n"
      "ds_read_b64_tr_b16 v[112:113], v12 offset:32\n"
      "ds_read_b64_tr_b16 v[114:115], v12 offset:800\n"
      "s_lshl_b32 s65, s66, 10\n"
      "s_and_b32 s66, %[ji], 3\n"
      "s_mul_i32 s66, s66, 44032\n"
      "v_mfma_f32_32x32x16_bf16 v[80:95], v[40:43], v[184:187], v[80:95]\n"
      "ds_read_b64_tr_b16 v[116:117], v12 offset:64\n"
      "ds_read_b64_tr_b16 v[118:119], v12 offset:832\n"
      "v_add_u32 v12, 28672, v12\n"
      "s_mul_i32 s67, %[ti], 11008\n"
      "s_waitcnt lgkmcnt(6)\n"
      "v_mfma_f32_32x32x16_bf16 v[48:63], v[96:99], v[188:191], v[48:63]\n"
      "ds_read_b64_tr_b16 v[32:33], v13 offset:0\n"
      "ds_read_b64_tr_b16 v[34:35], v13 offset:768\n"
      "s_add_u32 s66, s66, s67\n"
      "s_add_u32 s64, s64, s66\n"
      "s_add_u32 m0, %[sg], 116736\n"
      "v_mfma_f32_32x32x16_bf16 v[64:79], v[100:103], v[188:191], v[64:79]\n"
      "buffer_load_dwordx4 v10, %[srdg], s70 offen lds\n"
      "ds_read_b64_tr_b16 v[36:37], v13 offset:32\n"
      "ds_read_b64_tr_b16 v[38:39], v13 offset:800\n"
      "s_mul_i32 s66, %[ri], 8704\n"
      "s_lshl_b32 s67, %[wid], 3\n"
      "v_mfma_f32_32x32x16_bf16 v[80:95], v[104:107], v[188:191], v[80:95]\n"
      "ds_read_b64_tr_b16 v[40:41], v13 offset:64\n"
      "ds_read_b64_tr_b16 v[42:43], v13 offset:832\n"
      "v_add_u32 v13, 28672, v13\n"
      "s_add_u32 s72, %[rot], 7\n"
      "s_and_b32 s72, s72, 7\n"
      "s_mul_i32 s70, s72, 176128\n"
      "s_waitcnt lgkmcnt(6)\n"
      "v_mfma_f32_32x32x16_bf16 v[48:63], v[108:111], v[192:195], v[48:63]\n"
      "ds_read_b64_tr_b16 v[96:97], v14 offset:0\n"
      "ds_read_b64_tr_b16 v[98:99], v14 offset:768\n"
      "v_mov_b32 v191, v11\n"
      "v_mbcnt_lo_u32_b32 v189, -1, 0\n"
      "v_mfma_f32_16x16x32_bf16 v[16:19], v[28:31], v[108:111], v[16:19]\n"
      "s_add_u32 m0, %[sg], 0\n"
      "v_mfma_f32_32x32x16_bf16 v[64:79], v[112:115], v[192:195], v[64:79]\n"
      "buffer_load_dwordx4 v8, %[srdg], s70 offen lds\n"
      "ds_read_b64_tr_b16 v[100:101], v14 offset:32\n"
      "ds_read_b64_tr_b16 v[102:103], v14 offset:800\n"
      "v_mbcnt_hi_u32_b32 v189, -1, v189\n"
      "v_and_b32 v190, 31, v189\n"
      "v_mfma_f32_16x16x32_bf16 v[20:23], v[28:31], v[112:115], v[20:23]\n"
      "v_mfma_f32_32x32x16_bf16 v[80:95], v[116:119], v[192:195], v[80:95]\n"
      "ds_read_b64_tr_b16 v[104:105], v14 offset:64\n"
      "ds_read_b64_tr_b16 v[106:107], v14 offset:832\n"
      "v_add_u32 v14, 28672, v14\n"
      "v_mfma_f32_16x16x32_bf16 v[24:27], v[28:31], v[116:119], v[24:27]\n"
      "s_waitcnt lgkmcnt(6)\n"
      "v_mfma_f32_32x32x16_bf16 v[48:63], v[32:35], v[196:199], v[48:63]\n"
      "ds_read_b64_tr_b16 v[108:109], v15 offset:0\n"
      "ds_read_b64_tr_b16 v[110:111], v15 offset:768\n"
      "v_cmp_eq_u32 vcc, 16, v190\n"
      "v_mov_b32 v188, 0x3f803f80\n"
      "v_lshlrev_b32 v189, 4, v189\n"
      "s_add_u32 m0, %[sg], 1024\n"
      "v_mfma_f32_32x32x16_bf16 v[64:79], v[36:39], v[196:199], v[64:79]\n"
      "buffer_load_dwordx4 v9, %[srdg], s70 offen lds\n"
      "ds_read_b64_tr_b16 v[112:113], v15 offset:32\n"
      "ds_read_b64_tr_b16 v[114:115], v15 offset:800\n"
      "v_cndmask_b32 v164, 0, v188, vcc\n"
      "v_lshrrev_b32 v190, 9, v189\n"
      "v_lshlrev_b32 v190, 8, v190\n"
      "v_mfma_f32_32x32x16_bf16 v[80:95], v[40:43], v[196:199], v[80:95]\n"
      "ds_read_b64_tr_b16 v[116:117], v15 offset:64\n"
      "ds_read_b64_tr_b16 v[118:119], v15 offset:832\n"
      "v_add_u32 v15, 28672, v15\n"
      "v_and_b32 v188, 0xf0, v189\n"
      "v_add3_u32 v190, v190, v188, s40\n"
      "v_mov_b32 v188, 0xdddddddd\n"
      "s_waitcnt vmcnt(5)\n"
      "s_barrier\n"
      "s_waitcnt lgkmcnt(6)\n"
      "v_mfma_f32_32x32x16_bf16 v[48:63], v[96:99], v[200:203], v[48:63]\n"
      "ds_read_b64_tr_b16 v[32:33], v12 offset:0\n"
      "ds_read_b64_tr_b16 v[34:35], v12 offset:768\n"
      "v_mov_b32 v165, v164\n"
      "v_mov_b32 v166, v164\n"
      "v_mov_b32 v167, v164\n"
      "v_mov_b32 v168, v164\n"
      "s_add_u32 m0, %[sg], 2048\n"
      "v_mfma_f32_32x32x16_bf16 v[64:79], v[100:103], v[200:203], v[64:79]\n"
      "buffer_load_dwordx4 v10, %[srdg], s70 offen lds\n"
      "ds_read_b64_tr_b16 v[36:37], v12 offset:32\n"
      "ds_read_b64_tr_b16 v[38:39], v12 offset:800\n"
      "v_mov_b32 v169, v164\n"
      "v_mov_b32 v170, v164\n"
      "v_mov_b32 v171, v164\n"
      "v_mov_b32 v172, v164\n"
      "v_mfma_f32_32x32x16_bf16 v[80:95], v[104:107], v[200:203], v[80:95]\n"
      "ds_read_b64_tr_b16 v[40:41], v12 offset:64\n"
      "ds_read_b64_tr_b16 v[42:43], v12 offset:832\n"
      "v_add_u32 v12, 28672, v12\n"
      "v_mov_b32 v173, v164\n"
      "v_mov_b32 v174, v164\n"
      "v_mov_b32 v175, v164\n"
      "v_mov_b32 v176, v164\n"
      "s_cmp_lg_u32 %[wid], 0\n"
      "s_cbranch_scc1 1f\n"
      "s_mov_b64 exec, 1\n"
      "v_mov_b32 v4, s42\n"
      "v_mov_b32 v5, s43\n"
      "v_mov_b32 v6, s46\n"
      "v_subrev_u32 v6, 32768, v6\n"
      "buffer_store_dwordx2 v[4:5], v6, %[srd], 0 offen sc1\n"
      "s_mov_b64 exec, -1\n"
      "1:\n"
      "s_waitcnt lgkmcnt(6)\n"
      "v_mfma_f32_32x32x16_bf16 v[48:63], v[108:111], v[204:207], v[48:63]\n"
      "ds_read_b64_tr_b16 v[96:97], v13 offset:0\n"
      "ds_read_b64_tr_b16 v[98:99], v13 offset:768\n"
      "v_mov_b32 v177, v164\n"
      "v_mov_b32 v178, v164\n"
      "v_mov_b32 v179, v164\n"
      "v_mov_b32 v180, v164\n"
      "v_mfma_f32_32x32x16_bf16 v[64:79], v[112:115], v[204:207], v[64:79]\n"
      "ds_read_b64_tr_b16 v[100:101], v13 offset:32\n"
      "ds_read_b64_tr_b16 v[102:103], v13 offset:800\n"
      "v_mov_b32 v181, v164\n"
      "v_mov_b32 v182, v164\n"
      "v_mov_b32 v183, v164\n"
      "v_mov_b32 v184, v164\n"
      "v_mfma_f32_32x32x16_bf16 v[80:95], v[116:119], v[204:207], v[80:95]\n"
      "ds_read_b64_tr_b16 v[104:105], v13 offset:64\n"
      "ds_read_b64_tr_b16 v[106:107], v13 offset:832\n"
      "v_add_u32 v13, 28672, v13\n"
      "v_mov_b32 v185, v164\n"
      "v_mov_b32 v186, v164\n"
      "v_mov_b32 v187, v164\n"
      "s_waitcnt lgkmcnt(6)\n"
      "v_mfma_f32_32x32x16_bf16 v[48:63], v[32:35], v[208:211], v[48:63]\n"
      "ds_read_b64_tr_b16 v[108:109], v14 offset:0\n"
      "ds_read_b64_tr_b16 v[110:111], v14 offset:768\n"
      "v_mfma_f32_16x16x32_bf16 v[16:19], v[28:31], v[32:35], v[16:19]\n"
      "v_mfma_f32_32x32x16_bf16 v[64:79], v[36:39], v[208:211], v[64:79]\n"
      "ds_read_b64_tr_b16 v[112:113], v14 offset:32\n"
      "ds_read_b64_tr_b16 v[114:115], v14 offset:800\n"
      "v_mfma_f32_16x16x32_bf16 v[20:23], v[28:31], v[36:39], v[20:23]\n"
      "v_mfma_f32_32x32x16_bf16 v[80:95], v[40:43], v[208:211], v[80:95]\n"
      "ds_read_b64_tr_b16 v[116:117], v14 offset:64\n"
      "ds_read_b64_tr_b16 v[118:119], v14 offset:832\n"
      "v_add_u32 v14, 28672, v14\n"
      "v_mfma_f32_16x16x32_bf16 v[24:27], v[28:31], v[40:43], v[24:27]\n"
      "s_waitcnt lgkmcnt(6)\n"
      "v_mfma_f32_32x32x16_bf16 v[48:63], v[96:99], v[212:215], v[48:63]\n"
      "ds_read_b64_tr_b16 v[32:33], v15 offset:0\n"
      "ds_read_b64_tr_b16 v[34:35], v15 offset:768\n"
      "v_mfma_f32_32x32x16_bf16 v[64:79], v[100:103], v[212:215], v[64:79]\n"
      "ds_read_b64_tr_b16 v[36:37], v15 offset:32\n"
      "ds_read_b64_tr_b16 v[38:39], v15 offset:800\n"
      "v_mfma_f32_32x32x16_bf16 v[80:95], v[104:107], v[212:215], v[80:95]\n"
      "ds_read_b64_tr_b16 v[40:41], v15 offset:64\n"
      "ds_read_b64_tr_b16 v[42:43], v15 offset:832\n"
      "v_add_u32 v15, 28672, v15\n"
      "s_cmp_eq_u32 %[wid], 0\n"
      "s_cbranch_scc1 5f\n"
      "s_waitcnt vmcnt(0)\n"
      "5:\n"
      "s_waitcnt vmcnt(1)\n"
      "s_barrier\n"
      "s_waitcnt lgkmcnt(6)\n"
      "v_mfma_f32_32x32x16_bf16 v[48:63], v[108:111], v[216:219], v[48:63]\n"
      "ds_read_b64_tr_b16 v[96:97], v12 offset:0\n"
      "ds_read_b64_tr_b16 v[98:99], v12 offset:768\n"
      "v_mfma_f32_32x32x16_bf16 v[64:79], v[112:115], v[216:219], v[64:79]\n"
      "ds_read_b64_tr_b16 v[100:101], v12 offset:32\n"
      "ds_read_b64_tr_b16 v[102:103], v12 offset:800\n"
      "v_mfma_f32_32x32x16_bf16 v[80:95], v[116:119], v[216:219], v[80:95]\n"
      "ds_read_b64_tr_b16 v[104:105], v12 offset:64\n"
      "ds_read_b64_tr_b16 v[106:107], v12 offset:832\n"
      "v_add_u32 v12, 4294852608, v12\n"
      "s_waitcnt lgkmcnt(6)\n"
      "v_mfma_f32_32x32x16_bf16 v[48:63], v[32:35], v[220:223], v[48:63]\n"
      "ds_read_b64_tr_b16 v[108:109], v13 offset:0\n"
      "ds_read_b64_tr_b16 v[110:111], v13 offset:768\n"
      "v_mfma_f32_32x32x16_bf16 v[64:79], v[36:39], v[220:223], v[64:79]\n"
      "ds_read_b64_tr_b16 v[112:113], v13 offset:32\n"
      "ds_read_b64_tr_b16 v[114:115], v13 offset:800\n"
      "v_mfma_f32_32x32x16_bf16 v[80:95], v[40:43], v[220:223], v[80:95]\n"
      "ds_read_b64_tr_b16 v[116:117], v13 offset:64\n"
      "ds_read_b64_tr_b16 v[118:119], v13 offset:832\n"
      "v_add_u32 v13, 4294852608, v13\n"
      "s_cmp_lg_u32 %[ri], 0\n"
      "s_cbranch_scc1 7f\n"
      "buffer_load_dwordx2 v[2:3], v11, %[srd], 0 offen sc1\n"
      "7:\n"
      "s_waitcnt lgkmcnt(6)\n"
      "v_mfma_f32_32x32x16_bf16 v[48:63], v[96:99], v[224:227], v[48:63]\n"
      "ds_read_b64_tr_b16 v[32:33], v14 offset:0\n"
      "ds_read_b64_tr_b16 v[34:35], v14 offset:768\n"
      "v_mfma_f32_16x16x32_bf16 v[16:19], v[28:31], v[96:99], v[16:19]\n"
      "v_mfma_f32_32x32x16_bf16 v[64:79], v[100:103], v[224:227], v[64:79]\n"
      "ds_read_b64_tr_b16 v[36:37], v14 offset:32\n"
      "ds_read_b64_tr_b16 v[38:39], v14 offset:800\n"
      "v_mfma_f32_16x16x32_bf16 v[20:23], v[28:31], v[100:103], v[20:23]\n"
      "v_mfma_f32_32x32x16_bf16 v[80:95], v[104:107], v[224:227], v[80:95]\n"
      "ds_read_b64_tr_b16 v[40:41], v14 offset:64\n"
      "ds_read_b64_tr_b16 v[42:43], v14 offset:832\n"
      "v_add_u32 v14, 4294852608, v14\n"
      "v_mfma_f32_16x16x32_bf16 v[24:27], v[28:31], v[104:107], v[24:27]\n"
      "s_waitcnt lgkmcnt(6)\n"
      "v_mfma_f32_32x32x16_bf16 v[48:63], v[108:111], v[228:231], v[48:63]\n"
      "ds_read_b64_tr_b16 v[96:97], v15 offset:0\n"
      "ds_read_b64_tr_b16 v[98:99], v15 offset:768\n"
      "v_mfma_f32_32x32x16_bf16 v[64:79], v[112:115], v[228:231], v[64:79]\n"
      "ds_read_b64_tr_b16 v[100:101], v15 offset:32\n"
      "ds_read_b64_tr_b16 v[102:103], v15 offset:800\n"
      "v_mfma_f32_32x32x16_bf16 v[80:95], v[116:119], v[228:231], v[80:95]\n"
      "ds_read_b64_tr_b16 v[104:105], v15 offset:64\n"
      "ds_read_b64_tr_b16 v[106:107], v15 offset:832\n"
      "v_add_u32 v15, 4294852608, v15\n"
      "s_cmp_lg_u32 %[ri], 0\n"
      "s_cbranch_scc1 7f\n"
      "s_waitcnt vmcnt(0)\n"
      "s_mov_b32 s73, 0\n"
      "2:\n"
      "v_cmp_ne_u64 vcc, s[42:43], v[2:3]\n"
      "s_cbranch_vccz 3f\n"
      "s_add_u32 s73, s73, 1\n"
      "s_cmp_gt_u32 s73, 0x100000\n"
      "s_cbranch_scc1 3f\n"
      "s_sleep 1\n"
      "buffer_load_dwordx2 v[2:3], v11, %[srd], 0 offen sc1\n"
      "s_waitcnt vmcnt(0)\n"
      "s_branch 2b\n"
      "3:\n"
      "s_or_b32 s73, s37, s36\n"
      "buffer_load_dwordx4 v[136:139], v189, %[srd], s73 offen sc1\n"
      "s_add_u32 s73, s37, 1024\n"
      "s_and_b32 s73, s73, 0xfff\n"
      "s_or_b32 s73, s73, s36\n"
      "buffer_load_dwordx4 v[140:143], v189, %[srd], s73 offen sc1\n"
      "7:\n"
      "s_waitcnt lgkmcnt(6)\n"
      "v_mfma_f32_32x32x16_bf16 v[48:63], v[32:35], v[232:235], v[48:63]\n"
      "ds_read_b64_tr_b16 v[108:109], v12 offset:0\n"
      "ds_read_b64_tr_b16 v[110:111], v12 offset:768\n"
      "v_mfma_f32_32x32x16_bf16 v[64:79], v[36:39], v[232:235], v[64:79]\n"
      "ds_read_b64_tr_b16 v[112:113], v12 offset:32\n"
      "ds_read_b64_tr_b16 v[114:115], v12 offset:800\n"
      "v_mfma_f32_32x32x16_bf16 v[80:95], v[40:43], v[232:235], v[80:95]\n"
      "ds_read_b64_tr_b16 v[116:117], v12 offset:64\n"
      "ds_read_b64_tr_b16 v[118:119], v12 offset:832\n"
      "s_cmp_lg_u32 %[ri], 0\n"
      "s_cbranch_scc1 7f\n"
      "s_add_u32 s73, s37, 2048\n"
      "s_and_b32 s73, s73, 0xfff\n"
      "s_or_b32 s73, s73, s36\n"
      "buffer_load_dwordx4 v[144:147], v189, %[srd], s73 offen sc1\n"
      "s_add_u32 s73, s37, 3072\n"
      "s_and_b32 s73, s73, 0xfff\n"
      "s_or_b32 s73, s73, s36\n"
      "buffer_load_dwordx4 v[148:151], v189, %[srd], s73 offen sc1\n"
      "7:\n"
      "s_waitcnt lgkmcnt(6)\n"
      "v_mfma_f32_32x32x16_bf16 v[48:63], v[96:99], v[236:239], v[48:63]\n"
      "ds_read_b64_tr_b16 v[32:33], v13 offset:0\n"
      "ds_read_b64_tr_b16 v[34:35], v13 offset:768\n"
      "v_mfma_f32_32x32x16_bf16 v[64:79], v[100:103], v[236:239], v[64:79]\n"
      "ds_read_b64_tr_b16 v[36:37], v13 offset:32\n"
      "ds_read_b64_tr_b16 v[38:39], v13 offset:800\n"
      "v_mfma_f32_32x32x16_bf16 v[80:95], v[104:107], v[236:239], v[80:95]\n"
      "ds_read_b64_tr_b16 v[40:41], v13 offset:64\n"
      "ds_read_b64_tr_b16 v[42:43], v13 offset:832\n"
      "s_cmp_lg_u32 %[ri], 0\n"
      "s_cbranch_scc1 7f\n"
      "buffer_load_dwordx4 v[132:135], v189, %[srd], s38 offen sc1\n"
      "buffer_load_dwordx4 v[152:155], v189, %[srd], s39 offen sc1\n"
      "7:\n"
      "s_waitcnt lgkmcnt(6)\n"
      "v_mfma_f32_32x32x16_bf16 v[48:63], v[108:111], v[240:243], v[48:63]\n"
      "ds_read_b64_tr_b16 v[96:97], v14 offset:0\n"
      "ds_read_b64_tr_b16 v[98:99], v14 offset:768\n"
      "v_mfma_f32_16x16x32_bf16 v[16:19], v[28:31], v[108:111], v[16:19]\n"
      "v_mfma_f32_32x32x16_bf16 v[64:79], v[112:115], v[240:243], v[64:79]\n"
      "ds_read_b64_tr_b16 v[100:101], v14 offset:32\n"
      "ds_read_b64_tr_b16 v[102:103], v14 offset:800\n"
      "v_mfma_f32_16x16x32_bf16 v[20:23], v[28:31], v[112:115], v[20:23]\n"
      "v_mfma_f32_32x32x16_bf16 v[80:95], v[116:119], v[240:243], v[80:95]\n"
      "ds_read_b64_tr_b16 v[104:105], v14 offset:64\n"
      "ds_read_b64_tr_b16 v[106:107], v14 offset:832\n"
      "v_mfma_f32_16x16x32_bf16 v[24:27], v[28:31], v[116:119], v[24:27]\n"
      "s_cmp_lg_u32 %[ri], 0\n"
      "s_cbranch_scc1 7f\n"
      "s_mov_b64 exec, s[44:45]\n"
      "buffer_load_dwordx4 v[164:167], v190, %[srd], s39 offen offset:0 sc1\n"
      "buffer_load_dwordx4 v[168:171], v190, %[srd], s39 offen offset:512 sc1\n"
      "s_mov_b64 exec, -1\n"
      "7:\n"
      "s_waitcnt lgkmcnt(6)\n"
      "v_mfma_f32_32x32x16_bf16 v[48:63], v[32:35], v[244:247], v[48:63]\n"
      "ds_read_b64_tr_b16 v[108:109], v15 offset:0\n"
      "ds_read_b64_tr_b16 v[110:111], v15 offset:768\n"
      "v_mfma_f32_32x32x16_bf16 v[64:79], v[36:39], v[244:247], v[64:79]\n"
      "ds_read_b64_tr_b16 v[112:113], v15 offset:32\n"
      "ds_read_b64_tr_b16 v[114:115], v15 offset:800\n"
      "v_mfma_f32_32x32x16_bf16 v[80:95], v[40:43], v[244:247], v[80:95]\n"
      "ds_read_b64_tr_b16 v[116:117], v15 offset:64\n"
      "ds_read_b64_tr_b16 v[118:119], v15 offset:832\n"
      "s_cmp_lg_u32 %[ri], 0\n"
      "s_cbranch_scc1 7f\n"
      "buffer_load_dwordx4 v[156:159], v189, %[srd], s39 offen offset:1024 sc1\n"
      "s_mov_b64 exec, s[44:45]\n"
      "buffer_load_dwordx4 v[172:175], v190, %[srd], s39 offen offset:1024 sc1\n"
      "buffer_load_dwordx4 v[176:179], v190, %[srd], s39 offen offset:1536 sc1\n"
      "s_mov_b64 exec, -1\n"
      "7:\n"
      "s_waitcnt lgkmcnt(6)\n"
      "v_mfma_f32_32x32x16_bf16 v[48:63], v[96:99], v[248:251], v[48:63]\n"
      "v_mfma_f32_32x32x16_bf16 v[64:79], v[100:103], v[248:251], v[64:79]\n"
      "v_mfma_f32_32x32x16_bf16 v[80:95], v[104:107], v[248:251], v[80:95]\n"
      "s_cmp_lg_u32 %[ri], 0\n"
      "s_cbranch_scc1 7f\n"
      "buffer_load_dwordx4 v[160:163], v189, %[srd], s39 offen offset:2048 sc1\n"
      "s_mov_b64 exec, s[44:45]\n"
      "buffer_load_dwordx4 v[180:183], v190, %[srd], s39 offen offset:2048 sc1\n"
      "buffer_load_dwordx4 v[184:187], v190, %[srd], s39 offen offset:2560 sc1\n"
      "s_mov_b64 exec, -1\n"
      "7:\n"
      "s_waitcnt lgkmcnt(0)\n"
      "v_mfma_f32_32x32x16_bf16 v[48:63], v[108:111], v[252:255], v[48:63]\n"
      "v_mfma_f32_32x32x16_bf16 v[64:79], v[112:115], v[252:255], v[64:79]\n"
      "v_mfma_f32_32x32x16_bf16 v[80:95], v[116:119], v[252:255], v[80:95]\n"
      "s_setprio 3\n"
      "s_cmp_lg_u32 s41, 0\n"
      "s_cbranch_scc1 6f\n"
      "s_waitcnt vmcnt(9)\n"
      "v_mfma_f32_32x32x16_bf16 v[192:207], v[132:135], v[136:139], 0\n"
      "v_mfma_f32_32x32x16_bf16 v[208:223], v[132:135], v[140:143], 0\n"
      "v_mfma_f32_32x32x16_bf16 v[224:239], v[132:135], v[144:147], 0\n"
      "v_mfma_f32_32x32x16_bf16 v[240:255], v[132:135], v[148:151], 0\n"
      "s_nop 8\n"
      "v_max3_f32 v4, v192, v193, v194\n"
      "v_max3_f32 v4, v4, v195, v196\n"
      "v_max3_f32 v4, v4, v197, v198\n"
      "v_max3_f32 v4, v4, v199, v200\n"
      "v_max3_f32 v4, v4, v201, v202\n"
      "v_max3_f32 v4, v4, v203, v204\n"
      "v_max3_f32 v4, v4, v205, v206\n"
      "v_max_f32 v4, v4, v207\n"
      "v_mov_b32 v192, v4\n"
      "v_max3_f32 v5, v208, v209, v210\n"
      "v_max3_f32 v5, v5, v211, v212\n"
      "v_max3_f32 v5, v5, v213, v214\n"
      "v_max3_f32 v5, v5, v215, v216\n"
      "v_max3_f32 v5, v5, v217, v218\n"
      "v_max3_f32 v5, v5, v219, v220\n"
      "v_max3_f32 v5, v5, v221, v222\n"
      "v_max_f32 v5, v5, v223\n"
      "v_mov_b32 v208, v5\n"
      "v_max3_f32 v6, v224, v225, v226\n"
      "v_max3_f32 v6, v6, v227, v228\n"
      "v_max3_f32 v6, v6, v229, v230\n"
      "v_max3_f32 v6, v6, v231, v232\n"
      "v_max3_f32 v6, v6, v233, v234\n"
      "v_max3_f32 v6, v6, v235, v236\n"
      "v_max3_f32 v6, v6, v237, v238\n"
      "v_max_f32 v6, v6, v239\n"
      "v_mov_b32 v224, v6\n"
      "v_max3_f32 v7, v240, v241, v242\n"
      "v_max3_f32 v7, v7, v243, v244\n"
      "v_max3_f32 v7, v7, v245, v246\n"
      "v_max3_f32 v7, v7, v247, v248\n"
      "v_max3_f32 v7, v7, v249, v250\n"
      "v_max3_f32 v7, v7, v251, v252\n"
      "v_max3_f32 v7, v7, v253, v254\n"
      "v_max_f32 v7, v7, v255\n"
      "v_mov_b32 v240, v7\n"
      "s_nop 1\n"
      "v_permlane32_swap_b32 v192, v4\n"
      "v_permlane32_swap_b32 v208, v5\n"
      "v_permlane32_swap_b32 v224, v6\n"
      "v_permlane32_swap_b32 v240, v7\n"
      "s_nop 1\n"
      "v_max_f32 v4, v4, v192\n"
      "v_max_f32 v5, v5, v208\n"
      "v_max_f32 v6, v6, v224\n"
      "v_max_f32 v7, v7, v240\n"
      "v_sub_f32 v4, 0xc2c80000, v4\n"
      "v_sub_f32 v5, 0xc2c80000, v5\n"
      "v_sub_f32 v6, 0xc2c80000, v6\n"
      "v_sub_f32 v7, 0xc2c80000, v7\n"
      "6:\n"
      "s_nop 15\n"
      : "=&{v[48:63]}"(a2), "=&{v[64:79]}"(a3), "=&{v[80:95]}"(a4), "=&{v[16:23]}"(gcs), "=&{v[24:27]}"(gcs2), "+{v[12:15]}"(wq),
        "=&{s[36:51]}"(pa), "=&{s[52:67]}"(pb), "+{v[128:143]}"(x0), "+{v[144:159]}"(x1), "+{v[160:175]}"(x2), "+{v[176:191]}"(x3),
        "+{v[192:207]}"(x4), "+{v[208:223]}"(x5), "+{v[224:239]}"(x6), "+{v[240:255]}"(x7), "=&{v[4:7]}"(nm)
      : [srdg] "s"(srdg), "{v[8:11]}"(xa), "{v[28:31]}"(pat2), [rot] "s"(rot), [sg] "s"(sg), [wid] "s"(w), [srd] "s"(wsr),
        [srdwarm] "s"(wrr), [gi] "s"(g), [ji] "s"(j), [ti] "s"(t), [ri] "s"(role), [ep0] "s"(ep0), [ep1] "s"(ep1)
      : "memory", "scc", "vcc", "v2", "v3", "v32", "v33", "v34", "v35", "v36", "v37", "v38", "v39", "v40", "v41", "v42", "v43", "v96", "v97", "v98", "v99", "v100", "v101", "v102", "v103", "v104", "v105", "v106", "v107", "v108", "v109", "v110", "v111", "v112", "v113", "v114", "v115", "v116", "v117", "v118", "v119", "s68", "s69", "s70", "s71", "s72", "s73");
    STAMP(3); RSTAMP(2);
    RELANE();
    const u32 ca = 2 * SLOT + role * 1536 + r31 * 16;
    *(LAS float*)(uintptr_t)(ca + t * 4) = gcs[0];
    *(LAS float*)(uintptr_t)(ca + 512 + t * 4) = gcs[4];
    *(LAS float*)(uintptr_t)(ca + 1024 + t * 4) = gcs2[0];
    wait_bar<63>();
    const u32 unit = U0 + 3 * role, hso = WS_H + (tile32 * 86 + unit) * 1024;
    const u32x4 c0 = cs_frag(ca), c1 = cs_frag(ca + 512), c2 = cs_frag(ca + 1024);
    a2 = mfma32(c0, muf, a2); a3 = mfma32(c1, muf, a3); a4 = mfma32(c2, muf, a4);
    PIN(a2); PIN(a3); PIN(a4);
    st_sc1(wsr, lane * 16, unit >= 6 * j ? hso : WS_END, geglu(a2, rstd));
    st_sc1(wsr, lane * 16, unit + 1 >= 6 * j ? hso + 1024 : WS_END, geglu(a3, rstd));
    st_sc1(wsr, lane * 16, unit + 2 >= 6 * j ? hso + 2048 : WS_END, geglu(a4, rstd));
  }
  STAMP(4); RSTAMP(3);
  {
    const rsrc_t wdr = __builtin_amdgcn_make_buffer_rsrc((void*)wd, 0, 1376 * 1024, 0x00020000);
    const rsrc_t wor = __builtin_amdgcn_make_buffer_rsrc((void*)wo, 0, 512 * 1024, 0x00020000);
    const rsrc_t outr = __builtin_amdgcn_make_buffer_rsrc((void*)out, 0, 2048 * 1024, 0x00020000);
    u32 apub = 0;
    for (u32 pass = 0;; ++pass) {
      u32 bad;
      asm volatile(
      "s_mov_b32 s68, s39\n"
      "s_mov_b32 s70, 0\n"
      "s_setprio 2\n"
      "s_cmp_lg_u32 %[usem], 0\n"
      "s_cbranch_scc1 8f\n"
      "s_cmp_eq_u32 s41, 0\n"
      "s_cbranch_scc1 2f\n"
      "s_mov_b32 s69, 0\n"
      "12:\n"
      "buffer_load_dwordx2 v[8:9], v191, %[srd], 0 offen sc1\n"
      "s_waitcnt vmcnt(0)\n"
      "v_cmp_ne_u64 vcc, s[42:43], v[8:9]\n"
      "s_cbranch_vccz 13f\n"
      "s_add_u32 s69, s69, 1\n"
      "s_cmp_gt_u32 s69, 0x100000\n"
      "s_cbranch_scc1 13f\n"
      "s_sleep 1\n"
      "s_branch 12b\n"
      "13:\n"
      "s_or_b32 s69, s37, s36\n"
      "buffer_load_dwordx4 v[136:139], v189, %[srd], s69 offen sc1\n"
      "s_add_u32 s69, s37, 1024\n"
      "s_and_b32 s69, s69, 0xfff\n"
      "s_or_b32 s69, s69, s36\n"
      "buffer_load_dwordx4 v[140:143], v189, %[srd], s69 offen sc1\n"
      "s_add_u32 s69, s37, 2048\n"
      "s_and_b32 s69, s69, 0xfff\n"
      "s_or_b32 s69, s69, s36\n"
      "buffer_load_dwordx4 v[144:147], v189, %[srd], s69 offen sc1\n"
      "s_add_u32 s69, s37, 3072\n"
      "s_and_b32 s69, s69, 0xfff\n"
      "s_or_b32 s69, s69, s36\n"
      "buffer_load_dwordx4 v[148:151], v189, %[srd], s69 offen sc1\n"
      "buffer_load_dwordx4 v[132:135], v189, %[srd], s38 offen sc1\n"
      "buffer_load_dwordx4 v[152:155], v189, %[srd], s39 offen sc1\n"
      "s_mov_b64 exec, s[44:45]\n"
      "buffer_load_dwordx4 v[164:167], v190, %[srd], s39 offen offset:0 sc1\n"
      "buffer_load_dwordx4 v[168:171], v190, %[srd], s39 offen offset:512 sc1\n"
      "s_mov_b64 exec, -1\n"
      "buffer_load_dwordx4 v[156:159], v189, %[srd], s39 offen offset:1024 sc1\n"
      "s_mov_b64 exec, s[44:45]\n"
      "buffer_load_dwordx4 v[172:175], v190, %[srd], s39 offen offset:1024 sc1\n"
      "buffer_load_dwordx4 v[176:179], v190, %[srd], s39 offen offset:1536 sc1\n"
      "s_mov_b64 exec, -1\n"
      "buffer_load_dwordx4 v[160:163], v189, %[srd], s39 offen offset:2048 sc1\n"
      "s_mov_b64 exec, s[44:45]\n"
      "buffer_load_dwordx4 v[180:183], v190, %[srd], s39 offen offset:2048 sc1\n"
      "buffer_load_dwordx4 v[184:187], v190, %[srd], s39 offen offset:2560 sc1\n"
      "s_mov_b64 exec, -1\n"
      "v_bfe_u32 v0, v189, 6, 2\n"
      "v_lshrrev_b32 v1, 9, v189\n"
      "v_lshl_add_u32 v1, v1, 2, v0\n"
      "v_lshlrev_b32 v1, 7, v1\n"
      "v_lshrrev_b32 v0, 1, v0\n"
      "v_lshl_or_b32 v1, v0, 6, v1\n"
      "v_bfe_u32 v0, v189, 8, 1\n"
      "v_lshl_or_b32 v1, v0, 5, v1\n"
      "v_bfe_u32 v0, v189, 4, 2\n"
      "v_lshl_or_b32 v1, v0, 3, v1\n"
      "v_add_u32 v0, s58, v1\n"
      "v_xor_b32 v1, 64, v0\n"
      "v_lshrrev_b32 v8, 7, v189\n"
      "v_and_b32 v2, 0x70, v189\n"
      "v_lshl_or_b32 v3, v8, 10, v2\n"
      "v_add_u32 v3, s63, v3\n"
      "v_add_u32 v8, s67, v8\n"
      "v_mul_u32_u24 v8, 272, v8\n"
      "v_lshl_add_u32 v2, v2, 1, v8\n"
      "s_mov_b64 exec, 1\n"
      "v_mov_b32 v72, 163776\n"
      "v_mov_b32 v73, 1\n"
      "ds_add_rtn_u32 v73, v72, v73\n"
      "v_mov_b32 v74, s42\n"
      "v_mov_b32 v75, s43\n"
      "v_mov_b32 v72, s46\n"
      "s_waitcnt lgkmcnt(0)\n"
      "v_readfirstlane_b32 s69, v73\n"
      "s_cmp_lg_u32 s69, 7\n"
      "s_cbranch_scc1 5f\n"
      "buffer_store_dwordx2 v[74:75], v72, %[srd], 0 offen sc1\n"
      "5:\n"
      "s_mov_b64 exec, -1\n"
      "s_setprio 1\n"
      "6:\n"
      "s_waitcnt vmcnt(0)\n"
      "v_mfma_f32_32x32x16_bf16 v[8:23], v[132:135], v[136:139], 0\n"
      "v_mfma_f32_32x32x16_bf16 v[24:39], v[132:135], v[140:143], 0\n"
      "v_mfma_f32_32x32x16_bf16 v[40:55], v[132:135], v[144:147], 0\n"
      "v_mfma_f32_32x32x16_bf16 v[56:71], v[132:135], v[148:151], 0\n"
      "s_nop 8\n"
      "v_max3_f32 v4, v8, v9, v10\n"
      "v_max3_f32 v4, v4, v11, v12\n"
      "v_max3_f32 v4, v4, v13, v14\n"
      "v_max3_f32 v4, v4, v15, v16\n"
      "v_max3_f32 v4, v4, v17, v18\n"
      "v_max3_f32 v4, v4, v19, v20\n"
      "v_max3_f32 v4, v4, v21, v22\n"
      "v_max_f32 v4, v4, v23\n"
      "v_mov_b32 v8, v4\n"
      "v_max3_f32 v5, v24, v25, v26\n"
      "v_max3_f32 v5, v5, v27, v28\n"
      "v_max3_f32 v5, v5, v29, v30\n"
      "v_max3_f32 v5, v5, v31, v32\n"
      "v_max3_f32 v5, v5, v33, v34\n"
      "v_max3_f32 v5, v5, v35, v36\n"
      "v_max3_f32 v5, v5, v37, v38\n"
      "v_max_f32 v5, v5, v39\n"
      "v_mov_b32 v24, v5\n"
      "v_max3_f32 v6, v40, v41, v42\n"
      "v_max3_f32 v6, v6, v43, v44\n"
      "v_max3_f32 v6, v6, v45, v46\n"
      "v_max3_f32 v6, v6, v47, v48\n"
      "v_max3_f32 v6, v6, v49, v50\n"
      "v_max3_f32 v6, v6, v51, v52\n"
      "v_max3_f32 v6, v6, v53, v54\n"
      "v_max_f32 v6, v6, v55\n"
      "v_mov_b32 v40, v6\n"
      "v_max3_f32 v7, v56, v57, v58\n"
      "v_max3_f32 v7, v7, v59, v60\n"
      "v_max3_f32 v7, v7, v61, v62\n"
      "v_max3_f32 v7, v7, v63, v64\n"
      "v_max3_f32 v7, v7, v65, v66\n"
      "v_max3_f32 v7, v7, v67, v68\n"
      "v_max3_f32 v7, v7, v69, v70\n"
      "v_max_f32 v7, v7, v71\n"
      "v_mov_b32 v56, v7\n"
      "s_nop 1\n"
      "v_permlane32_swap_b32 v8, v4\n"
      "v_permlane32_swap_b32 v24, v5\n"
      "v_permlane32_swap_b32 v40, v6\n"
      "v_permlane32_swap_b32 v56, v7\n"
      "s_nop 1\n"
      "v_max_f32 v4, v4, v8\n"
      "v_max_f32 v5, v5, v24\n"
      "v_max_f32 v6, v6, v40\n"
      "v_max_f32 v7, v7, v56\n"
      "v_sub_f32 v4, 0xc2c80000, v4\n"
      "v_sub_f32 v5, 0xc2c80000, v5\n"
      "v_sub_f32 v6, 0xc2c80000, v6\n"
      "v_sub_f32 v7, 0xc2c80000, v7\n"
      "2:\n"
      "v_cmp_gt_u32 vcc, 0x200, v189\n"
      "v_mov_b32 v56, 0\n"
      "v_mov_b32 v57, 0\n"
      "v_mov_b32 v58, 0\n"
      "v_mov_b32 v59, 0\n"
      "v_cndmask_b32 v191, 0, 1.0, vcc\n"
      "s_nop 1\n"
      "v_mfma_f32_32x32x2_f32 v[72:87], v191, v4, 0\n"
      "s_waitcnt vmcnt(11)\n"
      "v_mfma_f32_32x32x16_bf16 v[8:23], v[152:155], v[136:139], v[72:87]\n"
      "v_mfma_f32_32x32x2_f32 v[88:103], v191, v5, 0\n"
      "v_mfma_f32_32x32x16_bf16 v[24:39], v[152:155], v[140:143], v[88:103]\n"
      "v_mfma_f32_32x32x2_f32 v[104:119], v191, v6, 0\n"
      "v_mfma_f32_32x32x16_bf16 v[192:207], v[56:59], v[56:59], 0\n"
      "v_mfma_f32_32x32x16_bf16 v[208:223], v[56:59], v[56:59], 0\n"
      "v_mfma_f32_32x32x16_bf16 v[224:239], v[56:59], v[56:59], 0\n"
      "v_mfma_f32_32x32x16_bf16 v[240:255], v[56:59], v[56:59], 0\n"
      "v_mfma_f32_32x32x2_f32 v[120:135], v191, v7, 0\n"
      "s_nop 3\n"
      "v_exp_f32 v8, v8\n"
      "v_exp_f32 v9, v9\n"
      "v_exp_f32 v10, v10\n"
      "v_exp_f32 v11, v11\n"
      "v_exp_f32 v12, v12\n"
      "v_exp_f32 v13, v13\n"
      "v_exp_f32 v14, v14\n"
      "v_mfma_f32_32x32x16_bf16 v[40:55], v[152:155], v[144:147], v[104:119]\n"
      "v_exp_f32 v15, v15\n"
      "v_exp_f32 v16, v16\n"
      "v_exp_f32 v17, v17\n"
      "v_exp_f32 v18, v18\n"
      "v_exp_f32 v19, v19\n"
      "v_exp_f32 v20, v20\n"
      "s_waitcnt vmcnt(9)\n"
      "v_smfmac_f32_32x32x32_bf16 v[192:207], v[164:167], v[8:15], v188\n"
      "v_exp_f32 v21, v21\n"
      "v_exp_f32 v22, v22\n"
      "v_exp_f32 v23, v23\n"
      "v_exp_f32 v24, v24\n"
      "v_exp_f32 v25, v25\n"
      "v_smfmac_f32_32x32x32_bf16 v[192:207], v[168:171], v[16:23], v188\n"
      "v_exp_f32 v26, v26\n"
      "v_exp_f32 v27, v27\n"
      "v_exp_f32 v28, v28\n"
      "v_exp_f32 v29, v29\n"
      "v_exp_f32 v30, v30\n"
      "v_mfma_f32_32x32x16_bf16 v[56:71], v[152:155], v[148:151], v[120:135]\n"
      "buffer_load_dwordx4 v[152:155], v189, %[srd], s68 offen offset:3072 sc1\n"
      "v_exp_f32 v31, v31\n"
      "v_exp_f32 v32, v32\n"
      "v_exp_f32 v33, v33\n"
      "v_exp_f32 v34, v34\n"
      "v_exp_f32 v35, v35\n"
      "v_exp_f32 v36, v36\n"
      "v_smfmac_f32_32x32x32_bf16 v[208:223], v[164:167], v[24:31], v188\n"
      "v_exp_f32 v37, v37\n"
      "v_exp_f32 v38, v38\n"
      "v_exp_f32 v39, v39\n"
      "v_exp_f32 v40, v40\n"
      "v_exp_f32 v41, v41\n"
      "v_smfmac_f32_32x32x32_bf16 v[208:223], v[168:171], v[32:39], v188\n"
      "v_exp_f32 v42, v42\n"
      "v_exp_f32 v43, v43\n"
      "v_exp_f32 v44, v44\n"
      "v_exp_f32 v45, v45\n"
      "v_exp_f32 v46, v46\n"
      "s_waitcnt vmcnt(9)\n"
      "v_mfma_f32_32x32x16_bf16 v[8:23], v[156:159], v[136:139], v[72:87]\n"
      "v_exp_f32 v47, v47\n"
      "v_exp_f32 v48, v48\n"
      "v_exp_f32 v49, v49\n"
      "v_exp_f32 v50, v50\n"
      "v_exp_f32 v51, v51\n"
      "v_exp_f32 v52, v52\n"
      "v_smfmac_f32_32x32x32_bf16 v[224:239], v[164:167], v[40:47], v188\n"
      "v_exp_f32 v53, v53\n"
      "v_exp_f32 v54, v54\n"
      "v_exp_f32 v55, v55\n"
      "v_exp_f32 v56, v56\n"
      "v_exp_f32 v57, v57\n"
      "v_smfmac_f32_32x32x32_bf16 v[224:239], v[168:171], v[48:55], v188\n"
      "v_exp_f32 v58, v58\n"
      "v_exp_f32 v59, v59\n"
      "v_exp_f32 v60, v60\n"
      "v_exp_f32 v61, v61\n"
      "v_exp_f32 v62, v62\n"
      "v_mfma_f32_32x32x16_bf16 v[24:39], v[156:159], v[140:143], v[88:103]\n"
      "v_exp_f32 v63, v63\n"
      "v_exp_f32 v64, v64\n"
      "v_exp_f32 v65, v65\n"
      "v_exp_f32 v66, v66\n"
      "v_exp_f32 v67, v67\n"
      "v_exp_f32 v68, v68\n"
      "v_smfmac_f32_32x32x32_bf16 v[240:255], v[164:167], v[56:63], v188\n"
      "v_exp_f32 v69, v69\n"
      "v_exp_f32 v70, v70\n"
      "v_exp_f32 v71, v71\n"
      "v_exp_f32 v8, v8\n"
      "v_exp_f32 v9, v9\n"
      "v_smfmac_f32_32x32x32_bf16 v[240:255], v[168:171], v[64:71], v188\n"
      "s_mov_b64 exec, s[44:45]\n"
      "buffer_load_dwordx4 v[164:167], v190, %[srd], s68 offen offset:3072 sc1\n"
      "buffer_load_dwordx4 v[168:171], v190, %[srd], s68 offen offset:3584 sc1\n"
      "s_mov_b64 exec, -1\n"
      "v_exp_f32 v10, v10\n"
      "v_exp_f32 v11, v11\n"
      "v_exp_f32 v12, v12\n"
      "v_exp_f32 v13, v13\n"
      "v_exp_f32 v14, v14\n"
      "v_mfma_f32_32x32x16_bf16 v[40:55], v[156:159], v[144:147], v[104:119]\n"
      "v_exp_f32 v15, v15\n"
      "v_exp_f32 v16, v16\n"
      "v_exp_f32 v17, v17\n"
      "v_exp_f32 v18, v18\n"
      "v_exp_f32 v19, v19\n"
      "v_exp_f32 v20, v20\n"
      "s_waitcnt vmcnt(9)\n"
      "v_smfmac_f32_32x32x32_bf16 v[192:207], v[172:175], v[8:15], v188\n"
      "v_exp_f32 v21, v21\n"
      "v_exp_f32 v22, v22\n"
      "v_exp_f32 v23, v23\n"
      "v_exp_f32 v24, v24\n"
      "v_exp_f32 v25, v25\n"
      "v_smfmac_f32_32x32x32_bf16 v[192:207], v[176:179], v[16:23], v188\n"
      "v_exp_f32 v26, v26\n"
      "v_exp_f32 v27, v27\n"
      "v_exp_f32 v28, v28\n"
      "v_exp_f32 v29, v29\n"
      "v_exp_f32 v30, v30\n"
      "v_mfma_f32_32x32x16_bf16 v[56:71], v[156:159], v[148:151], v[120:135]\n"
      "s_add_u32 s68, s68, 0x1000\n"
      "buffer_load_dwordx4 v[156:159], v189, %[srd], s68 offen offset:0 sc1\n"
      "v_exp_f32 v31, v31\n"
      "v_exp_f32 v32, v32\n"
      "v_exp_f32 v33, v33\n"
      "v_exp_f32 v34, v34\n"
      "v_exp_f32 v35, v35\n"
      "v_exp_f32 v36, v36\n"
      "v_smfmac_f32_32x32x32_bf16 v[208:223], v[172:175], v[24:31], v188\n"
      "v_exp_f32 v37, v37\n"
      "v_exp_f32 v38, v38\n"
      "v_exp_f32 v39, v39\n"
      "v_exp_f32 v40, v40\n"
      "v_exp_f32 v41, v41\n"
      "v_smfmac_f32_32x32x32_bf16 v[208:223], v[176:179], v[32:39], v188\n"
      "v_exp_f32 v42, v42\n"
      "v_exp_f32 v43, v43\n"
      "v_exp_f32 v44, v44\n"
      "v_exp_f32 v45, v45\n"
      "v_exp_f32 v46, v46\n"
      "s_waitcnt vmcnt(9)\n"
      "v_mfma_f32_32x32x16_bf16 v[8:23], v[160:163], v[136:139], v[72:87]\n"
      "v_exp_f32 v47, v47\n"
      "v_exp_f32 v48, v48\n"
      "v_exp_f32 v49, v49\n"
      "v_exp_f32 v50, v50\n"
      "v_exp_f32 v51, v51\n"
      "v_exp_f32 v52, v52\n"
      "v_smfmac_f32_32x32x32_bf16 v[224:239], v[172:175], v[40:47], v188\n"
      "v_exp_f32 v53, v53\n"
      "v_exp_f32 v54, v54\n"
      "v_exp_f32 v55, v55\n"
      "v_exp_f32 v56, v56\n"
      "v_exp_f32 v57, v57\n"
      "v_smfmac_f32_32x32x32_bf16 v[224:239], v[176:179], v[48:55], v188\n"
      "v_exp_f32 v58, v58\n"
      "v_exp_f32 v59, v59\n"
      "v_exp_f32 v60, v60\n"
      "v_exp_f32 v61, v61\n"
      "v_exp_f32 v62, v62\n"
      "v_mfma_f32_32x32x16_bf16 v[24:39], v[160:163], v[140:143], v[88:103]\n"
      "v_exp_f32 v63, v63\n"
      "v_exp_f32 v64, v64\n"
      "v_exp_f32 v65, v65\n"
      "v_exp_f32 v66, v66\n"
      "v_exp_f32 v67, v67\n"
      "v_exp_f32 v68, v68\n"
      "v_smfmac_f32_32x32x32_bf16 v[240:255], v[172:175], v[56:63], v188\n"
      "v_exp_f32 v69, v69\n"
      "v_exp_f32 v70, v70\n"
      "v_exp_f32 v71, v71\n"
      "v_exp_f32 v8, v8\n"
      "v_exp_f32 v9, v9\n"
      "v_smfmac_f32_32x32x32_bf16 v[240:255], v[176:179], v[64:71], v188\n"
      "s_mov_b64 exec, s[44:45]\n"
      "buffer_load_dwordx4 v[172:175], v190, %[srd], s68 offen offset:0 sc1\n"
      "buffer_load_dwordx4 v[176:179], v190, %[srd], s68 offen offset:512 sc1\n"
      "s_mov_b64 exec, -1\n"
      "v_exp_f32 v10, v10\n"
      "v_exp_f32 v11, v11\n"
      "v_exp_f32 v12, v12\n"
      "v_exp_f32 v13, v13\n"
      "v_exp_f32 v14, v14\n"
      "v_mfma_f32_32x32x16_bf16 v[40:55], v[160:163], v[144:147], v[104:119]\n"
      "v_exp_f32 v15, v15\n"
      "v_exp_f32 v16, v16\n"
      "v_exp_f32 v17, v17\n"
      "v_exp_f32 v18, v18\n"
      "v_exp_f32 v19, v19\n"
      "v_exp_f32 v20, v20\n"
      "s_waitcnt vmcnt(9)\n"
      "v_smfmac_f32_32x32x32_bf16 v[192:207], v[180:183], v[8:15], v188\n"
      "v_exp_f32 v21, v21\n"
      "v_exp_f32 v22, v22\n"
      "v_exp_f32 v23, v23\n"
      "v_exp_f32 v24, v24\n"
      "v_exp_f32 v25, v25\n"
      "v_smfmac_f32_32x32x32_bf16 v[192:207], v[184:187], v[16:23], v188\n"
      "v_exp_f32 v26, v26\n"
      "v_exp_f32 v27, v27\n"
      "v_exp_f32 v28, v28\n"
      "v_exp_f32 v29, v29\n"
      "v_exp_f32 v30, v30\n"
      "v_mfma_f32_32x32x16_bf16 v[56:71], v[160:163], v[148:151], v[120:135]\n"
      "buffer_load_dwordx4 v[160:163], v189, %[srd], s68 offen offset:1024 sc1\n"
      "v_exp_f32 v31, v31\n"
      "v_exp_f32 v32, v32\n"
      "v_exp_f32 v33, v33\n"
      "v_exp_f32 v34, v34\n"
      "v_exp_f32 v35, v35\n"
      "v_exp_f32 v36, v36\n"
      "v_smfmac_f32_32x32x32_bf16 v[208:223], v[180:183], v[24:31], v188\n"
      "v_exp_f32 v37, v37\n"
      "v_exp_f32 v38, v38\n"
      "v_exp_f32 v39, v39\n"
      "v_exp_f32 v40, v40\n"
      "v_exp_f32 v41, v41\n"
      "v_smfmac_f32_32x32x32_bf16 v[208:223], v[184:187], v[32:39], v188\n"
      "v_exp_f32 v42, v42\n"
      "v_exp_f32 v43, v43\n"
      "v_exp_f32 v44, v44\n"
      "v_exp_f32 v45, v45\n"
      "v_exp_f32 v46, v46\n"
      "s_waitcnt vmcnt(6)\n"
      "v_mfma_f32_32x32x16_bf16 v[8:23], v[152:155], v[136:139], v[72:87]\n"
      "v_exp_f32 v47, v47\n"
      "v_exp_f32 v48, v48\n"
      "v_exp_f32 v49, v49\n"
      "v_exp_f32 v50, v50\n"
      "v_exp_f32 v51, v51\n"
      "v_exp_f32 v52, v52\n"
      "v_smfmac_f32_32x32x32_bf16 v[224:239], v[180:183], v[40:47], v188\n"
      "v_exp_f32 v53, v53\n"
      "v_exp_f32 v54, v54\n"
      "v_exp_f32 v55, v55\n"
      "v_exp_f32 v56, v56\n"
      "v_exp_f32 v57, v57\n"
      "v_smfmac_f32_32x32x32_bf16 v[224:239], v[184:187], v[48:55], v188\n"
      "v_exp_f32 v58, v58\n"
      "v_exp_f32 v59, v59\n"
      "v_exp_f32 v60, v60\n"
      "v_exp_f32 v61, v61\n"
      "v_exp_f32 v62, v62\n"
      "v_mfma_f32_32x32x16_bf16 v[24:39], v[152:155], v[140:143], v[88:103]\n"
      "v_exp_f32 v63, v63\n"
      "v_exp_f32 v64, v64\n"
      "v_exp_f32 v65, v65\n"
      "v_exp_f32 v66, v66\n"
      "v_exp_f32 v67, v67\n"
      "v_exp_f32 v68, v68\n"
      "v_smfmac_f32_32x32x32_bf16 v[240:255], v[180:183], v[56:63], v188\n"
      "v_exp_f32 v69, v69\n"
      "v_exp_f32 v70, v70\n"
      "v_exp_f32 v71, v71\n"
      "v_exp_f32 v8, v8\n"
      "v_exp_f32 v9, v9\n"
      "v_smfmac_f32_32x32x32_bf16 v[240:255], v[184:187], v[64:71], v188\n"
      "s_mov_b64 exec, s[44:45]\n"
      "buffer_load_dwordx4 v[180:183], v190, %[srd], s68 offen offset:1024 sc1\n"
      "buffer_load_dwordx4 v[184:187], v190, %[srd], s68 offen offset:1536 sc1\n"
      "s_mov_b64 exec, -1\n"
      "v_exp_f32 v10, v10\n"
      "v_exp_f32 v11, v11\n"
      "v_exp_f32 v12, v12\n"
      "v_exp_f32 v13, v13\n"
      "v_exp_f32 v14, v14\n"
      "v_mfma_f32_32x32x16_bf16 v[40:55], v[152:155], v[144:147], v[104:119]\n"
      "v_exp_f32 v15, v15\n"
      "v_exp_f32 v16, v16\n"
      "v_exp_f32 v17, v17\n"
      "v_exp_f32 v18, v18\n"
      "v_exp_f32 v19, v19\n"
      "v_exp_f32 v20, v20\n"
      "s_waitcnt vmcnt(6)\n"
      "v_smfmac_f32_32x32x32_bf16 v[192:207], v[164:167], v[8:15], v188\n"
      "v_exp_f32 v21, v21\n"
      "v_exp_f32 v22, v22\n"
      "v_exp_f32 v23, v23\n"
      "v_exp_f32 v24, v24\n"
      "v_exp_f32 v25, v25\n"
      "v_smfmac_f32_32x32x32_bf16 v[192:207], v[168:171], v[16:23], v188\n"
      "v_exp_f32 v26, v26\n"
      "v_exp_f32 v27, v27\n"
      "v_exp_f32 v28, v28\n"
      "v_exp_f32 v29, v29\n"
      "v_exp_f32 v30, v30\n"
      "v_mfma_f32_32x32x16_bf16 v[56:71], v[152:155], v[148:151], v[120:135]\n"
      "buffer_load_dwordx4 v[152:155], v189, %[srd], s68 offen offset:2048 sc1\n"
      "v_exp_f32 v31, v31\n"
      "v_exp_f32 v32, v32\n"
      "v_exp_f32 v33, v33\n"
      "v_exp_f32 v34, v34\n"
      "v_exp_f32 v35, v35\n"
      "v_exp_f32 v36, v36\n"
      "v_smfmac_f32_32x32x32_bf16 v[208:223], v[164:167], v[24:31], v188\n"
      "v_exp_f32 v37, v37\n"
      "v_exp_f32 v38, v38\n"
      "v_exp_f32 v39, v39\n"
      "v_exp_f32 v40, v40\n"
      "v_exp_f32 v41, v41\n"
      "v_smfmac_f32_32x32x32_bf16 v[208:223], v[168:171], v[32:39], v188\n"
      "v_exp_f32 v42, v42\n"
      "v_exp_f32 v43, v43\n"
      "v_exp_f32 v44, v44\n"
      "v_exp_f32 v45, v45\n"
      "v_exp_f32 v46, v46\n"
      "s_waitcnt vmcnt(6)\n"
      "v_mfma_f32_32x32x16_bf16 v[8:23], v[156:159], v[136:139], v[72:87]\n"
      "v_exp_f32 v47, v47\n"
      "v_exp_f32 v48, v48\n"
      "v_exp_f32 v49, v49\n"
      "v_exp_f32 v50, v50\n"
      "v_exp_f32 v51, v51\n"
      "v_exp_f32 v52, v52\n"
      "v_smfmac_f32_32x32x32_bf16 v[224:239], v[164:167], v[40:47], v188\n"
      "v_exp_f32 v53, v53\n"
      "v_exp_f32 v54, v54\n"
      "v_exp_f32 v55, v55\n"
      "v_exp_f32 v56, v56\n"
      "v_exp_f32 v57, v57\n"
      "v_smfmac_f32_32x32x32_bf16 v[224:239], v[168:171], v[48:55], v188\n"
      "v_exp_f32 v58, v58\n"
      "v_exp_f32 v59, v59\n"
      "v_exp_f32 v60, v60\n"
      "v_exp_f32 v61, v61\n"
      "v_exp_f32 v62, v62\n"
      "v_mfma_f32_32x32x16_bf16 v[24:39], v[156:159], v[140:143], v[88:103]\n"
      "v_exp_f32 v63, v63\n"
      "v_exp_f32 v64, v64\n"
      "v_exp_f32 v65, v65\n"
      "v_exp_f32 v66, v66\n"
      "v_exp_f32 v67, v67\n"
      "v_exp_f32 v68, v68\n"
      "v_smfmac_f32_32x32x32_bf16 v[240:255], v[164:167], v[56:63], v188\n"
      "v_exp_f32 v69, v69\n"
      "v_exp_f32 v70, v70\n"
      "v_exp_f32 v71, v71\n"
      "s_or_b32 s69, %[usem], s41\n"
      "s_cbranch_scc1 4f\n"
      "s_mov_b64 exec, 1\n"
      "v_mov_b32 v4, 163776\n"
      "v_mov_b32 v5, 1\n"
      "ds_add_rtn_u32 v5, v4, v5\n"
      "v_mov_b32 v6, s42\n"
      "v_mov_b32 v7, s43\n"
      "v_mov_b32 v4, s46\n"
      "s_waitcnt lgkmcnt(0)\n"
      "v_readfirstlane_b32 s69, v5\n"
      "s_cmp_lg_u32 s69, 7\n"
      "s_cbranch_scc1 3f\n"
      "buffer_store_dwordx2 v[6:7], v4, %[srd], 0 offen sc1\n"
      "3:\n"
      "s_mov_b64 exec, -1\n"
      "4:\n"
      "v_exp_f32 v8, v8\n"
      "v_exp_f32 v9, v9\n"
      "v_smfmac_f32_32x32x32_bf16 v[240:255], v[168:171], v[64:71], v188\n"
      "s_mov_b64 exec, s[44:45]\n"
      "buffer_load_dwordx4 v[164:167], v190, %[srd], s68 offen offset:2048 sc1\n"
      "buffer_load_dwordx4 v[168:171], v190, %[srd], s68 offen offset:2560 sc1\n"
      "s_mov_b64 exec, -1\n"
      "v_exp_f32 v10, v10\n"
      "v_exp_f32 v11, v11\n"
      "v_exp_f32 v12, v12\n"
      "v_exp_f32 v13, v13\n"
      "v_exp_f32 v14, v14\n"
      "v_mfma_f32_32x32x16_bf16 v[40:55], v[156:159], v[144:147], v[104:119]\n"
      "v_exp_f32 v15, v15\n"
      "v_exp_f32 v16, v16\n"
      "v_exp_f32 v17, v17\n"
      "v_exp_f32 v18, v18\n"
      "v_exp_f32 v19, v19\n"
      "v_exp_f32 v20, v20\n"
      "s_waitcnt vmcnt(6)\n"
      "v_smfmac_f32_32x32x32_bf16 v[192:207], v[172:175], v[8:15], v188\n"
      "v_exp_f32 v21, v21\n"
      "v_exp_f32 v22, v22\n"
      "v_exp_f32 v23, v23\n"
      "v_exp_f32 v24, v24\n"
      "v_exp_f32 v25, v25\n"
      "v_smfmac_f32_32x32x32_bf16 v[192:207], v[176:179], v[16:23], v188\n"
      "v_exp_f32 v26, v26\n"
      "v_exp_f32 v27, v27\n"
      "v_exp_f32 v28, v28\n"
      "v_exp_f32 v29, v29\n"
      "v_exp_f32 v30, v30\n"
      "v_mfma_f32_32x32x16_bf16 v[56:71], v[156:159], v[148:151], v[120:135]\n"
      "buffer_load_dwordx4 v[156:159], v189, %[srd], s68 offen offset:3072 sc1\n"
      "v_exp_f32 v31, v31\n"
      "v_exp_f32 v32, v32\n"
      "v_exp_f32 v33, v33\n"
      "v_exp_f32 v34, v34\n"
      "v_exp_f32 v35, v35\n"
      "v_exp_f32 v36, v36\n"
      "v_smfmac_f32_32x32x32_bf16 v[208:223], v[172:175], v[24:31], v188\n"
      "v_exp_f32 v37, v37\n"
      "v_exp_f32 v38, v38\n"
      "v_exp_f32 v39, v39\n"
      "v_exp_f32 v40, v40\n"
      "v_exp_f32 v41, v41\n"
      "v_smfmac_f32_32x32x32_bf16 v[208:223], v[176:179], v[32:39], v188\n"
      "v_exp_f32 v42, v42\n"
      "v_exp_f32 v43, v43\n"
      "v_exp_f32 v44, v44\n"
      "v_exp_f32 v45, v45\n"
      "v_exp_f32 v46, v46\n"
      "s_waitcnt vmcnt(6)\n"
      "v_mfma_f32_32x32x16_bf16 v[8:23], v[160:163], v[136:139], v[72:87]\n"
      "v_exp_f32 v47, v47\n"
      "v_exp_f32 v48, v48\n"
      "v_exp_f32 v49, v49\n"
      "v_exp_f32 v50, v50\n"
      "v_exp_f32 v51, v51\n"
      "v_exp_f32 v52, v52\n"
      "v_smfmac_f32_32x32x32_bf16 v[224:239], v[172:175], v[40:47], v188\n"
      "v_exp_f32 v53, v53\n"
      "v_exp_f32 v54, v54\n"
      "v_exp_f32 v55, v55\n"
      "v_exp_f32 v56, v56\n"
      "v_exp_f32 v57, v57\n"
      "v_smfmac_f32_32x32x32_bf16 v[224:239], v[176:179], v[48:55], v188\n"
      "v_exp_f32 v58, v58\n"
      "v_exp_f32 v59, v59\n"
      "v_exp_f32 v60, v60\n"
      "v_exp_f32 v61, v61\n"
      "v_exp_f32 v62, v62\n"
      "v_mfma_f32_32x32x16_bf16 v[24:39], v[160:163], v[140:143], v[88:103]\n"
      "v_exp_f32 v63, v63\n"
      "v_exp_f32 v64, v64\n"
      "v_exp_f32 v65, v65\n"
      "v_exp_f32 v66, v66\n"
      "v_exp_f32 v67, v67\n"
      "v_exp_f32 v68, v68\n"
      "v_smfmac_f32_32x32x32_bf16 v[240:255], v[172:175], v[56:63], v188\n"
      "v_exp_f32 v69, v69\n"
      "v_exp_f32 v70, v70\n"
      "v_exp_f32 v71, v71\n"
      "v_exp_f32 v8, v8\n"
      "v_exp_f32 v9, v9\n"
      "v_smfmac_f32_32x32x32_bf16 v[240:255], v[176:179], v[64:71], v188\n"
      "s_mov_b64 exec, s[44:45]\n"
      "buffer_load_dwordx4 v[172:175], v190, %[srd], s68 offen offset:3072 sc1\n"
      "buffer_load_dwordx4 v[176:179], v190, %[srd], s68 offen offset:3584 sc1\n"
      "s_mov_b64 exec, -1\n"
      "v_exp_f32 v10, v10\n"
      "v_exp_f32 v11, v11\n"
      "v_exp_f32 v12, v12\n"
      "v_exp_f32 v13, v13\n"
      "v_exp_f32 v14, v14\n"
      "v_mfma_f32_32x32x16_bf16 v[40:55], v[160:163], v[144:147], v[104:119]\n"
      "v_exp_f32 v15, v15\n"
      "v_exp_f32 v16, v16\n"
      "v_exp_f32 v17, v17\n"
      "v_exp_f32 v18, v18\n"
      "v_exp_f32 v19, v19\n"
      "v_exp_f32 v20, v20\n"
      "s_waitcnt vmcnt(6)\n"
      "v_smfmac_f32_32x32x32_bf16 v[192:207], v[180:183], v[8:15], v188\n"
      "v_exp_f32 v21, v21\n"
      "v_exp_f32 v22, v22\n"
      "v_exp_f32 v23, v23\n"
      "v_exp_f32 v24, v24\n"
      "v_exp_f32 v25, v25\n"
      "v_smfmac_f32_32x32x32_bf16 v[192:207], v[184:187], v[16:23], v188\n"
      "v_exp_f32 v26, v26\n"
      "v_exp_f32 v27, v27\n"
      "v_exp_f32 v28, v28\n"
      "v_exp_f32 v29, v29\n"
      "v_exp_f32 v30, v30\n"
      "v_mfma_f32_32x32x16_bf16 v[56:71], v[160:163], v[148:151], v[120:135]\n"
      "s_add_u32 s68, s68, 0x1000\n"
      "buffer_load_dwordx4 v[160:163], v189, %[srd], s68 offen offset:0 sc1\n"
      "v_exp_f32 v31, v31\n"
      "v_exp_f32 v32, v32\n"
      "v_exp_f32 v33, v33\n"
      "v_exp_f32 v34, v34\n"
      "v_exp_f32 v35, v35\n"
      "v_exp_f32 v36, v36\n"
      "v_smfmac_f32_32x32x32_bf16 v[208:223], v[180:183], v[24:31], v188\n"
      "v_exp_f32 v37, v37\n"
      "v_exp_f32 v38, v38\n"
      "v_exp_f32 v39, v39\n"
      "v_exp_f32 v40, v40\n"
      "v_exp_f32 v41, v41\n"
      "v_smfmac_f32_32x32x32_bf16 v[208:223], v[184:187], v[32:39], v188\n"
      "v_exp_f32 v42, v42\n"
      "v_exp_f32 v43, v43\n"
      "v_exp_f32 v44, v44\n"
      "v_exp_f32 v45, v45\n"
      "v_exp_f32 v46, v46\n"
      "s_waitcnt vmcnt(6)\n"
      "v_mfma_f32_32x32x16_bf16 v[8:23], v[152:155], v[136:139], v[72:87]\n"
      "v_exp_f32 v47, v47\n"
      "v_exp_f32 v48, v48\n"
      "v_exp_f32 v49, v49\n"
      "v_exp_f32 v50, v50\n"
      "v_exp_f32 v51, v51\n"
      "v_exp_f32 v52, v52\n"
      "v_smfmac_f32_32x32x32_bf16 v[224:239], v[180:183], v[40:47], v188\n"
      "v_exp_f32 v53, v53\n"
      "v_exp_f32 v54, v54\n"
      "v_exp_f32 v55, v55\n"
      "v_exp_f32 v56, v56\n"
      "v_exp_f32 v57, v57\n"
      "v_smfmac_f32_32x32x32_bf16 v[224:239], v[184:187], v[48:55], v188\n"
      "v_exp_f32 v58, v58\n"
      "v_exp_f32 v59, v59\n"
      "v_exp_f32 v60, v60\n"
      "v_exp_f32 v61, v61\n"
      "v_exp_f32 v62, v62\n"
      "v_mfma_f32_32x32x16_bf16 v[24:39], v[152:155], v[140:143], v[88:103]\n"
      "v_exp_f32 v63, v63\n"
      "v_exp_f32 v64, v64\n"
      "v_exp_f32 v65, v65\n"
      "v_exp_f32 v66, v66\n"
      "v_exp_f32 v67, v67\n"
      "v_exp_f32 v68, v68\n"
      "v_smfmac_f32_32x32x32_bf16 v[240:255], v[180:183], v[56:63], v188\n"
      "v_exp_f32 v69, v69\n"
      "v_exp_f32 v70, v70\n"
      "v_exp_f32 v71, v71\n"
      "v_exp_f32 v8, v8\n"
      "v_exp_f32 v9, v9\n"
      "v_smfmac_f32_32x32x32_bf16 v[240:255], v[184:187], v[64:71], v188\n"
      "s_mov_b64 exec, s[44:45]\n"
      "buffer_load_dwordx4 v[180:183], v190, %[srd], s68 offen offset:0 sc1\n"
      "buffer_load_dwordx4 v[184:187], v190, %[srd], s68 offen offset:512 sc1\n"
      "s_mov_b64 exec, -1\n"
      "v_exp_f32 v10, v10\n"
      "v_exp_f32 v11, v11\n"
      "v_exp_f32 v12, v12\n"
      "v_exp_f32 v13, v13\n"
      "v_exp_f32 v14, v14\n"
      "v_mfma_f32_32x32x16_bf16 v[40:55], v[152:155], v[144:147], v[104:119]\n"
      "v_exp_f32 v15, v15\n"
      "v_exp_f32 v16, v16\n"
      "v_exp_f32 v17, v17\n"
      "v_exp_f32 v18, v18\n"
      "v_exp_f32 v19, v19\n"
      "v_exp_f32 v20, v20\n"
      "s_waitcnt vmcnt(6)\n"
      "v_smfmac_f32_32x32x32_bf16 v[192:207], v[164:167], v[8:15], v188\n"
      "v_exp_f32 v21, v21\n"
      "v_exp_f32 v22, v22\n"
      "v_exp_f32 v23, v23\n"
      "v_exp_f32 v24, v24\n"
      "v_exp_f32 v25, v25\n"
      "v_smfmac_f32_32x32x32_bf16 v[192:207], v[168:171], v[16:23], v188\n"
      "v_exp_f32 v26, v26\n"
      "v_exp_f32 v27, v27\n"
      "v_exp_f32 v28, v28\n"
      "v_exp_f32 v29, v29\n"
      "v_exp_f32 v30, v30\n"
      "v_mfma_f32_32x32x16_bf16 v[56:71], v[152:155], v[148:151], v[120:135]\n"
      "buffer_load_dwordx4 v[152:155], v189, %[srd], s68 offen offset:1024 sc1\n"
      "v_exp_f32 v31, v31\n"
      "v_exp_f32 v32, v32\n"
      "v_exp_f32 v33, v33\n"
      "v_exp_f32 v34, v34\n"
      "v_exp_f32 v35, v35\n"
      "v_exp_f32 v36, v36\n"
      "v_smfmac_f32_32x32x32_bf16 v[208:223], v[164:167], v[24:31], v188\n"
      "v_exp_f32 v37, v37\n"
      "v_exp_f32 v38, v38\n"
      "v_exp_f32 v39, v39\n"
      "v_exp_f32 v40, v40\n"
      "v_exp_f32 v41, v41\n"
      "v_smfmac_f32_32x32x32_bf16 v[208:223], v[168:171], v[32:39], v188\n"
      "v_exp_f32 v42, v42\n"
      "v_exp_f32 v43, v43\n"
      "v_exp_f32 v44, v44\n"
      "v_exp_f32 v45, v45\n"
      "v_exp_f32 v46, v46\n"
      "s_waitcnt vmcnt(6)\n"
      "v_mfma_f32_32x32x16_bf16 v[8:23], v[156:159], v[136:139], v[72:87]\n"
      "v_exp_f32 v47, v47\n"
      "v_exp_f32 v48, v48\n"
      "v_exp_f32 v49, v49\n"
      "v_exp_f32 v50, v50\n"
      "v_exp_f32 v51, v51\n"
      "v_exp_f32 v52, v52\n"
      "v_smfmac_f32_32x32x32_bf16 v[224:239], v[164:167], v[40:47], v188\n"
      "v_exp_f32 v53, v53\n"
      "v_exp_f32 v54, v54\n"
      "v_exp_f32 v55, v55\n"
      "v_exp_f32 v56, v56\n"
      "v_exp_f32 v57, v57\n"
      "v_smfmac_f32_32x32x32_bf16 v[224:239], v[168:171], v[48:55], v188\n"
      "v_exp_f32 v58, v58\n"
      "v_exp_f32 v59, v59\n"
      "v_exp_f32 v60, v60\n"
      "v_exp_f32 v61, v61\n"
      "v_exp_f32 v62, v62\n"
      "v_mfma_f32_32x32x16_bf16 v[24:39], v[156:159], v[140:143], v[88:103]\n"
      "v_exp_f32 v63, v63\n"
      "v_exp_f32 v64, v64\n"
      "v_exp_f32 v65, v65\n"
      "v_exp_f32 v66, v66\n"
      "v_exp_f32 v67, v67\n"
      "v_exp_f32 v68, v68\n"
      "v_smfmac_f32_32x32x32_bf16 v[240:255], v[164:167], v[56:63], v188\n"
      "v_exp_f32 v69, v69\n"
      "v_exp_f32 v70, v70\n"
      "v_exp_f32 v71, v71\n"
      "v_exp_f32 v8, v8\n"
      "v_exp_f32 v9, v9\n"
      "v_smfmac_f32_32x32x32_bf16 v[240:255], v[168:171], v[64:71], v188\n"
      "s_mov_b64 exec, s[44:45]\n"
      "buffer_load_dwordx4 v[164:167], v190, %[srd], s68 offen offset:1024 sc1\n"
      "buffer_load_dwordx4 v[168:171], v190, %[srd], s68 offen offset:1536 sc1\n"
      "s_mov_b64 exec, -1\n"
      "v_exp_f32 v10, v10\n"
      "v_exp_f32 v11, v11\n"
      "v_exp_f32 v12, v12\n"
      "v_exp_f32 v13, v13\n"
      "v_exp_f32 v14, v14\n"
      "v_mfma_f32_32x32x16_bf16 v[40:55], v[156:159], v[144:147], v[104:119]\n"
      "v_exp_f32 v15, v15\n"
      "v_exp_f32 v16, v16\n"
      "v_exp_f32 v17, v17\n"
      "v_exp_f32 v18, v18\n"
      "v_exp_f32 v19, v19\n"
      "v_exp_f32 v20, v20\n"
      "s_waitcnt vmcnt(6)\n"
      "v_smfmac_f32_32x32x32_bf16 v[192:207], v[172:175], v[8:15], v188\n"
      "v_exp_f32 v21, v21\n"
      "v_exp_f32 v22, v22\n"
      "v_exp_f32 v23, v23\n"
      "v_exp_f32 v24, v24\n"
      "v_exp_f32 v25, v25\n"
      "v_smfmac_f32_32x32x32_bf16 v[192:207], v[176:179], v[16:23], v188\n"
      "v_exp_f32 v26, v26\n"
      "v_exp_f32 v27, v27\n"
      "v_exp_f32 v28, v28\n"
      "v_exp_f32 v29, v29\n"
      "v_exp_f32 v30, v30\n"
      "v_mfma_f32_32x32x16_bf16 v[56:71], v[156:159], v[148:151], v[120:135]\n"
      "buffer_load_dwordx4 v[156:159], v189, %[srd], s68 offen offset:2048 sc1\n"
      "v_exp_f32 v31, v31\n"
      "v_exp_f32 v32, v32\n"
      "v_exp_f32 v33, v33\n"
      "v_exp_f32 v34, v34\n"
      "v_exp_f32 v35, v35\n"
      "v_exp_f32 v36, v36\n"
      "v_smfmac_f32_32x32x32_bf16 v[208:223], v[172:175], v[24:31], v188\n"
      "v_exp_f32 v37, v37\n"
      "v_exp_f32 v38, v38\n"
      "v_exp_f32 v39, v39\n"
      "v_exp_f32 v40, v40\n"
      "v_exp_f32 v41, v41\n"
      "v_smfmac_f32_32x32x32_bf16 v[208:223], v[176:179], v[32:39], v188\n"
      "v_exp_f32 v42, v42\n"
      "v_exp_f32 v43, v43\n"
      "v_exp_f32 v44, v44\n"
      "v_exp_f32 v45, v45\n"
      "v_exp_f32 v46, v46\n"
      "s_waitcnt vmcnt(6)\n"
      "v_mfma_f32_32x32x16_bf16 v[8:23], v[160:163], v[136:139], v[72:87]\n"
      "v_exp_f32 v47, v47\n"
      "v_exp_f32 v48, v48\n"
      "v_exp_f32 v49, v49\n"
      "v_exp_f32 v50, v50\n"
      "v_exp_f32 v51, v51\n"
      "v_exp_f32 v52, v52\n"
      "v_smfmac_f32_32x32x32_bf16 v[224:239], v[172:175], v[40:47], v188\n"
      "v_exp_f32 v53, v53\n"
      "v_exp_f32 v54, v54\n"
      "v_exp_f32 v55, v55\n"
      "v_exp_f32 v56, v56\n"
      "v_exp_f32 v57, v57\n"
      "v_smfmac_f32_32x32x32_bf16 v[224:239], v[176:179], v[48:55], v188\n"
      "v_exp_f32 v58, v58\n"
      "v_exp_f32 v59, v59\n"
      "v_exp_f32 v60, v60\n"
      "v_exp_f32 v61, v61\n"
      "v_exp_f32 v62, v62\n"
      "v_mfma_f32_32x32x16_bf16 v[24:39], v[160:163], v[140:143], v[88:103]\n"
      "v_exp_f32 v63, v63\n"
      "v_exp_f32 v64, v64\n"
      "v_exp_f32 v65, v65\n"
      "v_exp_f32 v66, v66\n"
      "v_exp_f32 v67, v67\n"
      "v_exp_f32 v68, v68\n"
      "v_smfmac_f32_32x32x32_bf16 v[240:255], v[172:175], v[56:63], v188\n"
      "v_exp_f32 v69, v69\n"
      "v_exp_f32 v70, v70\n"
      "v_exp_f32 v71, v71\n"
      "v_exp_f32 v8, v8\n"
      "v_exp_f32 v9, v9\n"
      "v_smfmac_f32_32x32x32_bf16 v[240:255], v[176:179], v[64:71], v188\n"
      "s_mov_b64 exec, s[44:45]\n"
      "buffer_load_dwordx4 v[172:175], v190, %[srd], s68 offen offset:2048 sc1\n"
      "buffer_load_dwordx4 v[176:179], v190, %[srd], s68 offen offset:2560 sc1\n"
      "s_mov_b64 exec, -1\n"
      "v_exp_f32 v10, v10\n"
      "v_exp_f32 v11, v11\n"
      "v_exp_f32 v12, v12\n"
      "v_exp_f32 v13, v13\n"
      "v_exp_f32 v14, v14\n"
      "v_mfma_f32_32x32x16_bf16 v[40:55], v[160:163], v[144:147], v[104:119]\n"
      "v_exp_f32 v15, v15\n"
      "v_exp_f32 v16, v16\n"
      "v_exp_f32 v17, v17\n"
      "v_exp_f32 v18, v18\n"
      "v_exp_f32 v19, v19\n"
      "v_exp_f32 v20, v20\n"
      "s_waitcnt vmcnt(6)\n"
      "v_smfmac_f32_32x32x32_bf16 v[192:207], v[180:183], v[8:15], v188\n"
      "v_exp_f32 v21, v21\n"
      "v_exp_f32 v22, v22\n"
      "v_exp_f32 v23, v23\n"
      "v_exp_f32 v24, v24\n"
      "v_exp_f32 v25, v25\n"
      "v_smfmac_f32_32x32x32_bf16 v[192:207], v[184:187], v[16:23], v188\n"
      "v_exp_f32 v26, v26\n"
      "v_exp_f32 v27, v27\n"
      "v_exp_f32 v28, v28\n"
      "v_exp_f32 v29, v29\n"
      "v_exp_f32 v30, v30\n"
      "v_mfma_f32_32x32x16_bf16 v[56:71], v[160:163], v[148:151], v[120:135]\n"
      "buffer_load_dwordx4 v[160:163], v189, %[srd], s68 offen offset:3072 sc1\n"
      "v_exp_f32 v31, v31\n"
      "v_exp_f32 v32, v32\n"
      "v_exp_f32 v33, v33\n"
      "v_exp_f32 v34, v34\n"
      "v_exp_f32 v35, v35\n"
      "v_exp_f32 v36, v36\n"
      "v_smfmac_f32_32x32x32_bf16 v[208:223], v[180:183], v[24:31], v188\n"
      "v_exp_f32 v37, v37\n"
      "v_exp_f32 v38, v38\n"
      "v_exp_f32 v39, v39\n"
      "v_exp_f32 v40, v40\n"
      "v_exp_f32 v41, v41\n"
      "v_smfmac_f32_32x32x32_bf16 v[208:223], v[184:187], v[32:39], v188\n"
      "v_exp_f32 v42, v42\n"
      "v_exp_f32 v43, v43\n"
      "v_exp_f32 v44, v44\n"
      "v_exp_f32 v45, v45\n"
      "v_exp_f32 v46, v46\n"
      "s_waitcnt vmcnt(6)\n"
      "v_mfma_f32_32x32x16_bf16 v[8:23], v[152:155], v[136:139], v[72:87]\n"
      "v_exp_f32 v47, v47\n"
      "v_exp_f32 v48, v48\n"
      "v_exp_f32 v49, v49\n"
      "v_exp_f32 v50, v50\n"
      "v_exp_f32 v51, v51\n"
      "v_exp_f32 v52, v52\n"
      "v_smfmac_f32_32x32x32_bf16 v[224:239], v[180:183], v[40:47], v188\n"
      "v_exp_f32 v53, v53\n"
      "v_exp_f32 v54, v54\n"
      "v_exp_f32 v55, v55\n"
      "v_exp_f32 v56, v56\n"
      "v_exp_f32 v57, v57\n"
      "v_smfmac_f32_32x32x32_bf16 v[224:239], v[184:187], v[48:55], v188\n"
      "v_exp_f32 v58, v58\n"
      "v_exp_f32 v59, v59\n"
      "v_exp_f32 v60, v60\n"
      "v_exp_f32 v61, v61\n"
      "v_exp_f32 v62, v62\n"
      "v_mfma_f32_32x32x16_bf16 v[24:39], v[152:155], v[140:143], v[88:103]\n"
      "v_exp_f32 v63, v63\n"
      "v_exp_f32 v64, v64\n"
      "v_exp_f32 v65, v65\n"
      "v_exp_f32 v66, v66\n"
      "v_exp_f32 v67, v67\n"
      "v_exp_f32 v68, v68\n"
      "v_smfmac_f32_32x32x32_bf16 v[240:255], v[180:183], v[56:63], v188\n"
      "v_exp_f32 v69, v69\n"
      "v_exp_f32 v70, v70\n"
      "v_exp_f32 v71, v71\n"
      "v_exp_f32 v8, v8\n"
      "v_exp_f32 v9, v9\n"
      "v_smfmac_f32_32x32x32_bf16 v[240:255], v[184:187], v[64:71], v188\n"
      "s_mov_b64 exec, s[44:45]\n"
      "buffer_load_dwordx4 v[180:183], v190, %[srd], s68 offen offset:3072 sc1\n"
      "buffer_load_dwordx4 v[184:187], v190, %[srd], s68 offen offset:3584 sc1\n"
      "s_mov_b64 exec, -1\n"
      "v_exp_f32 v10, v10\n"
      "v_exp_f32 v11, v11\n"
      "v_exp_f32 v12, v12\n"
      "v_exp_f32 v13, v13\n"
      "v_exp_f32 v14, v14\n"
      "v_mfma_f32_32x32x16_bf16 v[40:55], v[152:155], v[144:147], v[104:119]\n"
      "v_exp_f32 v15, v15\n"
      "v_exp_f32 v16, v16\n"
      "v_exp_f32 v17, v17\n"
      "v_exp_f32 v18, v18\n"
      "v_exp_f32 v19, v19\n"
      "v_exp_f32 v20, v20\n"
      "s_waitcnt vmcnt(6)\n"
      "v_smfmac_f32_32x32x32_bf16 v[192:207], v[164:167], v[8:15], v188\n"
      "v_exp_f32 v21, v21\n"
      "v_exp_f32 v22, v22\n"
      "v_exp_f32 v23, v23\n"
      "v_exp_f32 v24, v24\n"
      "v_exp_f32 v25, v25\n"
      "v_smfmac_f32_32x32x32_bf16 v[192:207], v[168:171], v[16:23], v188\n"
      "v_exp_f32 v26, v26\n"
      "v_exp_f32 v27, v27\n"
      "v_exp_f32 v28, v28\n"
      "v_exp_f32 v29, v29\n"
      "v_exp_f32 v30, v30\n"
      "v_mfma_f32_32x32x16_bf16 v[56:71], v[152:155], v[148:151], v[120:135]\n"
      "s_add_u32 s68, s68, 0x1000\n"
      "buffer_load_dwordx4 v[152:155], v189, %[srd], s68 offen offset:0 sc1\n"
      "v_exp_f32 v31, v31\n"
      "v_exp_f32 v32, v32\n"
      "v_exp_f32 v33, v33\n"
      "v_exp_f32 v34, v34\n"
      "v_exp_f32 v35, v35\n"
      "v_exp_f32 v36, v36\n"
      "v_smfmac_f32_32x32x32_bf16 v[208:223], v[164:167], v[24:31], v188\n"
      "v_exp_f32 v37, v37\n"
      "v_exp_f32 v38, v38\n"
      "v_exp_f32 v39, v39\n"
      "v_exp_f32 v40, v40\n"
      "v_exp_f32 v41, v41\n"
      "v_smfmac_f32_32x32x32_bf16 v[208:223], v[168:171], v[32:39], v188\n"
      "v_exp_f32 v42, v42\n"
      "v_exp_f32 v43, v43\n"
      "v_exp_f32 v44, v44\n"
      "v_exp_f32 v45, v45\n"
      "v_exp_f32 v46, v46\n"
      "s_waitcnt vmcnt(6)\n"
      "v_mfma_f32_32x32x16_bf16 v[8:23], v[156:159], v[136:139], v[72:87]\n"
      "v_exp_f32 v47, v47\n"
      "v_exp_f32 v48, v48\n"
      "v_exp_f32 v49, v49\n"
      "v_exp_f32 v50, v50\n"
      "v_exp_f32 v51, v51\n"
      "v_exp_f32 v52, v52\n"
      "v_smfmac_f32_32x32x32_bf16 v[224:239], v[164:167], v[40:47], v188\n"
      "v_exp_f32 v53, v53\n"
      "v_exp_f32 v54, v54\n"
      "v_exp_f32 v55, v55\n"
      "v_exp_f32 v56, v56\n"
      "v_exp_f32 v57, v57\n"
      "v_smfmac_f32_32x32x32_bf16 v[224:239], v[168:171], v[48:55], v188\n"
      "v_exp_f32 v58, v58\n"
      "v_exp_f32 v59, v59\n"
      "v_exp_f32 v60, v60\n"
      "v_exp_f32 v61, v61\n"
      "v_exp_f32 v62, v62\n"
      "v_mfma_f32_32x32x16_bf16 v[24:39], v[156:159], v[140:143], v[88:103]\n"
      "v_exp_f32 v63, v63\n"
      "v_exp_f32 v64, v64\n"
      "v_exp_f32 v65, v65\n"
      "v_exp_f32 v66, v66\n"
      "v_exp_f32 v67, v67\n"
      "v_exp_f32 v68, v68\n"
      "v_smfmac_f32_32x32x32_bf16 v[240:255], v[164:167], v[56:63], v188\n"
      "v_exp_f32 v69, v69\n"
      "v_exp_f32 v70, v70\n"
      "v_exp_f32 v71, v71\n"
      "v_exp_f32 v8, v8\n"
      "v_exp_f32 v9, v9\n"
      "v_smfmac_f32_32x32x32_bf16 v[240:255], v[168:171], v[64:71], v188\n"
      "s_mov_b64 exec, s[44:45]\n"
      "buffer_load_dwordx4 v[164:167], v190, %[srd], s68 offen offset:0 sc1\n"
      "buffer_load_dwordx4 v[168:171], v190, %[srd], s68 offen offset:512 sc1\n"
      "s_mov_b64 exec, -1\n"
      "v_exp_f32 v10, v10\n"
      "v_exp_f32 v11, v11\n"
      "v_exp_f32 v12, v12\n"
      "v_exp_f32 v13, v13\n"
      "v_exp_f32 v14, v14\n"
      "v_mfma_f32_32x32x16_bf16 v[40:55], v[156:159], v[144:147], v[104:119]\n"
      "v_exp_f32 v15, v15\n"
      "v_exp_f32 v16, v16\n"
      "v_exp_f32 v17, v17\n"
      "v_exp_f32 v18, v18\n"
      "v_exp_f32 v19, v19\n"
      "v_exp_f32 v20, v20\n"
      "s_waitcnt vmcnt(6)\n"
      "v_smfmac_f32_32x32x32_bf16 v[192:207], v[172:175], v[8:15], v188\n"
      "v_exp_f32 v21, v21\n"
      "v_exp_f32 v22, v22\n"
      "v_exp_f32 v23, v23\n"
      "v_exp_f32 v24, v24\n"
      "v_exp_f32 v25, v25\n"
      "v_smfmac_f32_32x32x32_bf16 v[192:207], v[176:179], v[16:23], v188\n"
      "v_exp_f32 v26, v26\n"
      "v_exp_f32 v27, v27\n"
      "v_exp_f32 v28, v28\n"
      "v_exp_f32 v29, v29\n"
      "v_exp_f32 v30, v30\n"
      "v_mfma_f32_32x32x16_bf16 v[56:71], v[156:159], v[148:151], v[120:135]\n"
      "buffer_load_dwordx4 v[156:159], v189, %[srd], s68 offen offset:1024 sc1\n"
      "v_exp_f32 v31, v31\n"
      "v_exp_f32 v32, v32\n"
      "v_exp_f32 v33, v33\n"
      "v_exp_f32 v34, v34\n"
      "v_exp_f32 v35, v35\n"
      "v_exp_f32 v36, v36\n"
      "v_smfmac_f32_32x32x32_bf16 v[208:223], v[172:175], v[24:31], v188\n"
      "v_exp_f32 v37, v37\n"
      "v_exp_f32 v38, v38\n"
      "v_exp_f32 v39, v39\n"
      "v_exp_f32 v40, v40\n"
      "v_exp_f32 v41, v41\n"
      "v_smfmac_f32_32x32x32_bf16 v[208:223], v[176:179], v[32:39], v188\n"
      "v_exp_f32 v42, v42\n"
      "v_exp_f32 v43, v43\n"
      "v_exp_f32 v44, v44\n"
      "v_exp_f32 v45, v45\n"
      "v_exp_f32 v46, v46\n"
      "s_waitcnt vmcnt(6)\n"
      "v_mfma_f32_32x32x16_bf16 v[8:23], v[160:163], v[136:139], v[72:87]\n"
      "v_exp_f32 v47, v47\n"
      "v_exp_f32 v48, v48\n"
      "v_exp_f32 v49, v49\n"
      "v_exp_f32 v50, v50\n"
      "v_exp_f32 v51, v51\n"
      "v_exp_f32 v52, v52\n"
      "v_smfmac_f32_32x32x32_bf16 v[224:239], v[172:175], v[40:47], v188\n"
      "v_exp_f32 v53, v53\n"
      "v_exp_f32 v54, v54\n"
      "v_exp_f32 v55, v55\n"
      "v_exp_f32 v56, v56\n"
      "v_exp_f32 v57, v57\n"
      "v_smfmac_f32_32x32x32_bf16 v[224:239], v[176:179], v[48:55], v188\n"
      "v_exp_f32 v58, v58\n"
      "v_exp_f32 v59, v59\n"
      "v_exp_f32 v60, v60\n"
      "v_exp_f32 v61, v61\n"
      "v_exp_f32 v62, v62\n"
      "v_mfma_f32_32x32x16_bf16 v[24:39], v[160:163], v[140:143], v[88:103]\n"
      "v_exp_f32 v63, v63\n"
      "v_exp_f32 v64, v64\n"
      "v_exp_f32 v65, v65\n"
      "v_exp_f32 v66, v66\n"
      "v_exp_f32 v67, v67\n"
      "v_exp_f32 v68, v68\n"
      "v_smfmac_f32_32x32x32_bf16 v[240:255], v[172:175], v[56:63], v188\n"
      "v_exp_f32 v69, v69\n"
      "v_exp_f32 v70, v70\n"
      "v_exp_f32 v71, v71\n"
      "v_exp_f32 v8, v8\n"
      "v_exp_f32 v9, v9\n"
      "v_smfmac_f32_32x32x32_bf16 v[240:255], v[176:179], v[64:71], v188\n"
      "s_mov_b64 exec, s[44:45]\n"
      "buffer_load_dwordx4 v[172:175], v190, %[srd], s68 offen offset:1024 sc1\n"
      "buffer_load_dwordx4 v[176:179], v190, %[srd], s68 offen offset:1536 sc1\n"
      "s_mov_b64 exec, -1\n"
      "v_exp_f32 v10, v10\n"
      "v_exp_f32 v11, v11\n"
      "v_exp_f32 v12, v12\n"
      "v_exp_f32 v13, v13\n"
      "v_exp_f32 v14, v14\n"
      "v_mfma_f32_32x32x16_bf16 v[40:55], v[160:163], v[144:147], v[104:119]\n"
      "v_exp_f32 v15, v15\n"
      "v_exp_f32 v16, v16\n"
      "v_exp_f32 v17, v17\n"
      "v_exp_f32 v18, v18\n"
      "v_exp_f32 v19, v19\n"
      "v_exp_f32 v20, v20\n"
      "s_waitcnt vmcnt(6)\n"
      "v_smfmac_f32_32x32x32_bf16 v[192:207], v[180:183], v[8:15], v188\n"
      "v_exp_f32 v21, v21\n"
      "v_exp_f32 v22, v22\n"
      "v_exp_f32 v23, v23\n"
      "v_exp_f32 v24, v24\n"
      "v_exp_f32 v25, v25\n"
      "v_smfmac_f32_32x32x32_bf16 v[192:207], v[184:187], v[16:23], v188\n"
      "v_exp_f32 v26, v26\n"
      "v_exp_f32 v27, v27\n"
      "v_exp_f32 v28, v28\n"
      "v_exp_f32 v29, v29\n"
      "v_exp_f32 v30, v30\n"
      "v_mfma_f32_32x32x16_bf16 v[56:71], v[160:163], v[148:151], v[120:135]\n"
      "buffer_load_dwordx4 v[160:163], v189, %[srd], s68 offen offset:2048 sc1\n"
      "v_exp_f32 v31, v31\n"
      "v_exp_f32 v32, v32\n"
      "v_exp_f32 v33, v33\n"
      "v_exp_f32 v34, v34\n"
      "v_exp_f32 v35, v35\n"
      "v_exp_f32 v36, v36\n"
      "v_smfmac_f32_32x32x32_bf16 v[208:223], v[180:183], v[24:31], v188\n"
      "v_exp_f32 v37, v37\n"
      "v_exp_f32 v38, v38\n"
      "v_exp_f32 v39, v39\n"
      "v_exp_f32 v40, v40\n"
      "v_exp_f32 v41, v41\n"
      "v_smfmac_f32_32x32x32_bf16 v[208:223], v[184:187], v[32:39], v188\n"
      "v_exp_f32 v42, v42\n"
      "v_exp_f32 v43, v43\n"
      "v_exp_f32 v44, v44\n"
      "v_exp_f32 v45, v45\n"
      "v_exp_f32 v46, v46\n"
      "s_waitcnt vmcnt(6)\n"
      "v_mfma_f32_32x32x16_bf16 v[8:23], v[152:155], v[136:139], v[72:87]\n"
      "v_exp_f32 v47, v47\n"
      "v_exp_f32 v48, v48\n"
      "v_exp_f32 v49, v49\n"
      "v_exp_f32 v50, v50\n"
      "v_exp_f32 v51, v51\n"
      "v_exp_f32 v52, v52\n"
      "v_smfmac_f32_32x32x32_bf16 v[224:239], v[180:183], v[40:47], v188\n"
      "v_exp_f32 v53, v53\n"
      "v_exp_f32 v54, v54\n"
      "v_exp_f32 v55, v55\n"
      "v_exp_f32 v56, v56\n"
      "v_exp_f32 v57, v57\n"
      "v_smfmac_f32_32x32x32_bf16 v[224:239], v[184:187], v[48:55], v188\n"
      "v_exp_f32 v58, v58\n"
      "v_exp_f32 v59, v59\n"
      "v_exp_f32 v60, v60\n"
      "v_exp_f32 v61, v61\n"
      "v_exp_f32 v62, v62\n"
      "v_mfma_f32_32x32x16_bf16 v[24:39], v[152:155], v[140:143], v[88:103]\n"
      "v_exp_f32 v63, v63\n"
      "v_exp_f32 v64, v64\n"
      "v_exp_f32 v65, v65\n"
      "v_exp_f32 v66, v66\n"
      "v_exp_f32 v67, v67\n"
      "v_exp_f32 v68, v68\n"
      "v_smfmac_f32_32x32x32_bf16 v[240:255], v[180:183], v[56:63], v188\n"
      "v_exp_f32 v69, v69\n"
      "v_exp_f32 v70, v70\n"
      "v_exp_f32 v71, v71\n"
      "v_exp_f32 v8, v8\n"
      "v_exp_f32 v9, v9\n"
      "v_smfmac_f32_32x32x32_bf16 v[240:255], v[184:187], v[64:71], v188\n"
      "s_mov_b64 exec, s[44:45]\n"
      "buffer_load_dwordx4 v[180:183], v190, %[srd], s68 offen offset:2048 sc1\n"
      "buffer_load_dwordx4 v[184:187], v190, %[srd], s68 offen offset:2560 sc1\n"
      "s_mov_b64 exec, -1\n"
      "v_exp_f32 v10, v10\n"
      "v_exp_f32 v11, v11\n"
      "v_exp_f32 v12, v12\n"
      "v_exp_f32 v13, v13\n"
      "v_exp_f32 v14, v14\n"
      "v_mfma_f32_32x32x16_bf16 v[40:55], v[152:155], v[144:147], v[104:119]\n"
      "v_exp_f32 v15, v15\n"
      "v_exp_f32 v16, v16\n"
      "v_exp_f32 v17, v17\n"
      "v_exp_f32 v18, v18\n"
      "v_exp_f32 v19, v19\n"
      "v_exp_f32 v20, v20\n"
      "s_waitcnt vmcnt(6)\n"
      "v_smfmac_f32_32x32x32_bf16 v[192:207], v[164:167], v[8:15], v188\n"
      "v_exp_f32 v21, v21\n"
      "v_exp_f32 v22, v22\n"
      "v_exp_f32 v23, v23\n"
      "v_exp_f32 v24, v24\n"
      "v_exp_f32 v25, v25\n"
      "v_smfmac_f32_32x32x32_bf16 v[192:207], v[168:171], v[16:23], v188\n"
      "v_exp_f32 v26, v26\n"
      "v_exp_f32 v27, v27\n"
      "v_exp_f32 v28, v28\n"
      "v_exp_f32 v29, v29\n"
      "v_exp_f32 v30, v30\n"
      "v_mfma_f32_32x32x16_bf16 v[56:71], v[152:155], v[148:151], v[120:135]\n"
      "buffer_load_dwordx4 v[152:155], v189, %[srd], s68 offen offset:3072 sc1\n"
      "v_exp_f32 v31, v31\n"
      "v_exp_f32 v32, v32\n"
      "v_exp_f32 v33, v33\n"
      "v_exp_f32 v34, v34\n"
      "v_exp_f32 v35, v35\n"
      "v_exp_f32 v36, v36\n"
      "v_smfmac_f32_32x32x32_bf16 v[208:223], v[164:167], v[24:31], v188\n"
      "v_exp_f32 v37, v37\n"
      "v_exp_f32 v38, v38\n"
      "v_exp_f32 v39, v39\n"
      "v_exp_f32 v40, v40\n"
      "v_exp_f32 v41, v41\n"
      "v_smfmac_f32_32x32x32_bf16 v[208:223], v[168:171], v[32:39], v188\n"
      "v_exp_f32 v42, v42\n"
      "v_exp_f32 v43, v43\n"
      "v_exp_f32 v44, v44\n"
      "v_exp_f32 v45, v45\n"
      "v_exp_f32 v46, v46\n"
      "s_waitcnt vmcnt(6)\n"
      "v_mfma_f32_32x32x16_bf16 v[8:23], v[156:159], v[136:139], v[72:87]\n"
      "v_exp_f32 v47, v47\n"
      "v_exp_f32 v48, v48\n"
      "v_exp_f32 v49, v49\n"
      "v_exp_f32 v50, v50\n"
      "v_exp_f32 v51, v51\n"
      "v_exp_f32 v52, v52\n"
      "v_smfmac_f32_32x32x32_bf16 v[224:239], v[164:167], v[40:47], v188\n"
      "v_exp_f32 v53, v53\n"
      "v_exp_f32 v54, v54\n"
      "v_exp_f32 v55, v55\n"
      "v_exp_f32 v56, v56\n"
      "v_exp_f32 v57, v57\n"
      "v_smfmac_f32_32x32x32_bf16 v[224:239], v[168:171], v[48:55], v188\n"
      "v_exp_f32 v58, v58\n"
      "v_exp_f32 v59, v59\n"
      "v_exp_f32 v60, v60\n"
      "v_exp_f32 v61, v61\n"
      "v_exp_f32 v62, v62\n"
      "v_mfma_f32_32x32x16_bf16 v[24:39], v[156:159], v[140:143], v[88:103]\n"
      "v_exp_f32 v63, v63\n"
      "v_exp_f32 v64, v64\n"
      "v_exp_f32 v65, v65\n"
      "v_exp_f32 v66, v66\n"
      "v_exp_f32 v67, v67\n"
      "v_exp_f32 v68, v68\n"
      "v_smfmac_f32_32x32x32_bf16 v[240:255], v[164:167], v[56:63], v188\n"
      "v_exp_f32 v69, v69\n"
      "v_exp_f32 v70, v70\n"
      "v_exp_f32 v71, v71\n"
      "s_cmp_lg_u32 s41, 0\n"
      "s_cbranch_scc1 10f\n"
      "v_and_b32 v4, 0xf0, v189\n"
      "v_lshlrev_b32 v4, 3, v4\n"
      "buffer_load_dwordx2 v[6:7], v4, %[srd], s62 offen sc1\n"
      "10:\n"
      "v_exp_f32 v8, v8\n"
      "v_exp_f32 v9, v9\n"
      "v_smfmac_f32_32x32x32_bf16 v[240:255], v[168:171], v[64:71], v188\n"
      "s_mov_b64 exec, s[44:45]\n"
      "buffer_load_dwordx4 v[164:167], v190, %[srd], s68 offen offset:3072 sc1\n"
      "buffer_load_dwordx4 v[168:171], v190, %[srd], s68 offen offset:3584 sc1\n"
      "s_mov_b64 exec, -1\n"
      "v_exp_f32 v10, v10\n"
      "v_exp_f32 v11, v11\n"
      "v_exp_f32 v12, v12\n"
      "v_exp_f32 v13, v13\n"
      "v_exp_f32 v14, v14\n"
      "v_mfma_f32_32x32x16_bf16 v[40:55], v[156:159], v[144:147], v[104:119]\n"
      "v_exp_f32 v15, v15\n"
      "v_exp_f32 v16, v16\n"
      "v_exp_f32 v17, v17\n"
      "v_exp_f32 v18, v18\n"
      "v_exp_f32 v19, v19\n"
      "v_exp_f32 v20, v20\n"
      "s_waitcnt vmcnt(6)\n"
      "v_smfmac_f32_32x32x32_bf16 v[192:207], v[172:175], v[8:15], v188\n"
      "v_exp_f32 v21, v21\n"
      "v_exp_f32 v22, v22\n"
      "v_exp_f32 v23, v23\n"
      "v_exp_f32 v24, v24\n"
      "v_exp_f32 v25, v25\n"
      "v_smfmac_f32_32x32x32_bf16 v[192:207], v[176:179], v[16:23], v188\n"
      "v_exp_f32 v26, v26\n"
      "v_exp_f32 v27, v27\n"
      "v_exp_f32 v28, v28\n"
      "v_exp_f32 v29, v29\n"
      "v_exp_f32 v30, v30\n"
      "v_mfma_f32_32x32x16_bf16 v[56:71], v[156:159], v[148:151], v[120:135]\n"
      "v_exp_f32 v31, v31\n"
      "v_exp_f32 v32, v32\n"
      "v_exp_f32 v33, v33\n"
      "v_exp_f32 v34, v34\n"
      "v_exp_f32 v35, v35\n"
      "v_exp_f32 v36, v36\n"
      "v_smfmac_f32_32x32x32_bf16 v[208:223], v[172:175], v[24:31], v188\n"
      "v_exp_f32 v37, v37\n"
      "v_exp_f32 v38, v38\n"
      "v_exp_f32 v39, v39\n"
      "v_exp_f32 v40, v40\n"
      "v_exp_f32 v41, v41\n"
      "v_smfmac_f32_32x32x32_bf16 v[208:223], v[176:179], v[32:39], v188\n"
      "v_exp_f32 v42, v42\n"
      "v_exp_f32 v43, v43\n"
      "v_exp_f32 v44, v44\n"
      "v_exp_f32 v45, v45\n"
      "v_exp_f32 v46, v46\n"
      "s_waitcnt vmcnt(5)\n"
      "v_mfma_f32_32x32x16_bf16 v[8:23], v[160:163], v[136:139], v[72:87]\n"
      "v_exp_f32 v47, v47\n"
      "v_exp_f32 v48, v48\n"
      "v_exp_f32 v49, v49\n"
      "v_exp_f32 v50, v50\n"
      "v_exp_f32 v51, v51\n"
      "v_exp_f32 v52, v52\n"
      "v_smfmac_f32_32x32x32_bf16 v[224:239], v[172:175], v[40:47], v188\n"
      "v_exp_f32 v53, v53\n"
      "v_exp_f32 v54, v54\n"
      "v_exp_f32 v55, v55\n"
      "v_exp_f32 v56, v56\n"
      "v_exp_f32 v57, v57\n"
      "v_smfmac_f32_32x32x32_bf16 v[224:239], v[176:179], v[48:55], v188\n"
      "v_exp_f32 v58, v58\n"
      "v_exp_f32 v59, v59\n"
      "v_exp_f32 v60, v60\n"
      "v_exp_f32 v61, v61\n"
      "v_exp_f32 v62, v62\n"
      "v_mfma_f32_32x32x16_bf16 v[24:39], v[160:163], v[140:143], v[88:103]\n"
      "v_exp_f32 v63, v63\n"
      "v_exp_f32 v64, v64\n"
      "v_exp_f32 v65, v65\n"
      "v_exp_f32 v66, v66\n"
      "v_exp_f32 v67, v67\n"
      "v_exp_f32 v68, v68\n"
      "v_smfmac_f32_32x32x32_bf16 v[240:255], v[172:175], v[56:63], v188\n"
      "v_exp_f32 v69, v69\n"
      "v_exp_f32 v70, v70\n"
      "v_exp_f32 v71, v71\n"
      "v_exp_f32 v8, v8\n"
      "v_exp_f32 v9, v9\n"
      "v_smfmac_f32_32x32x32_bf16 v[240:255], v[176:179], v[64:71], v188\n"
      "v_exp_f32 v10, v10\n"
      "v_exp_f32 v11, v11\n"
      "v_exp_f32 v12, v12\n"
      "v_exp_f32 v13, v13\n"
      "v_exp_f32 v14, v14\n"
      "v_mfma_f32_32x32x16_bf16 v[40:55], v[160:163], v[144:147], v[104:119]\n"
      "v_exp_f32 v15, v15\n"
      "v_exp_f32 v16, v16\n"
      "v_exp_f32 v17, v17\n"
      "v_exp_f32 v18, v18\n"
      "v_exp_f32 v19, v19\n"
      "v_exp_f32 v20, v20\n"
      "s_waitcnt vmcnt(3)\n"
      "v_smfmac_f32_32x32x32_bf16 v[192:207], v[180:183], v[8:15], v188\n"
      "v_exp_f32 v21, v21\n"
      "v_exp_f32 v22, v22\n"
      "v_exp_f32 v23, v23\n"
      "v_exp_f32 v24, v24\n"
      "v_exp_f32 v25, v25\n"
      "v_smfmac_f32_32x32x32_bf16 v[192:207], v[184:187], v[16:23], v188\n"
      "v_exp_f32 v26, v26\n"
      "v_exp_f32 v27, v27\n"
      "v_exp_f32 v28, v28\n"
      "v_exp_f32 v29, v29\n"
      "v_exp_f32 v30, v30\n"
      "v_mfma_f32_32x32x16_bf16 v[56:71], v[160:163], v[148:151], v[120:135]\n"
      "v_exp_f32 v31, v31\n"
      "v_exp_f32 v32, v32\n"
      "v_exp_f32 v33, v33\n"
      "v_exp_f32 v34, v34\n"
      "v_exp_f32 v35, v35\n"
      "v_exp_f32 v36, v36\n"
      "v_smfmac_f32_32x32x32_bf16 v[208:223], v[180:183], v[24:31], v188\n"
      "v_exp_f32 v37, v37\n"
      "v_exp_f32 v38, v38\n"
      "v_exp_f32 v39, v39\n"
      "v_exp_f32 v40, v40\n"
      "v_exp_f32 v41, v41\n"
      "v_smfmac_f32_32x32x32_bf16 v[208:223], v[184:187], v[32:39], v188\n"
      "v_exp_f32 v42, v42\n"
      "v_exp_f32 v43, v43\n"
      "v_exp_f32 v44, v44\n"
      "v_exp_f32 v45, v45\n"
      "v_exp_f32 v46, v46\n"
      "s_waitcnt vmcnt(2)\n"
      "v_mfma_f32_32x32x16_bf16 v[8:23], v[152:155], v[136:139], v[72:87]\n"
      "v_exp_f32 v47, v47\n"
      "v_exp_f32 v48, v48\n"
      "v_exp_f32 v49, v49\n"
      "v_exp_f32 v50, v50\n"
      "v_exp_f32 v51, v51\n"
      "v_exp_f32 v52, v52\n"
      "v_smfmac_f32_32x32x32_bf16 v[224:239], v[180:183], v[40:47], v188\n"
      "v_exp_f32 v53, v53\n"
      "v_exp_f32 v54, v54\n"
      "v_exp_f32 v55, v55\n"
      "v_exp_f32 v56, v56\n"
      "v_exp_f32 v57, v57\n"
      "v_smfmac_f32_32x32x32_bf16 v[224:239], v[184:187], v[48:55], v188\n"
      "v_exp_f32 v58, v58\n"
      "v_exp_f32 v59, v59\n"
      "v_exp_f32 v60, v60\n"
      "v_exp_f32 v61, v61\n"
      "v_exp_f32 v62, v62\n"
      "v_mfma_f32_32x32x16_bf16 v[24:39], v[152:155], v[140:143], v[88:103]\n"
      "v_exp_f32 v63, v63\n"
      "v_exp_f32 v64, v64\n"
      "v_exp_f32 v65, v65\n"
      "v_exp_f32 v66, v66\n"
      "v_exp_f32 v67, v67\n"
      "v_exp_f32 v68, v68\n"
      "v_smfmac_f32_32x32x32_bf16 v[240:255], v[180:183], v[56:63], v188\n"
      "v_exp_f32 v69, v69\n"
      "v_exp_f32 v70, v70\n"
      "v_exp_f32 v71, v71\n"
      "s_cmp_lg_u32 s41, 0\n"
      "s_cbranch_scc1 11f\n"
      "v_cmp_ne_u64 vcc, s[42:43], v[6:7]\n"
      "v_lshlrev_b32 v4, 3, v189\n"
      "s_cbranch_vccnz 11f\n"
      "buffer_load_dword v5, v4, %[srd], s64 offen sc1\n"
      "buffer_load_dword v5, v4, %[srd], s64 offen offset:2816 sc1\n"
      "s_mov_b32 s70, 1\n"
      "11:\n"
      "v_exp_f32 v8, v8\n"
      "v_exp_f32 v9, v9\n"
      "v_smfmac_f32_32x32x32_bf16 v[240:255], v[184:187], v[64:71], v188\n"
      "v_exp_f32 v10, v10\n"
      "v_exp_f32 v11, v11\n"
      "v_exp_f32 v12, v12\n"
      "v_exp_f32 v13, v13\n"
      "v_exp_f32 v14, v14\n"
      "v_mfma_f32_32x32x16_bf16 v[40:55], v[152:155], v[144:147], v[104:119]\n"
      "v_exp_f32 v15, v15\n"
      "v_exp_f32 v16, v16\n"
      "v_exp_f32 v17, v17\n"
      "v_exp_f32 v18, v18\n"
      "v_exp_f32 v19, v19\n"
      "v_exp_f32 v20, v20\n"
      "s_waitcnt vmcnt(0)\n"
      "v_smfmac_f32_32x32x32_bf16 v[192:207], v[164:167], v[8:15], v188\n"
      "v_exp_f32 v21, v21\n"
      "v_exp_f32 v22, v22\n"
      "v_exp_f32 v23, v23\n"
      "v_exp_f32 v24, v24\n"
      "v_exp_f32 v25, v25\n"
      "v_smfmac_f32_32x32x32_bf16 v[192:207], v[168:171], v[16:23], v188\n"
      "v_exp_f32 v26, v26\n"
      "v_exp_f32 v27, v27\n"
      "v_exp_f32 v28, v28\n"
      "v_exp_f32 v29, v29\n"
      "v_exp_f32 v30, v30\n"
      "v_mfma_f32_32x32x16_bf16 v[56:71], v[152:155], v[148:151], v[120:135]\n"
      "v_exp_f32 v31, v31\n"
      "v_exp_f32 v32, v32\n"
      "v_exp_f32 v33, v33\n"
      "v_exp_f32 v34, v34\n"
      "v_exp_f32 v35, v35\n"
      "v_exp_f32 v36, v36\n"
      "v_smfmac_f32_32x32x32_bf16 v[208:223], v[164:167], v[24:31], v188\n"
      "v_exp_f32 v37, v37\n"
      "v_exp_f32 v38, v38\n"
      "v_exp_f32 v39, v39\n"
      "v_exp_f32 v40, v40\n"
      "v_exp_f32 v41, v41\n"
      "v_smfmac_f32_32x32x32_bf16 v[208:223], v[168:171], v[32:39], v188\n"
      "v_exp_f32 v42, v42\n"
      "v_exp_f32 v43, v43\n"
      "v_exp_f32 v44, v44\n"
      "v_exp_f32 v45, v45\n"
      "v_exp_f32 v46, v46\n"
      "v_exp_f32 v47, v47\n"
      "v_exp_f32 v48, v48\n"
      "v_exp_f32 v49, v49\n"
      "v_exp_f32 v50, v50\n"
      "v_exp_f32 v51, v51\n"
      "v_exp_f32 v52, v52\n"
      "v_smfmac_f32_32x32x32_bf16 v[224:239], v[164:167], v[40:47], v188\n"
      "v_exp_f32 v53, v53\n"
      "v_exp_f32 v54, v54\n"
      "v_exp_f32 v55, v55\n"
      "v_exp_f32 v56, v56\n"
      "v_exp_f32 v57, v57\n"
      "v_smfmac_f32_32x32x32_bf16 v[224:239], v[168:171], v[48:55], v188\n"
      "v_exp_f32 v58, v58\n"
      "v_exp_f32 v59, v59\n"
      "v_exp_f32 v60, v60\n"
      "v_exp_f32 v61, v61\n"
      "v_exp_f32 v62, v62\n"
      "v_exp_f32 v63, v63\n"
      "v_exp_f32 v64, v64\n"
      "v_exp_f32 v65, v65\n"
      "v_exp_f32 v66, v66\n"
      "v_exp_f32 v67, v67\n"
      "v_exp_f32 v68, v68\n"
      "v_smfmac_f32_32x32x32_bf16 v[240:255], v[164:167], v[56:63], v188\n"
      "v_exp_f32 v69, v69\n"
      "v_exp_f32 v70, v70\n"
      "v_exp_f32 v71, v71\n"
      "s_nop 1\n"
      "v_smfmac_f32_32x32x32_bf16 v[240:255], v[168:171], v[64:71], v188\n"
      "s_waitcnt vmcnt(0)\n"
      "v_lshrrev_b32 v186, 2, v189\n"
      "v_add_u32 v184, s48, v189\n"
      "v_add_u32 v185, s48, v186\n"
      "ds_write_b128 v184, v[208:211]\n"
      "ds_write_b128 v184, v[212:215] offset:1024\n"
      "ds_write_b32 v185, v216 offset:2048\n"
      "v_max3_f32 v4, |v192|, |v193|, |v194|\n"
      "v_max3_f32 v5, |v195|, |v196|, |v197|\n"
      "v_max3_f32 v4, |v198|, |v199|, v4\n"
      "v_max3_f32 v5, |v208|, |v209|, v5\n"
      "v_max3_f32 v4, |v210|, |v211|, v4\n"
      "v_add_u32 v184, s49, v189\n"
      "v_add_u32 v185, s49, v186\n"
      "ds_write_b128 v184, v[224:227]\n"
      "ds_write_b128 v184, v[228:231] offset:1024\n"
      "ds_write_b32 v185, v232 offset:2048\n"
      "v_max3_f32 v5, |v212|, |v213|, v5\n"
      "v_max3_f32 v4, |v214|, |v215|, v4\n"
      "v_max3_f32 v5, |v224|, |v225|, v5\n"
      "v_max3_f32 v4, |v226|, |v227|, v4\n"
      "v_max3_f32 v5, |v228|, |v229|, v5\n"
      "v_max3_f32 v4, |v230|, |v231|, v4\n"
      "v_add_u32 v184, s50, v189\n"
      "v_add_u32 v185, s50, v186\n"
      "ds_write_b128 v184, v[240:243]\n"
      "ds_write_b128 v184, v[244:247] offset:1024\n"
      "ds_write_b32 v185, v248 offset:2048\n"
      "v_max3_f32 v5, |v240|, |v241|, v5\n"
      "v_max3_f32 v4, |v242|, |v243|, v4\n"
      "v_max3_f32 v5, |v244|, |v245|, v5\n"
      "v_max3_f32 v4, |v246|, |v247|, v4\n"
      "v_max_f32 v4, v4, v5\n"
      "v_max3_f32 v5, v200, v216, v232\n"
      "v_mul_f32 v4, 0x30800000, v4\n"
      "v_max3_f32 v4, v4, v5, v248\n"
      "v_cmp_nlt_f32 vcc, v4, %[big]\n"
      "v_mov_b32 v184, s52\n"
      "s_cmp_lg_u64 vcc, 0\n"
      "s_cselect_b32 s69, 1, 0\n"
      "v_mov_b32 v185, s69\n"
      "ds_write_b32 v184, v185\n"
      "s_cmp_eq_u32 s41, 0\n"
      "s_cbranch_scc1 20f\n"
      "21:\n"
      "s_waitcnt vmcnt(0) lgkmcnt(0)\n"
      "s_barrier\n"
      "s_cmp_lg_u32 s41, 0\n"
      "s_cbranch_scc0 26f\n"
      "v_mov_b32 v185, 163808\n"
      "v_add_u32 v184, s51, v189\n"
      "v_add_u32 v186, s51, v186\n"
      "ds_read_b128 v[72:75], v185\n"
      "ds_read_b128 v[76:79], v185 offset:16\n"
      "ds_read_b128 v[80:83], v184 offset:0\n"
      "ds_read_b128 v[84:87], v184 offset:1024\n"
      "ds_read_b32 v88, v186 offset:2048\n"
      "ds_read_b128 v[92:95], v184 offset:2304\n"
      "ds_read_b128 v[96:99], v184 offset:3328\n"
      "ds_read_b32 v100, v186 offset:4352\n"
      "ds_read_b128 v[104:107], v184 offset:4608\n"
      "ds_read_b128 v[108:111], v184 offset:5632\n"
      "ds_read_b32 v112, v186 offset:6656\n"
      "s_add_u32 s70, s65, 0\n"
      "s_and_b32 s70, s70, 0x1fff\n"
      "s_or_b32 s70, s70, s61\n"
      "buffer_load_dwordx4 v[128:131], v189, %[srd], s70 offen offset:0 sc1\n"
      "s_add_u32 s70, s65, 2048\n"
      "s_and_b32 s70, s70, 0x1fff\n"
      "s_or_b32 s70, s70, s61\n"
      "buffer_load_dwordx4 v[132:135], v189, %[srd], s70 offen offset:0 sc1\n"
      "s_add_u32 s70, s65, 4096\n"
      "s_and_b32 s70, s70, 0x1fff\n"
      "s_or_b32 s70, s70, s61\n"
      "buffer_load_dwordx4 v[136:139], v189, %[srd], s70 offen offset:0 sc1\n"
      "s_add_u32 s70, s65, 6144\n"
      "s_and_b32 s70, s70, 0x1fff\n"
      "s_or_b32 s70, s70, s61\n"
      "buffer_load_dwordx4 v[140:143], v189, %[srd], s70 offen offset:0 sc1\n"
      "buffer_load_dwordx4 v[180:183], v3, %[srdx], 0 offen\n"
      "s_waitcnt lgkmcnt(9)\n"
      "v_or3_b32 v72, v72, v73, v74\n"
      "v_or3_b32 v75, v75, v76, v77\n"
      "v_or3_b32 v72, v72, v78, v79\n"
      "v_or_b32 v72, v72, v75\n"
      "s_waitcnt lgkmcnt(6)\n"
      "v_readfirstlane_b32 %[bad], v72\n"
      "v_add_f32 v200, v200, v88\n"
      "v_add_f32 v192, v192, v80\n"
      "v_add_f32 v193, v193, v81\n"
      "v_add_f32 v194, v194, v82\n"
      "v_add_f32 v195, v195, v83\n"
      "v_add_f32 v196, v196, v84\n"
      "v_add_f32 v197, v197, v85\n"
      "v_add_f32 v198, v198, v86\n"
      "v_add_f32 v199, v199, v87\n"
      "s_waitcnt lgkmcnt(3)\n"
      "v_add_f32 v200, v200, v100\n"
      "v_add_f32 v192, v192, v92\n"
      "v_add_f32 v193, v193, v93\n"
      "v_add_f32 v194, v194, v94\n"
      "v_add_f32 v195, v195, v95\n"
      "v_add_f32 v196, v196, v96\n"
      "v_add_f32 v197, v197, v97\n"
      "v_add_f32 v198, v198, v98\n"
      "v_add_f32 v199, v199, v99\n"
      "s_waitcnt lgkmcnt(0)\n"
      "v_add_f32 v200, v200, v112\n"
      "v_mov_b32 v72, v200\n"
      "v_add_f32 v192, v192, v104\n"
      "v_add_f32 v193, v193, v105\n"
      "v_permlane32_swap_b32 v72, v200\n"
      "v_add_f32 v194, v194, v106\n"
      "v_add_f32 v195, v195, v107\n"
      "v_add_f32 v200, v200, v72\n"
      "v_rcp_f32 v200, v200\n"
      "v_add_f32 v196, v196, v108\n"
      "v_add_f32 v197, v197, v109\n"
      "v_add_f32 v198, v198, v110\n"
      "v_add_f32 v199, v199, v111\n"
      "s_andn2_b32 %[bad], %[bad], %[usem]\n"
      "s_cbranch_scc1 98f\n"
      "v_mul_f32 v192, v192, v200\n"
      "v_mul_f32 v193, v193, v200\n"
      "v_mul_f32 v194, v194, v200\n"
      "v_mul_f32 v195, v195, v200\n"
      "v_mul_f32 v196, v196, v200\n"
      "v_mul_f32 v197, v197, v200\n"
      "v_mul_f32 v198, v198, v200\n"
      "v_mul_f32 v199, v199, v200\n"
      "v_cvt_pk_bf16_f32 v72, v192, v193\n"
      "v_cvt_pk_bf16_f32 v73, v194, v195\n"
      "v_cvt_pk_bf16_f32 v74, v196, v197\n"
      "v_cvt_pk_bf16_f32 v75, v198, v199\n"
      "v_min_u32 v72, -2, v72\n"
      "v_min_u32 v73, -2, v73\n"
      "v_min_u32 v74, -2, v74\n"
      "v_min_u32 v75, -2, v75\n"
      "buffer_store_dwordx4 v[72:75], v189, %[srd], s60 offen sc1\n"
      "s_setprio 0\n"
      "v_lshrrev_b32 v6, 7, v189\n"
      "v_lshrrev_b32 v4, 2, v189\n"
      "v_and_b32 v4, 0x40, v4\n"
      "v_and_b32 v5, 0x70, v189\n"
      "v_xor_b32 v4, v4, v5\n"
      "v_lshl_or_b32 v6, v6, 10, v4\n"
      "v_bfe_u32 v4, v189, 4, 5\n"
      "v_mul_u32_u24 v4, 272, v4\n"
      "v_lshrrev_b32 v5, 9, v189\n"
      "v_lshl_add_u32 v4, v5, 4, v4\n"
      "v_add_u32 v7, s58, v4\n"
      "27:\n"
      "v_add_u32 v185, 81920, v2\n"
      "ds_read_b128 v[80:83], v2 offset:0\n"
      "ds_read_b128 v[84:87], v2 offset:16\n"
      "ds_read_b128 v[88:91], v2 offset:40960\n"
      "ds_read_b128 v[92:95], v2 offset:40976\n"
      "ds_read_b128 v[96:99], v185 offset:0\n"
      "ds_read_b128 v[100:103], v185 offset:16\n"
      "ds_read_b128 v[104:107], v185 offset:40960\n"
      "ds_read_b128 v[108:111], v185 offset:40976\n"
      "s_mov_b32 s68, 0\n"
      "s_waitcnt lgkmcnt(4)\n"
      "ds_read_b64_tr_b16 v[8:9], v0 offset:17408\n"
      "ds_read_b64_tr_b16 v[10:11], v0 offset:18432\n"
      "ds_read_b64_tr_b16 v[12:13], v1 offset:17408\n"
      "ds_read_b64_tr_b16 v[14:15], v1 offset:18432\n"
      "ds_read_b64_tr_b16 v[16:17], v0 offset:19456\n"
      "ds_read_b64_tr_b16 v[18:19], v0 offset:20480\n"
      "ds_read_b64_tr_b16 v[20:21], v1 offset:19456\n"
      "ds_read_b64_tr_b16 v[22:23], v1 offset:20480\n"
      "v_add_f32 v80, v80, v88\n"
      "v_add_f32 v81, v81, v89\n"
      "v_add_f32 v82, v82, v90\n"
      "v_add_f32 v83, v83, v91\n"
      "v_add_f32 v84, v84, v92\n"
      "v_add_f32 v85, v85, v93\n"
      "v_add_f32 v86, v86, v94\n"
      "v_add_f32 v87, v87, v95\n"
      "s_waitcnt lgkmcnt(8)\n"
      "s_barrier\n"
      "v_add_f32 v96, v96, v104\n"
      "v_add_f32 v97, v97, v105\n"
      "v_add_f32 v98, v98, v106\n"
      "v_add_f32 v99, v99, v107\n"
      "v_add_f32 v100, v100, v108\n"
      "v_add_f32 v101, v101, v109\n"
      "v_add_f32 v102, v102, v110\n"
      "v_add_f32 v103, v103, v111\n"
      "s_waitcnt lgkmcnt(4)\n"
      "ds_read_b64_tr_b16 v[24:25], v0 offset:21504\n"
      "ds_read_b64_tr_b16 v[26:27], v0 offset:22528\n"
      "ds_read_b64_tr_b16 v[28:29], v1 offset:21504\n"
      "ds_read_b64_tr_b16 v[30:31], v1 offset:22528\n"
      "ds_read_b64_tr_b16 v[32:33], v0 offset:23552\n"
      "ds_read_b64_tr_b16 v[34:35], v0 offset:24576\n"
      "ds_read_b64_tr_b16 v[36:37], v1 offset:23552\n"
      "ds_read_b64_tr_b16 v[38:39], v1 offset:24576\n"
      "v_add_f32 v80, v80, v96\n"
      "v_add_f32 v81, v81, v97\n"
      "v_add_f32 v82, v82, v98\n"
      "v_add_f32 v83, v83, v99\n"
      "v_add_f32 v84, v84, v100\n"
      "v_add_f32 v85, v85, v101\n"
      "v_add_f32 v86, v86, v102\n"
      "v_add_f32 v87, v87, v103\n"
      "s_waitcnt vmcnt(1) lgkmcnt(8)\n"
      "30:\n"
      "v_max3_u32 v186, v128, v129, v130\n"
      "v_max_u32 v186, v186, v131\n"
      "v_cmp_eq_u32 vcc, -1, v186\n"
      "s_cbranch_vccz 31f\n"
      "s_mov_b32 s71, 15\n"
      "s_call_b64 s[74:75], 32f\n"
      "s_cbranch_scc0 30b\n"
      "31:\n"
      "v_mfma_f32_32x32x16_bf16 v[208:223], v[8:11], v[128:131], 0\n"
      "v_mfma_f32_32x32x16_bf16 v[224:239], v[12:15], v[128:131], 0\n"
      "30:\n"
      "v_max3_u32 v186, v132, v133, v134\n"
      "v_max_u32 v186, v186, v135\n"
      "v_cmp_eq_u32 vcc, -1, v186\n"
      "s_cbranch_vccz 31f\n"
      "s_mov_b32 s71, 14\n"
      "s_call_b64 s[74:75], 32f\n"
      "s_cbranch_scc0 30b\n"
      "31:\n"
      "v_mfma_f32_32x32x16_bf16 v[208:223], v[16:19], v[132:135], v[208:223]\n"
      "v_mfma_f32_32x32x16_bf16 v[224:239], v[20:23], v[132:135], v[224:239]\n"
      "v_mul_f32 v88, 0xbfb8aa3b, v80\n"
      "v_mul_f32 v89, 0xbfb8aa3b, v81\n"
      "v_mul_f32 v90, 0xbfb8aa3b, v82\n"
      "v_mul_f32 v91, 0xbfb8aa3b, v83\n"
      "v_mul_f32 v92, 0xbfb8aa3b, v84\n"
      "v_mul_f32 v93, 0xbfb8aa3b, v85\n"
      "v_mul_f32 v94, 0xbfb8aa3b, v86\n"
      "v_mul_f32 v95, 0xbfb8aa3b, v87\n"
      "v_exp_f32 v88, v88\n"
      "v_exp_f32 v89, v89\n"
      "v_exp_f32 v90, v90\n"
      "v_exp_f32 v91, v91\n"
      "v_exp_f32 v92, v92\n"
      "v_exp_f32 v93, v93\n"
      "v_exp_f32 v94, v94\n"
      "v_exp_f32 v95, v95\n"
      "s_add_u32 s70, s65, 0\n"
      "s_and_b32 s70, s70, 0x1fff\n"
      "s_or_b32 s70, s70, s61\n"
      "buffer_load_dwordx4 v[144:147], v189, %[srd], s70 offen offset:1024 sc1\n"
      "s_add_u32 s70, s65, 2048\n"
      "s_and_b32 s70, s70, 0x1fff\n"
      "s_or_b32 s70, s70, s61\n"
      "buffer_load_dwordx4 v[148:151], v189, %[srd], s70 offen offset:1024 sc1\n"
      "s_add_u32 s70, s65, 4096\n"
      "s_and_b32 s70, s70, 0x1fff\n"
      "s_or_b32 s70, s70, s61\n"
      "buffer_load_dwordx4 v[152:155], v189, %[srd], s70 offen offset:1024 sc1\n"
      "s_add_u32 s70, s65, 6144\n"
      "s_and_b32 s70, s70, 0x1fff\n"
      "s_or_b32 s70, s70, s61\n"
      "buffer_load_dwordx4 v[156:159], v189, %[srd], s70 offen offset:1024 sc1\n"
      "s_waitcnt lgkmcnt(0)\n"
      "ds_read_b64_tr_b16 v[40:41], v0 offset:25600\n"
      "ds_read_b64_tr_b16 v[42:43], v0 offset:26624\n"
      "ds_read_b64_tr_b16 v[44:45], v1 offset:25600\n"
      "ds_read_b64_tr_b16 v[46:47], v1 offset:26624\n"
      "ds_read_b64_tr_b16 v[48:49], v0 offset:27648\n"
      "ds_read_b64_tr_b16 v[50:51], v0 offset:28672\n"
      "ds_read_b64_tr_b16 v[52:53], v1 offset:27648\n"
      "ds_read_b64_tr_b16 v[54:55], v1 offset:28672\n"
      "ds_read_b64_tr_b16 v[56:57], v0 offset:29696\n"
      "ds_read_b64_tr_b16 v[58:59], v0 offset:30720\n"
      "ds_read_b64_tr_b16 v[60:61], v1 offset:29696\n"
      "ds_read_b64_tr_b16 v[62:63], v1 offset:30720\n"
      "30:\n"
      "v_max3_u32 v186, v136, v137, v138\n"
      "v_max_u32 v186, v186, v139\n"
      "v_cmp_eq_u32 vcc, -1, v186\n"
      "s_cbranch_vccz 31f\n"
      "s_mov_b32 s71, 12\n"
      "s_call_b64 s[74:75], 32f\n"
      "s_cbranch_scc0 30b\n"
      "31:\n"
      "v_mfma_f32_32x32x16_bf16 v[208:223], v[24:27], v[136:139], v[208:223]\n"
      "v_mfma_f32_32x32x16_bf16 v[224:239], v[28:31], v[136:139], v[224:239]\n"
      "v_add_f32 v88, 1.0, v88\n"
      "v_add_f32 v89, 1.0, v89\n"
      "v_add_f32 v90, 1.0, v90\n"
      "v_add_f32 v91, 1.0, v91\n"
      "v_add_f32 v92, 1.0, v92\n"
      "v_add_f32 v93, 1.0, v93\n"
      "v_add_f32 v94, 1.0, v94\n"
      "v_add_f32 v95, 1.0, v95\n"
      "30:\n"
      "v_max3_u32 v186, v140, v141, v142\n"
      "v_max_u32 v186, v186, v143\n"
      "v_cmp_eq_u32 vcc, -1, v186\n"
      "s_cbranch_vccz 31f\n"
      "s_mov_b32 s71, 8\n"
      "s_call_b64 s[74:75], 32f\n"
      "s_cbranch_scc0 30b\n"
      "31:\n"
      "v_mfma_f32_32x32x16_bf16 v[208:223], v[32:35], v[140:143], v[208:223]\n"
      "v_mfma_f32_32x32x16_bf16 v[224:239], v[36:39], v[140:143], v[224:239]\n"
      "v_rcp_f32 v88, v88\n"
      "v_rcp_f32 v89, v89\n"
      "v_rcp_f32 v90, v90\n"
      "v_rcp_f32 v91, v91\n"
      "v_rcp_f32 v92, v92\n"
      "v_rcp_f32 v93, v93\n"
      "v_rcp_f32 v94, v94\n"
      "v_rcp_f32 v95, v95\n"
      "v_add_u32 v7, s66, v7\n"
      "s_waitcnt lgkmcnt(8)\n"
      "ds_read_b64_tr_b16 v[64:65], v0 offset:31744\n"
      "ds_read_b64_tr_b16 v[66:67], v0 offset:32768\n"
      "ds_read_b64_tr_b16 v[68:69], v1 offset:31744\n"
      "ds_read_b64_tr_b16 v[70:71], v1 offset:32768\n"
      "v_mul_f32 v240, v80, v88\n"
      "v_mul_f32 v241, v81, v89\n"
      "v_mul_f32 v242, v82, v90\n"
      "v_mul_f32 v243, v83, v91\n"
      "v_mul_f32 v244, v84, v92\n"
      "v_mul_f32 v245, v85, v93\n"
      "v_mul_f32 v246, v86, v94\n"
      "v_mul_f32 v247, v87, v95\n"
      "v_lshlrev_b32 v104, 16, v180\n"
      "v_and_b32 v105, 0xffff0000, v180\n"
      "v_lshlrev_b32 v106, 16, v181\n"
      "v_and_b32 v107, 0xffff0000, v181\n"
      "v_lshlrev_b32 v108, 16, v182\n"
      "v_and_b32 v109, 0xffff0000, v182\n"
      "v_lshlrev_b32 v110, 16, v183\n"
      "v_and_b32 v111, 0xffff0000, v183\n"
      "s_waitcnt vmcnt(3) lgkmcnt(0)\n"
      "30:\n"
      "v_max3_u32 v186, v144, v145, v146\n"
      "v_max_u32 v186, v186, v147\n"
      "v_cmp_eq_u32 vcc, -1, v186\n"
      "s_cbranch_vccz 31f\n"
      "s_mov_b32 s71, 240\n"
      "s_call_b64 s[74:75], 32f\n"
      "s_cbranch_scc0 30b\n"
      "31:\n"
      "v_mfma_f32_32x32x16_bf16 v[208:223], v[40:43], v[144:147], v[208:223]\n"
      "v_mfma_f32_32x32x16_bf16 v[224:239], v[44:47], v[144:147], v[224:239]\n"
      "s_waitcnt vmcnt(2) lgkmcnt(0)\n"
      "30:\n"
      "v_max3_u32 v186, v148, v149, v150\n"
      "v_max_u32 v186, v186, v151\n"
      "v_cmp_eq_u32 vcc, -1, v186\n"
      "s_cbranch_vccz 31f\n"
      "s_mov_b32 s71, 224\n"
      "s_call_b64 s[74:75], 32f\n"
      "s_cbranch_scc0 30b\n"
      "31:\n"
      "v_mfma_f32_32x32x16_bf16 v[208:223], v[48:51], v[148:151], v[208:223]\n"
      "v_mfma_f32_32x32x16_bf16 v[224:239], v[52:55], v[148:151], v[224:239]\n"
      "s_waitcnt vmcnt(1) lgkmcnt(0)\n"
      "30:\n"
      "v_max3_u32 v186, v152, v153, v154\n"
      "v_max_u32 v186, v186, v155\n"
      "v_cmp_eq_u32 vcc, -1, v186\n"
      "s_cbranch_vccz 31f\n"
      "s_mov_b32 s71, 192\n"
      "s_call_b64 s[74:75], 32f\n"
      "s_cbranch_scc0 30b\n"
      "31:\n"
      "v_mfma_f32_32x32x16_bf16 v[208:223], v[56:59], v[152:155], v[208:223]\n"
      "v_mfma_f32_32x32x16_bf16 v[224:239], v[60:63], v[152:155], v[224:239]\n"
      "s_waitcnt vmcnt(0) lgkmcnt(0)\n"
      "30:\n"
      "v_max3_u32 v186, v156, v157, v158\n"
      "v_max_u32 v186, v186, v159\n"
      "v_cmp_eq_u32 vcc, -1, v186\n"
      "s_cbranch_vccz 31f\n"
      "s_mov_b32 s71, 128\n"
      "s_call_b64 s[74:75], 32f\n"
      "s_cbranch_scc0 30b\n"
      "31:\n"
      "v_mfma_f32_32x32x16_bf16 v[208:223], v[64:67], v[156:159], v[208:223]\n"
      "v_mfma_f32_32x32x16_bf16 v[224:239], v[68:71], v[156:159], v[224:239]\n"
      "s_nop 10\n"
      "ds_write_b128 v7, v[208:211] offset:0\n"
      "ds_write_b128 v7, v[212:215] offset:32\n"
      "ds_write_b128 v7, v[216:219] offset:64\n"
      "ds_write_b128 v7, v[220:223] offset:96\n"
      "ds_write_b128 v7, v[224:227] offset:128\n"
      "ds_write_b128 v7, v[228:231] offset:160\n"
      "ds_write_b128 v7, v[232:235] offset:192\n"
      "ds_write_b128 v7, v[236:239] offset:224\n"
      "s_waitcnt lgkmcnt(0)\n"
      "s_barrier\n"
      "ds_read_b128 v[72:75], v2 offset:0\n"
      "ds_read_b128 v[76:79], v2 offset:16\n"
      "ds_read_b128 v[80:83], v2 offset:40960\n"
      "ds_read_b128 v[84:87], v2 offset:40976\n"
      "ds_read_b128 v[88:91], v185 offset:0\n"
      "ds_read_b128 v[92:95], v185 offset:16\n"
      "ds_read_b128 v[96:99], v185 offset:40960\n"
      "ds_read_b128 v[100:103], v185 offset:40976\n"
      "s_waitcnt lgkmcnt(4)\n"
      "v_add_f32 v72, v72, v80\n"
      "v_add_f32 v73, v73, v81\n"
      "v_add_f32 v74, v74, v82\n"
      "v_add_f32 v75, v75, v83\n"
      "v_add_f32 v76, v76, v84\n"
      "v_add_f32 v77, v77, v85\n"
      "v_add_f32 v78, v78, v86\n"
      "v_add_f32 v79, v79, v87\n"
      "s_waitcnt lgkmcnt(0)\n"
      "v_add_f32 v88, v88, v96\n"
      "v_add_f32 v89, v89, v97\n"
      "v_add_f32 v90, v90, v98\n"
      "v_add_f32 v91, v91, v99\n"
      "v_add_f32 v92, v92, v100\n"
      "v_add_f32 v93, v93, v101\n"
      "v_add_f32 v94, v94, v102\n"
      "v_add_f32 v95, v95, v103\n"
      "v_add_f32 v72, v72, v88\n"
      "v_add_f32 v73, v73, v89\n"
      "v_add_f32 v74, v74, v90\n"
      "v_add_f32 v75, v75, v91\n"
      "v_add_f32 v76, v76, v92\n"
      "v_add_f32 v77, v77, v93\n"
      "v_add_f32 v78, v78, v94\n"
      "v_add_f32 v79, v79, v95\n"
      "v_fma_f32 v72, v72, v240, v104\n"
      "v_fma_f32 v73, v73, v241, v105\n"
      "v_fma_f32 v74, v74, v242, v106\n"
      "v_fma_f32 v75, v75, v243, v107\n"
      "v_fma_f32 v76, v76, v244, v108\n"
      "v_fma_f32 v77, v77, v245, v109\n"
      "v_fma_f32 v78, v78, v246, v110\n"
      "v_fma_f32 v79, v79, v247, v111\n"
      "v_cvt_pk_bf16_f32 v80, v72, v73\n"
      "v_cvt_pk_bf16_f32 v81, v74, v75\n"
      "v_cvt_pk_bf16_f32 v82, v76, v77\n"
      "v_cvt_pk_bf16_f32 v83, v78, v79\n"
      "buffer_store_dwordx4 v[80:83], v3, %[srdout], 0 offen sc1\n"
      "s_branch 99f\n"
      "32:\n"
      "s_sleep 2\n"
      "s_bitcmp1_b32 s71, 0\n"
      "s_cbranch_scc0 35f\n"
      "s_add_u32 s70, s65, 0\n"
      "s_and_b32 s70, s70, 0x1fff\n"
      "s_or_b32 s70, s70, s61\n"
      "buffer_load_dwordx4 v[128:131], v189, %[srd], s70 offen offset:0 sc1\n"
      "35:\n"
      "s_bitcmp1_b32 s71, 1\n"
      "s_cbranch_scc0 35f\n"
      "s_add_u32 s70, s65, 2048\n"
      "s_and_b32 s70, s70, 0x1fff\n"
      "s_or_b32 s70, s70, s61\n"
      "buffer_load_dwordx4 v[132:135], v189, %[srd], s70 offen offset:0 sc1\n"
      "35:\n"
      "s_bitcmp1_b32 s71, 2\n"
      "s_cbranch_scc0 35f\n"
      "s_add_u32 s70, s65, 4096\n"
      "s_and_b32 s70, s70, 0x1fff\n"
      "s_or_b32 s70, s70, s61\n"
      "buffer_load_dwordx4 v[136:139], v189, %[srd], s70 offen offset:0 sc1\n"
      "35:\n"
      "s_bitcmp1_b32 s71, 3\n"
      "s_cbranch_scc0 35f\n"
      "s_add_u32 s70, s65, 6144\n"
      "s_and_b32 s70, s70, 0x1fff\n"
      "s_or_b32 s70, s70, s61\n"
      "buffer_load_dwordx4 v[140:143], v189, %[srd], s70 offen offset:0 sc1\n"
      "35:\n"
      "s_bitcmp1_b32 s71, 4\n"
      "s_cbranch_scc0 35f\n"
      "s_add_u32 s70, s65, 0\n"
      "s_and_b32 s70, s70, 0x1fff\n"
      "s_or_b32 s70, s70, s61\n"
      "buffer_load_dwordx4 v[144:147], v189, %[srd], s70 offen offset:1024 sc1\n"
      "35:\n"
      "s_bitcmp1_b32 s71, 5\n"
      "s_cbranch_scc0 35f\n"
      "s_add_u32 s70, s65, 2048\n"
      "s_and_b32 s70, s70, 0x1fff\n"
      "s_or_b32 s70, s70, s61\n"
      "buffer_load_dwordx4 v[148:151], v189, %[srd], s70 offen offset:1024 sc1\n"
      "35:\n"
      "s_bitcmp1_b32 s71, 6\n"
      "s_cbranch_scc0 35f\n"
      "s_add_u32 s70, s65, 4096\n"
      "s_and_b32 s70, s70, 0x1fff\n"
      "s_or_b32 s70, s70, s61\n"
      "buffer_load_dwordx4 v[152:155], v189, %[srd], s70 offen offset:1024 sc1\n"
      "35:\n"
      "s_bitcmp1_b32 s71, 7\n"
      "s_cbranch_scc0 35f\n"
      "s_add_u32 s70, s65, 6144\n"
      "s_and_b32 s70, s70, 0x1fff\n"
      "s_or_b32 s70, s70, s61\n"
      "buffer_load_dwordx4 v[156:159], v189, %[srd], s70 offen offset:1024 sc1\n"
      "35:\n"
      "s_waitcnt vmcnt(0)\n"
      "s_add_u32 s68, s68, 1\n"
      "s_cmp_gt_u32 s68, 0x20000\n"
      "s_setpc_b64 s[74:75]\n"
      "98:\n"
      "s_waitcnt vmcnt(0)\n"
      "s_branch 99f\n"
      "26:\n"
      "v_mov_b32 v185, 163808\n"
      "ds_read_b128 v[72:75], v185\n"
      "ds_read_b128 v[76:79], v185 offset:16\n"
      "s_add_u32 s70, s65, 0\n"
      "s_and_b32 s70, s70, 0x1fff\n"
      "s_or_b32 s70, s70, s61\n"
      "buffer_load_dwordx4 v[128:131], v189, %[srd], s70 offen offset:0 sc1\n"
      "s_add_u32 s70, s65, 2048\n"
      "s_and_b32 s70, s70, 0x1fff\n"
      "s_or_b32 s70, s70, s61\n"
      "buffer_load_dwordx4 v[132:135], v189, %[srd], s70 offen offset:0 sc1\n"
      "s_add_u32 s70, s65, 4096\n"
      "s_and_b32 s70, s70, 0x1fff\n"
      "s_or_b32 s70, s70, s61\n"
      "buffer_load_dwordx4 v[136:139], v189, %[srd], s70 offen offset:0 sc1\n"
      "s_add_u32 s70, s65, 6144\n"
      "s_and_b32 s70, s70, 0x1fff\n"
      "s_or_b32 s70, s70, s61\n"
      "buffer_load_dwordx4 v[140:143], v189, %[srd], s70 offen offset:0 sc1\n"
      "buffer_load_dwordx4 v[180:183], v3, %[srdx], 0 offen\n"
      "s_waitcnt lgkmcnt(0)\n"
      "v_or3_b32 v72, v72, v73, v74\n"
      "v_or3_b32 v75, v75, v76, v77\n"
      "v_or3_b32 v72, v72, v78, v79\n"
      "v_or_b32 v72, v72, v75\n"
      "s_setprio 0\n"
      "s_waitcnt vmcnt(0)\n"
      "v_readfirstlane_b32 %[bad], v72\n"
      "s_nop 3\n"
      "s_andn2_b32 %[bad], %[bad], %[usem]\n"
      "s_cbranch_scc1 99f\n"
      "s_branch 27b\n"
      "8:\n"
      "s_cmp_eq_u32 s41, 0\n"
      "s_cbranch_scc1 9f\n"
      "s_setprio 1\n"
      "9:\n"
      "v_mbcnt_lo_u32_b32 v189, -1, 0\n"
      "v_mbcnt_hi_u32_b32 v189, -1, v189\n"
      "v_and_b32 v190, 31, v189\n"
      "v_cmp_eq_u32 vcc, 16, v190\n"
      "v_mov_b32 v188, 0x3f803f80\n"
      "v_lshlrev_b32 v189, 4, v189\n"
      "v_cndmask_b32 v164, 0, v188, vcc\n"
      "v_lshrrev_b32 v190, 9, v189\n"
      "v_lshlrev_b32 v190, 8, v190\n"
      "v_and_b32 v188, 0xf0, v189\n"
      "v_add3_u32 v190, v190, v188, s40\n"
      "v_mov_b32 v188, 0xdddddddd\n"
      "v_mov_b32 v165, v164\n"
      "v_mov_b32 v166, v164\n"
      "v_mov_b32 v167, v164\n"
      "v_mov_b32 v168, v164\n"
      "v_mov_b32 v169, v164\n"
      "v_mov_b32 v170, v164\n"
      "v_mov_b32 v171, v164\n"
      "v_mov_b32 v172, v164\n"
      "v_mov_b32 v173, v164\n"
      "v_mov_b32 v174, v164\n"
      "v_mov_b32 v175, v164\n"
      "v_mov_b32 v176, v164\n"
      "v_mov_b32 v177, v164\n"
      "v_mov_b32 v178, v164\n"
      "v_mov_b32 v179, v164\n"
      "v_mov_b32 v180, v164\n"
      "v_mov_b32 v181, v164\n"
      "v_mov_b32 v182, v164\n"
      "v_mov_b32 v183, v164\n"
      "v_mov_b32 v184, v164\n"
      "v_mov_b32 v185, v164\n"
      "v_mov_b32 v186, v164\n"
      "v_mov_b32 v187, v164\n"
      "s_or_b32 s69, s37, s36\n"
      "buffer_load_dwordx4 v[136:139], v189, %[srd], s69 offen sc1\n"
      "s_add_u32 s69, s37, 1024\n"
      "s_and_b32 s69, s69, 0xfff\n"
      "s_or_b32 s69, s69, s36\n"
      "buffer_load_dwordx4 v[140:143], v189, %[srd], s69 offen sc1\n"
      "s_add_u32 s69, s37, 2048\n"
      "s_and_b32 s69, s69, 0xfff\n"
      "s_or_b32 s69, s69, s36\n"
      "buffer_load_dwordx4 v[144:147], v189, %[srd], s69 offen sc1\n"
      "s_add_u32 s69, s37, 3072\n"
      "s_and_b32 s69, s69, 0xfff\n"
      "s_or_b32 s69, s69, s36\n"
      "buffer_load_dwordx4 v[148:151], v189, %[srd], s69 offen sc1\n"
      "buffer_load_dwordx4 v[132:135], v189, %[srd], s38 offen sc1\n"
      "buffer_load_dwordx4 v[152:155], v189, %[srd], s39 offen sc1\n"
      "s_mov_b64 exec, s[44:45]\n"
      "buffer_load_dwordx4 v[164:167], v190, %[srd], s39 offen offset:0 sc1\n"
      "buffer_load_dwordx4 v[168:171], v190, %[srd], s39 offen offset:512 sc1\n"
      "s_mov_b64 exec, -1\n"
      "buffer_load_dwordx4 v[156:159], v189, %[srd], s39 offen offset:1024 sc1\n"
      "s_mov_b64 exec, s[44:45]\n"
      "buffer_load_dwordx4 v[172:175], v190, %[srd], s39 offen offset:1024 sc1\n"
      "buffer_load_dwordx4 v[176:179], v190, %[srd], s39 offen offset:1536 sc1\n"
      "s_mov_b64 exec, -1\n"
      "buffer_load_dwordx4 v[160:163], v189, %[srd], s39 offen offset:2048 sc1\n"
      "s_mov_b64 exec, s[44:45]\n"
      "buffer_load_dwordx4 v[180:183], v190, %[srd], s39 offen offset:2048 sc1\n"
      "buffer_load_dwordx4 v[184:187], v190, %[srd], s39 offen offset:2560 sc1\n"
      "s_mov_b64 exec, -1\n"
      "v_bfe_u32 v0, v189, 6, 2\n"
      "v_lshrrev_b32 v1, 9, v189\n"
      "v_lshl_add_u32 v1, v1, 2, v0\n"
      "v_lshlrev_b32 v1, 7, v1\n"
      "v_lshrrev_b32 v0, 1, v0\n"
      "v_lshl_or_b32 v1, v0, 6, v1\n"
      "v_bfe_u32 v0, v189, 8, 1\n"
      "v_lshl_or_b32 v1, v0, 5, v1\n"
      "v_bfe_u32 v0, v189, 4, 2\n"
      "v_lshl_or_b32 v1, v0, 3, v1\n"
      "v_add_u32 v0, s58, v1\n"
      "v_xor_b32 v1, 64, v0\n"
      "v_lshrrev_b32 v8, 7, v189\n"
      "v_and_b32 v2, 0x70, v189\n"
      "v_lshl_or_b32 v3, v8, 10, v2\n"
      "v_add_u32 v3, s63, v3\n"
      "v_add_u32 v8, s67, v8\n"
      "v_mul_u32_u24 v8, 272, v8\n"
      "v_lshl_add_u32 v2, v2, 1, v8\n"
      "s_waitcnt vmcnt(0)\n"
      "s_branch 2b\n"
      "20:\n"
      "s_waitcnt lgkmcnt(0)\n"
      "s_mov_b64 exec, 1\n"
      "v_mov_b32 v4, 163780\n"
      "v_mov_b32 v5, 1\n"
      "ds_add_u32 v4, v5\n"
      "s_mov_b64 exec, -1\n"
      "v_lshrrev_b32 v6, 7, v189\n"
      "v_lshrrev_b32 v4, 2, v189\n"
      "v_and_b32 v4, 0x40, v4\n"
      "v_and_b32 v5, 0x70, v189\n"
      "v_xor_b32 v4, v4, v5\n"
      "v_lshl_or_b32 v6, v6, 10, v4\n"
      "v_bfe_u32 v4, v189, 4, 5\n"
      "v_mul_u32_u24 v4, 272, v4\n"
      "v_lshrrev_b32 v5, 9, v189\n"
      "v_lshl_add_u32 v4, v5, 4, v4\n"
      "v_add_u32 v7, s58, v4\n"
      "s_cmp_lg_u32 s70, 0\n"
      "s_cbranch_scc1 24f\n"
      "v_and_b32 v184, 0xf0, v189\n"
      "v_lshlrev_b32 v184, 3, v184\n"
      "23:\n"
      "buffer_load_dwordx2 v[72:73], v184, %[srd], s62 offen sc1\n"
      "s_waitcnt vmcnt(0)\n"
      "v_cmp_ne_u64 vcc, s[42:43], v[72:73]\n"
      "s_cbranch_vccz 24f\n"
      "s_add_u32 s70, s70, 1\n"
      "s_cmp_gt_u32 s70, 0x40000\n"
      "s_cbranch_scc1 24f\n"
      "s_sleep 8\n"
      "s_branch 23b\n"
      "24:\n"
      "s_add_u32 m0, s58, 0\n"
      "s_add_u32 s70, s55, 0\n"
      "buffer_load_dwordx4 v6, %[srdw], s70 offen lds\n"
      "s_add_u32 m0, s58, 1024\n"
      "s_add_u32 s70, s55, 8192\n"
      "buffer_load_dwordx4 v6, %[srdw], s70 offen lds\n"
      "s_add_u32 s70, s53, 0\n"
      "buffer_load_dwordx4 v[72:75], v189, %[srd], s70 offen sc1\n"
      "s_add_u32 s70, s70, 88064\n"
      "buffer_load_dwordx4 v[76:79], v189, %[srd], s70 offen sc1\n"
      "s_add_u32 m0, s58, 2048\n"
      "s_add_u32 s70, s55, 16384\n"
      "buffer_load_dwordx4 v6, %[srdw], s70 offen lds\n"
      "s_add_u32 m0, s58, 3072\n"
      "s_add_u32 s70, s55, 24576\n"
      "buffer_load_dwordx4 v6, %[srdw], s70 offen lds\n"
      "s_add_u32 s70, s53, 1024\n"
      "buffer_load_dwordx4 v[80:83], v189, %[srd], s70 offen sc1\n"
      "s_add_u32 s70, s70, 88064\n"
      "buffer_load_dwordx4 v[84:87], v189, %[srd], s70 offen sc1\n"
      "s_add_u32 m0, s58, 4096\n"
      "s_add_u32 s70, s55, 32768\n"
      "buffer_load_dwordx4 v6, %[srdw], s70 offen lds\n"
      "s_add_u32 m0, s58, 5120\n"
      "s_add_u32 s70, s55, 40960\n"
      "buffer_load_dwordx4 v6, %[srdw], s70 offen lds\n"
      "s_add_u32 s70, s53, 2048\n"
      "buffer_load_dwordx4 v[88:91], v189, %[srd], s70 offen sc1\n"
      "s_add_u32 s70, s70, 88064\n"
      "buffer_load_dwordx4 v[92:95], v189, %[srd], s70 offen sc1\n"
      "s_add_u32 m0, s58, 6144\n"
      "s_add_u32 s70, s55, 49152\n"
      "buffer_load_dwordx4 v6, %[srdw], s70 offen lds\n"
      "s_add_u32 m0, s58, 7168\n"
      "s_add_u32 s70, s55, 57344\n"
      "buffer_load_dwordx4 v6, %[srdw], s70 offen lds\n"
      "s_add_u32 s70, s53, 3072\n"
      "buffer_load_dwordx4 v[96:99], v189, %[srd], s70 offen sc1\n"
      "s_add_u32 s70, s70, 88064\n"
      "buffer_load_dwordx4 v[100:103], v189, %[srd], s70 offen sc1\n"
      "s_add_u32 m0, s58, 8192\n"
      "s_add_u32 s70, s55, 65536\n"
      "buffer_load_dwordx4 v6, %[srdw], s70 offen lds\n"
      "s_add_u32 m0, s58, 9216\n"
      "s_add_u32 s70, s55, 73728\n"
      "buffer_load_dwordx4 v6, %[srdw], s70 offen lds\n"
      "v_bfe_u32 v0, v189, 6, 2\n"
      "v_lshrrev_b32 v1, 9, v189\n"
      "v_lshl_add_u32 v1, v1, 2, v0\n"
      "v_lshlrev_b32 v1, 7, v1\n"
      "v_lshrrev_b32 v0, 1, v0\n"
      "v_lshl_or_b32 v1, v0, 6, v1\n"
      "v_bfe_u32 v0, v189, 8, 1\n"
      "v_lshl_or_b32 v1, v0, 5, v1\n"
      "v_bfe_u32 v0, v189, 4, 2\n"
      "v_lshl_or_b32 v1, v0, 3, v1\n"
      "v_add_u32 v0, s58, v1\n"
      "v_xor_b32 v1, 64, v0\n"
      "v_lshrrev_b32 v184, 7, v189\n"
      "v_and_b32 v2, 0x70, v189\n"
      "v_lshl_or_b32 v3, v184, 10, v2\n"
      "v_add_u32 v3, s63, v3\n"
      "v_add_u32 v184, s67, v184\n"
      "v_mul_u32_u24 v184, 272, v184\n"
      "v_lshl_add_u32 v2, v2, 1, v184\n"
      "v_mov_b32 v4, 163780\n"
      "s_lshl_b32 s71, %[usem], 2\n"
      "s_add_u32 s71, s71, 4\n"
      "s_mov_b32 s68, 0\n"
      "28:\n"
      "ds_read_b32 v5, v4\n"
      "s_waitcnt lgkmcnt(0)\n"
      "v_readfirstlane_b32 s69, v5\n"
      "s_nop 3\n"
      "s_cmp_ge_u32 s69, s71\n"
      "s_cbranch_scc1 29f\n"
      "s_add_u32 s68, s68, 1\n"
      "s_cmp_gt_u32 s68, 0x100000\n"
      "s_cbranch_scc1 29f\n"
      "s_sleep 1\n"
      "s_branch 28b\n"
      "29:\n"
      "v_mov_b32 v185, 163808\n"
      "v_add_u32 v184, s51, v189\n"
      "v_add_u32 v186, s51, v186\n"
      "ds_read_b128 v[208:211], v185\n"
      "ds_read_b128 v[216:219], v184 offset:0\n"
      "ds_read_b128 v[220:223], v184 offset:1024\n"
      "ds_read_b32 v224, v186 offset:2048\n"
      "ds_read_b128 v[228:231], v184 offset:2304\n"
      "ds_read_b128 v[232:235], v184 offset:3328\n"
      "ds_read_b32 v236, v186 offset:4352\n"
      "ds_read_b128 v[240:243], v184 offset:4608\n"
      "ds_read_b128 v[244:247], v184 offset:5632\n"
      "ds_read_b32 v248, v186 offset:6656\n"
      "s_waitcnt lgkmcnt(9)\n"
      "v_or3_b32 v208, v208, v209, v210\n"
      "v_or_b32 v208, v208, v211\n"
      "s_waitcnt lgkmcnt(6)\n"
      "v_readfirstlane_b32 %[bad], v208\n"
      "v_add_f32 v200, v200, v224\n"
      "v_add_f32 v192, v192, v216\n"
      "v_add_f32 v193, v193, v217\n"
      "v_add_f32 v194, v194, v218\n"
      "v_add_f32 v195, v195, v219\n"
      "v_add_f32 v196, v196, v220\n"
      "v_add_f32 v197, v197, v221\n"
      "v_add_f32 v198, v198, v222\n"
      "v_add_f32 v199, v199, v223\n"
      "s_waitcnt lgkmcnt(3)\n"
      "v_add_f32 v200, v200, v236\n"
      "v_add_f32 v192, v192, v228\n"
      "v_add_f32 v193, v193, v229\n"
      "v_add_f32 v194, v194, v230\n"
      "v_add_f32 v195, v195, v231\n"
      "v_add_f32 v196, v196, v232\n"
      "v_add_f32 v197, v197, v233\n"
      "v_add_f32 v198, v198, v234\n"
      "v_add_f32 v199, v199, v235\n"
      "s_waitcnt lgkmcnt(0)\n"
      "v_add_f32 v200, v200, v248\n"
      "v_mov_b32 v208, v200\n"
      "v_add_f32 v192, v192, v240\n"
      "v_add_f32 v193, v193, v241\n"
      "v_permlane32_swap_b32 v208, v200\n"
      "v_add_f32 v194, v194, v242\n"
      "v_add_f32 v195, v195, v243\n"
      "v_add_f32 v200, v200, v208\n"
      "v_rcp_f32 v200, v200\n"
      "v_add_f32 v196, v196, v244\n"
      "v_add_f32 v197, v197, v245\n"
      "v_add_f32 v198, v198, v246\n"
      "v_add_f32 v199, v199, v247\n"
      "s_andn2_b32 %[bad], %[bad], %[usem]\n"
      "s_cbranch_scc1 36f\n"
      "v_mul_f32 v192, v192, v200\n"
      "v_mul_f32 v193, v193, v200\n"
      "v_mul_f32 v194, v194, v200\n"
      "v_mul_f32 v195, v195, v200\n"
      "v_mul_f32 v196, v196, v200\n"
      "v_mul_f32 v197, v197, v200\n"
      "v_mul_f32 v198, v198, v200\n"
      "v_mul_f32 v199, v199, v200\n"
      "v_cvt_pk_bf16_f32 v208, v192, v193\n"
      "v_cvt_pk_bf16_f32 v209, v194, v195\n"
      "v_cvt_pk_bf16_f32 v210, v196, v197\n"
      "v_cvt_pk_bf16_f32 v211, v198, v199\n"
      "v_min_u32 v208, -2, v208\n"
      "v_min_u32 v209, -2, v209\n"
      "v_min_u32 v210, -2, v210\n"
      "v_min_u32 v211, -2, v211\n"
      "s_cmp_lg_u32 %[apub], 0\n"
      "s_cbranch_scc1 36f\n"
      "buffer_store_dwordx4 v[208:211], v189, %[srd], s60 offen sc1\n"
      "s_mov_b32 %[apub], 1\n"
      "36:\n"
      "s_setprio 0\n"
      "s_waitcnt vmcnt(16)\n"
      "ds_read_b64_tr_b16 v[168:169], v0 offset:0\n"
      "ds_read_b64_tr_b16 v[170:171], v0 offset:1024\n"
      "ds_read_b64_tr_b16 v[172:173], v1 offset:0\n"
      "ds_read_b64_tr_b16 v[174:175], v1 offset:1024\n"
      "s_waitcnt vmcnt(12)\n"
      "ds_read_b64_tr_b16 v[176:177], v0 offset:2048\n"
      "ds_read_b64_tr_b16 v[178:179], v0 offset:3072\n"
      "ds_read_b64_tr_b16 v[180:181], v1 offset:2048\n"
      "ds_read_b64_tr_b16 v[182:183], v1 offset:3072\n"
      "s_waitcnt lgkmcnt(4)\n"
      "v_mfma_f32_32x32x16_bf16 v[8:23], v[168:171], v[72:75], 0\n"
      "v_mfma_f32_32x32x16_bf16 v[24:39], v[172:175], v[72:75], 0\n"
      "v_mfma_f32_32x32x16_bf16 v[40:55], v[168:171], v[76:79], 0\n"
      "v_mfma_f32_32x32x16_bf16 v[56:71], v[172:175], v[76:79], 0\n"
      "s_add_u32 m0, s58, 10240\n"
      "s_add_u32 s70, s55, 81920\n"
      "buffer_load_dwordx4 v6, %[srdw], s70 offen lds\n"
      "s_add_u32 m0, s58, 11264\n"
      "s_add_u32 s70, s55, 90112\n"
      "buffer_load_dwordx4 v6, %[srdw], s70 offen lds\n"
      "s_add_u32 s70, s53, 4096\n"
      "buffer_load_dwordx4 v[104:107], v189, %[srd], s70 offen sc1\n"
      "s_add_u32 s70, s70, 88064\n"
      "buffer_load_dwordx4 v[108:111], v189, %[srd], s70 offen sc1\n"
      "s_waitcnt vmcnt(12)\n"
      "ds_read_b64_tr_b16 v[168:169], v0 offset:4096\n"
      "ds_read_b64_tr_b16 v[170:171], v0 offset:5120\n"
      "ds_read_b64_tr_b16 v[172:173], v1 offset:4096\n"
      "ds_read_b64_tr_b16 v[174:175], v1 offset:5120\n"
      "s_waitcnt lgkmcnt(4)\n"
      "v_mfma_f32_32x32x16_bf16 v[8:23], v[176:179], v[80:83], v[8:23]\n"
      "v_mfma_f32_32x32x16_bf16 v[24:39], v[180:183], v[80:83], v[24:39]\n"
      "v_mfma_f32_32x32x16_bf16 v[40:55], v[176:179], v[84:87], v[40:55]\n"
      "v_mfma_f32_32x32x16_bf16 v[56:71], v[180:183], v[84:87], v[56:71]\n"
      "s_add_u32 m0, s58, 12288\n"
      "s_add_u32 s70, s55, 98304\n"
      "buffer_load_dwordx4 v6, %[srdw], s70 offen lds\n"
      "s_add_u32 m0, s58, 13312\n"
      "s_add_u32 s70, s55, 106496\n"
      "buffer_load_dwordx4 v6, %[srdw], s70 offen lds\n"
      "s_add_u32 s70, s53, 5120\n"
      "buffer_load_dwordx4 v[112:115], v189, %[srd], s70 offen sc1\n"
      "s_add_u32 s70, s70, 88064\n"
      "buffer_load_dwordx4 v[116:119], v189, %[srd], s70 offen sc1\n"
      "s_waitcnt vmcnt(12)\n"
      "ds_read_b64_tr_b16 v[176:177], v0 offset:6144\n"
      "ds_read_b64_tr_b16 v[178:179], v0 offset:7168\n"
      "ds_read_b64_tr_b16 v[180:181], v1 offset:6144\n"
      "ds_read_b64_tr_b16 v[182:183], v1 offset:7168\n"
      "s_waitcnt lgkmcnt(4)\n"
      "v_mfma_f32_32x32x16_bf16 v[8:23], v[168:171], v[88:91], v[8:23]\n"
      "v_mfma_f32_32x32x16_bf16 v[24:39], v[172:175], v[88:91], v[24:39]\n"
      "v_mfma_f32_32x32x16_bf16 v[40:55], v[168:171], v[92:95], v[40:55]\n"
      "v_mfma_f32_32x32x16_bf16 v[56:71], v[172:175], v[92:95], v[56:71]\n"
      "s_add_u32 m0, s58, 14336\n"
      "s_add_u32 s70, s55, 114688\n"
      "buffer_load_dwordx4 v6, %[srdw], s70 offen lds\n"
      "s_add_u32 m0, s58, 15360\n"
      "s_add_u32 s70, s55, 122880\n"
      "buffer_load_dwordx4 v6, %[srdw], s70 offen lds\n"
      "s_add_u32 s70, s53, 6144\n"
      "buffer_load_dwordx4 v[120:123], v189, %[srd], s70 offen sc1\n"
      "s_add_u32 s70, s70, 88064\n"
      "buffer_load_dwordx4 v[124:127], v189, %[srd], s70 offen sc1\n"
      "s_waitcnt vmcnt(12)\n"
      "ds_read_b64_tr_b16 v[168:169], v0 offset:8192\n"
      "ds_read_b64_tr_b16 v[170:171], v0 offset:9216\n"
      "ds_read_b64_tr_b16 v[172:173], v1 offset:8192\n"
      "ds_read_b64_tr_b16 v[174:175], v1 offset:9216\n"
      "s_waitcnt lgkmcnt(4)\n"
      "v_mfma_f32_32x32x16_bf16 v[8:23], v[176:179], v[96:99], v[8:23]\n"
      "v_mfma_f32_32x32x16_bf16 v[24:39], v[180:183], v[96:99], v[24:39]\n"
      "v_mfma_f32_32x32x16_bf16 v[40:55], v[176:179], v[100:103], v[40:55]\n"
      "v_mfma_f32_32x32x16_bf16 v[56:71], v[180:183], v[100:103], v[56:71]\n"
      "s_add_u32 m0, s58, 0\n"
      "s_add_u32 s70, s55, 131072\n"
      "buffer_load_dwordx4 v6, %[srdw], s70 offen lds\n"
      "s_add_u32 m0, s58, 1024\n"
      "s_add_u32 s70, s55, 139264\n"
      "buffer_load_dwordx4 v6, %[srdw], s70 offen lds\n"
      "s_add_u32 s70, s53, 7168\n"
      "buffer_load_dwordx4 v[128:131], v189, %[srd], s70 offen sc1\n"
      "s_add_u32 s70, s70, 88064\n"
      "buffer_load_dwordx4 v[132:135], v189, %[srd], s70 offen sc1\n"
      "s_waitcnt vmcnt(12)\n"
      "ds_read_b64_tr_b16 v[176:177], v0 offset:10240\n"
      "ds_read_b64_tr_b16 v[178:179], v0 offset:11264\n"
      "ds_read_b64_tr_b16 v[180:181], v1 offset:10240\n"
      "ds_read_b64_tr_b16 v[182:183], v1 offset:11264\n"
      "s_waitcnt lgkmcnt(4)\n"
      "v_mfma_f32_32x32x16_bf16 v[8:23], v[168:171], v[104:107], v[8:23]\n"
      "v_mfma_f32_32x32x16_bf16 v[24:39], v[172:175], v[104:107], v[24:39]\n"
      "v_mfma_f32_32x32x16_bf16 v[40:55], v[168:171], v[108:111], v[40:55]\n"
      "v_mfma_f32_32x32x16_bf16 v[56:71], v[172:175], v[108:111], v[56:71]\n"
      "s_add_u32 m0, s58, 2048\n"
      "s_add_u32 s70, s55, 147456\n"
      "buffer_load_dwordx4 v6, %[srdw], s70 offen lds\n"
      "s_add_u32 m0, s58, 3072\n"
      "s_add_u32 s70, s55, 155648\n"
      "buffer_load_dwordx4 v6, %[srdw], s70 offen lds\n"
      "s_add_u32 s70, s53, 8192\n"
      "buffer_load_dwordx4 v[136:139], v189, %[srd], s70 offen sc1\n"
      "s_add_u32 s70, s70, 88064\n"
      "buffer_load_dwordx4 v[140:143], v189, %[srd], s70 offen sc1\n"
      "s_waitcnt vmcnt(12)\n"
      "ds_read_b64_tr_b16 v[168:169], v0 offset:12288\n"
      "ds_read_b64_tr_b16 v[170:171], v0 offset:13312\n"
      "ds_read_b64_tr_b16 v[172:173], v1 offset:12288\n"
      "ds_read_b64_tr_b16 v[174:175], v1 offset:13312\n"
      "s_waitcnt lgkmcnt(4)\n"
      "v_mfma_f32_32x32x16_bf16 v[8:23], v[176:179], v[112:115], v[8:23]\n"
      "v_mfma_f32_32x32x16_bf16 v[24:39], v[180:183], v[112:115], v[24:39]\n"
      "v_mfma_f32_32x32x16_bf16 v[40:55], v[176:179], v[116:119], v[40:55]\n"
      "v_mfma_f32_32x32x16_bf16 v[56:71], v[180:183], v[116:119], v[56:71]\n"
      "s_add_u32 m0, s58, 4096\n"
      "s_add_u32 s70, s55, 163840\n"
      "buffer_load_dwordx4 v6, %[srdw], s70 offen lds\n"
      "s_add_u32 m0, s58, 5120\n"
      "s_add_u32 s70, s55, 172032\n"
      "buffer_load_dwordx4 v6, %[srdw], s70 offen lds\n"
      "s_add_u32 s70, s53, 9216\n"
      "buffer_load_dwordx4 v[144:147], v189, %[srd], s70 offen sc1\n"
      "s_add_u32 s70, s70, 88064\n"
      "buffer_load_dwordx4 v[148:151], v189, %[srd], s70 offen sc1\n"
      "s_waitcnt vmcnt(12)\n"
      "ds_read_b64_tr_b16 v[176:177], v0 offset:14336\n"
      "ds_read_b64_tr_b16 v[178:179], v0 offset:15360\n"
      "ds_read_b64_tr_b16 v[180:181], v1 offset:14336\n"
      "ds_read_b64_tr_b16 v[182:183], v1 offset:15360\n"
      "s_waitcnt lgkmcnt(4)\n"
      "v_mfma_f32_32x32x16_bf16 v[8:23], v[168:171], v[120:123], v[8:23]\n"
      "v_mfma_f32_32x32x16_bf16 v[24:39], v[172:175], v[120:123], v[24:39]\n"
      "v_mfma_f32_32x32x16_bf16 v[40:55], v[168:171], v[124:127], v[40:55]\n"
      "v_mfma_f32_32x32x16_bf16 v[56:71], v[172:175], v[124:127], v[56:71]\n"
      "s_add_u32 m0, s58, 6144\n"
      "s_add_u32 s70, s55, 180224\n"
      "buffer_load_dwordx4 v6, %[srdw], s70 offen lds\n"
      "s_add_u32 m0, s58, 7168\n"
      "s_add_u32 s70, s55, 188416\n"
      "buffer_load_dwordx4 v6, %[srdw], s70 offen lds\n"
      "s_add_u32 s70, s53, 10240\n"
      "buffer_load_dwordx4 v[152:155], v189, %[srd], s70 offen sc1\n"
      "s_add_u32 s70, s70, 88064\n"
      "buffer_load_dwordx4 v[156:159], v189, %[srd], s70 offen sc1\n"
      "s_waitcnt vmcnt(12)\n"
      "ds_read_b64_tr_b16 v[168:169], v0 offset:0\n"
      "ds_read_b64_tr_b16 v[170:171], v0 offset:1024\n"
      "ds_read_b64_tr_b16 v[172:173], v1 offset:0\n"
      "ds_read_b64_tr_b16 v[174:175], v1 offset:1024\n"
      "s_waitcnt lgkmcnt(4)\n"
      "v_mfma_f32_32x32x16_bf16 v[8:23], v[176:179], v[128:131], v[8:23]\n"
      "v_mfma_f32_32x32x16_bf16 v[24:39], v[180:183], v[128:131], v[24:39]\n"
      "v_mfma_f32_32x32x16_bf16 v[40:55], v[176:179], v[132:135], v[40:55]\n"
      "v_mfma_f32_32x32x16_bf16 v[56:71], v[180:183], v[132:135], v[56:71]\n"
      "s_add_u32 m0, s58, 8192\n"
      "s_add_u32 s70, s55, 196608\n"
      "buffer_load_dwordx4 v6, %[srdw], s70 offen lds\n"
      "s_add_u32 m0, s58, 9216\n"
      "s_add_u32 s70, s55, 204800\n"
      "buffer_load_dwordx4 v6, %[srdw], s70 offen lds\n"
      "s_add_u32 s70, s53, 11264\n"
      "buffer_load_dwordx4 v[160:163], v189, %[srd], s70 offen sc1\n"
      "s_add_u32 s70, s70, 88064\n"
      "buffer_load_dwordx4 v[164:167], v189, %[srd], s70 offen sc1\n"
      "s_waitcnt vmcnt(12)\n"
      "ds_read_b64_tr_b16 v[176:177], v0 offset:2048\n"
      "ds_read_b64_tr_b16 v[178:179], v0 offset:3072\n"
      "ds_read_b64_tr_b16 v[180:181], v1 offset:2048\n"
      "ds_read_b64_tr_b16 v[182:183], v1 offset:3072\n"
      "s_waitcnt lgkmcnt(4)\n"
      "v_mfma_f32_32x32x16_bf16 v[8:23], v[168:171], v[136:139], v[8:23]\n"
      "v_mfma_f32_32x32x16_bf16 v[24:39], v[172:175], v[136:139], v[24:39]\n"
      "v_mfma_f32_32x32x16_bf16 v[40:55], v[168:171], v[140:143], v[40:55]\n"
      "v_mfma_f32_32x32x16_bf16 v[56:71], v[172:175], v[140:143], v[56:71]\n"
      "s_add_u32 m0, s58, 10240\n"
      "s_add_u32 s70, s55, 212992\n"
      "buffer_load_dwordx4 v6, %[srdw], s70 offen lds\n"
      "s_add_u32 m0, s58, 11264\n"
      "s_add_u32 s70, s55, 221184\n"
      "buffer_load_dwordx4 v6, %[srdw], s70 offen lds\n"
      "s_add_u32 s70, s53, 12288\n"
      "buffer_load_dwordx4 v[72:75], v189, %[srd], s70 offen sc1\n"
      "s_add_u32 s70, s70, 88064\n"
      "buffer_load_dwordx4 v[76:79], v189, %[srd], s70 offen sc1\n"
      "s_waitcnt vmcnt(12)\n"
      "ds_read_b64_tr_b16 v[168:169], v0 offset:4096\n"
      "ds_read_b64_tr_b16 v[170:171], v0 offset:5120\n"
      "ds_read_b64_tr_b16 v[172:173], v1 offset:4096\n"
      "ds_read_b64_tr_b16 v[174:175], v1 offset:5120\n"
      "s_waitcnt lgkmcnt(4)\n"
      "v_mfma_f32_32x32x16_bf16 v[8:23], v[176:179], v[144:147], v[8:23]\n"
      "v_mfma_f32_32x32x16_bf16 v[24:39], v[180:183], v[144:147], v[24:39]\n"
      "v_mfma_f32_32x32x16_bf16 v[40:55], v[176:179], v[148:151], v[40:55]\n"
      "v_mfma_f32_32x32x16_bf16 v[56:71], v[180:183], v[148:151], v[56:71]\n"
      "s_add_u32 m0, s58, 12288\n"
      "s_add_u32 s70, s55, 229376\n"
      "buffer_load_dwordx4 v6, %[srdw], s70 offen lds\n"
      "s_add_u32 m0, s58, 13312\n"
      "s_add_u32 s70, s55, 237568\n"
      "buffer_load_dwordx4 v6, %[srdw], s70 offen lds\n"
      "s_add_u32 s70, s53, 13312\n"
      "buffer_load_dwordx4 v[80:83], v189, %[srd], s70 offen sc1\n"
      "s_add_u32 s70, s70, 88064\n"
      "buffer_load_dwordx4 v[84:87], v189, %[srd], s70 offen sc1\n"
      "s_waitcnt vmcnt(12)\n"
      "ds_read_b64_tr_b16 v[176:177], v0 offset:6144\n"
      "ds_read_b64_tr_b16 v[178:179], v0 offset:7168\n"
      "ds_read_b64_tr_b16 v[180:181], v1 offset:6144\n"
      "ds_read_b64_tr_b16 v[182:183], v1 offset:7168\n"
      "s_waitcnt lgkmcnt(4)\n"
      "v_mfma_f32_32x32x16_bf16 v[8:23], v[168:171], v[152:155], v[8:23]\n"
      "v_mfma_f32_32x32x16_bf16 v[24:39], v[172:175], v[152:155], v[24:39]\n"
      "v_mfma_f32_32x32x16_bf16 v[40:55], v[168:171], v[156:159], v[40:55]\n"
      "v_mfma_f32_32x32x16_bf16 v[56:71], v[172:175], v[156:159], v[56:71]\n"
      "s_add_u32 m0, s58, 14336\n"
      "s_add_u32 s70, s55, 245760\n"
      "buffer_load_dwordx4 v6, %[srdw], s70 offen lds\n"
      "s_add_u32 m0, s58, 15360\n"
      "s_add_u32 s70, s55, 253952\n"
      "buffer_load_dwordx4 v6, %[srdw], s70 offen lds\n"
      "s_add_u32 s70, s53, 14336\n"
      "buffer_load_dwordx4 v[88:91], v189, %[srd], s70 offen sc1\n"
      "s_add_u32 s70, s70, 88064\n"
      "buffer_load_dwordx4 v[92:95], v189, %[srd], s70 offen sc1\n"
      "s_waitcnt vmcnt(12)\n"
      "ds_read_b64_tr_b16 v[168:169], v0 offset:8192\n"
      "ds_read_b64_tr_b16 v[170:171], v0 offset:9216\n"
      "ds_read_b64_tr_b16 v[172:173], v1 offset:8192\n"
      "ds_read_b64_tr_b16 v[174:175], v1 offset:9216\n"
      "s_waitcnt lgkmcnt(4)\n"
      "v_mfma_f32_32x32x16_bf16 v[8:23], v[176:179], v[160:163], v[8:23]\n"
      "v_mfma_f32_32x32x16_bf16 v[24:39], v[180:183], v[160:163], v[24:39]\n"
      "v_mfma_f32_32x32x16_bf16 v[40:55], v[176:179], v[164:167], v[40:55]\n"
      "v_mfma_f32_32x32x16_bf16 v[56:71], v[180:183], v[164:167], v[56:71]\n"
      "s_add_u32 m0, s58, 0\n"
      "s_add_u32 s70, s55, 262144\n"
      "buffer_load_dwordx4 v6, %[srdw], s70 offen lds\n"
      "s_add_u32 m0, s58, 1024\n"
      "s_add_u32 s70, s55, 270336\n"
      "buffer_load_dwordx4 v6, %[srdw], s70 offen lds\n"
      "s_add_u32 s70, s53, 15360\n"
      "buffer_load_dwordx4 v[96:99], v189, %[srd], s70 offen sc1\n"
      "s_add_u32 s70, s70, 88064\n"
      "buffer_load_dwordx4 v[100:103], v189, %[srd], s70 offen sc1\n"
      "s_waitcnt vmcnt(12)\n"
      "ds_read_b64_tr_b16 v[176:177], v0 offset:10240\n"
      "ds_read_b64_tr_b16 v[178:179], v0 offset:11264\n"
      "ds_read_b64_tr_b16 v[180:181], v1 offset:10240\n"
      "ds_read_b64_tr_b16 v[182:183], v1 offset:11264\n"
      "s_waitcnt lgkmcnt(4)\n"
      "v_mfma_f32_32x32x16_bf16 v[8:23], v[168:171], v[72:75], v[8:23]\n"
      "v_mfma_f32_32x32x16_bf16 v[24:39], v[172:175], v[72:75], v[24:39]\n"
      "v_mfma_f32_32x32x16_bf16 v[40:55], v[168:171], v[76:79], v[40:55]\n"
      "v_mfma_f32_32x32x16_bf16 v[56:71], v[172:175], v[76:79], v[56:71]\n"
      "s_add_u32 m0, s58, 2048\n"
      "s_add_u32 s70, s55, 278528\n"
      "buffer_load_dwordx4 v6, %[srdw], s70 offen lds\n"
      "s_add_u32 m0, s58, 3072\n"
      "s_add_u32 s70, s55, 286720\n"
      "buffer_load_dwordx4 v6, %[srdw], s70 offen lds\n"
      "s_add_u32 s70, s53, 16384\n"
      "buffer_load_dwordx4 v[104:107], v189, %[srd], s70 offen sc1\n"
      "s_add_u32 s70, s70, 88064\n"
      "buffer_load_dwordx4 v[108:111], v189, %[srd], s70 offen sc1\n"
      "s_lshl_b32 s68, s65, 4\n"
      "s_add_u32 s68, s68, 0\n"
      "s_and_b32 s68, s68, 0x1ffff\n"
      "s_add_u32 s68, s68, s57\n"
      "s_add_u32 m0, s58, 17408\n"
      "s_add_u32 s70, s68, 0\n"
      "buffer_load_dwordx4 v6, %[srdo], s70 offen lds\n"
      "s_add_u32 m0, s58, 18432\n"
      "s_add_u32 s70, s68, 8192\n"
      "buffer_load_dwordx4 v6, %[srdo], s70 offen lds\n"
      "s_waitcnt vmcnt(14)\n"
      "ds_read_b64_tr_b16 v[168:169], v0 offset:12288\n"
      "ds_read_b64_tr_b16 v[170:171], v0 offset:13312\n"
      "ds_read_b64_tr_b16 v[172:173], v1 offset:12288\n"
      "ds_read_b64_tr_b16 v[174:175], v1 offset:13312\n"
      "s_waitcnt lgkmcnt(4)\n"
      "v_mfma_f32_32x32x16_bf16 v[8:23], v[176:179], v[80:83], v[8:23]\n"
      "v_mfma_f32_32x32x16_bf16 v[24:39], v[180:183], v[80:83], v[24:39]\n"
      "v_mfma_f32_32x32x16_bf16 v[40:55], v[176:179], v[84:87], v[40:55]\n"
      "v_mfma_f32_32x32x16_bf16 v[56:71], v[180:183], v[84:87], v[56:71]\n"
      "s_add_u32 m0, s58, 4096\n"
      "s_add_u32 s70, s55, 294912\n"
      "buffer_load_dwordx4 v6, %[srdw], s70 offen lds\n"
      "s_add_u32 m0, s58, 5120\n"
      "s_add_u32 s70, s55, 303104\n"
      "buffer_load_dwordx4 v6, %[srdw], s70 offen lds\n"
      "s_add_u32 s70, s53, 17408\n"
      "buffer_load_dwordx4 v[112:115], v189, %[srd], s70 offen sc1\n"
      "s_add_u32 s70, s70, 88064\n"
      "buffer_load_dwordx4 v[116:119], v189, %[srd], s70 offen sc1\n"
      "s_lshl_b32 s68, s65, 4\n"
      "s_add_u32 s68, s68, 32768\n"
      "s_and_b32 s68, s68, 0x1ffff\n"
      "s_add_u32 s68, s68, s57\n"
      "s_add_u32 m0, s58, 19456\n"
      "s_add_u32 s70, s68, 0\n"
      "buffer_load_dwordx4 v6, %[srdo], s70 offen lds\n"
      "s_add_u32 m0, s58, 20480\n"
      "s_add_u32 s70, s68, 8192\n"
      "buffer_load_dwordx4 v6, %[srdo], s70 offen lds\n"
      "s_waitcnt vmcnt(16)\n"
      "ds_read_b64_tr_b16 v[176:177], v0 offset:14336\n"
      "ds_read_b64_tr_b16 v[178:179], v0 offset:15360\n"
      "ds_read_b64_tr_b16 v[180:181], v1 offset:14336\n"
      "ds_read_b64_tr_b16 v[182:183], v1 offset:15360\n"
      "s_waitcnt lgkmcnt(4)\n"
      "v_mfma_f32_32x32x16_bf16 v[8:23], v[168:171], v[88:91], v[8:23]\n"
      "v_mfma_f32_32x32x16_bf16 v[24:39], v[172:175], v[88:91], v[24:39]\n"
      "v_mfma_f32_32x32x16_bf16 v[40:55], v[168:171], v[92:95], v[40:55]\n"
      "v_mfma_f32_32x32x16_bf16 v[56:71], v[172:175], v[92:95], v[56:71]\n"
      "s_add_u32 m0, s58, 6144\n"
      "s_add_u32 s70, s55, 311296\n"
      "buffer_load_dwordx4 v6, %[srdw], s70 offen lds\n"
      "s_add_u32 m0, s58, 7168\n"
      "s_add_u32 s70, s55, 319488\n"
      "buffer_load_dwordx4 v6, %[srdw], s70 offen lds\n"
      "s_add_u32 s70, s53, 18432\n"
      "buffer_load_dwordx4 v[120:123], v189, %[srd], s70 offen sc1\n"
      "s_add_u32 s70, s70, 88064\n"
      "buffer_load_dwordx4 v[124:127], v189, %[srd], s70 offen sc1\n"
      "s_lshl_b32 s68, s65, 4\n"
      "s_add_u32 s68, s68, 65536\n"
      "s_and_b32 s68, s68, 0x1ffff\n"
      "s_add_u32 s68, s68, s57\n"
      "s_add_u32 m0, s58, 21504\n"
      "s_add_u32 s70, s68, 0\n"
      "buffer_load_dwordx4 v6, %[srdo], s70 offen lds\n"
      "s_add_u32 m0, s58, 22528\n"
      "s_add_u32 s70, s68, 8192\n"
      "buffer_load_dwordx4 v6, %[srdo], s70 offen lds\n"
      "s_waitcnt vmcnt(18)\n"
      "ds_read_b64_tr_b16 v[168:169], v0 offset:0\n"
      "ds_read_b64_tr_b16 v[170:171], v0 offset:1024\n"
      "ds_read_b64_tr_b16 v[172:173], v1 offset:0\n"
      "ds_read_b64_tr_b16 v[174:175], v1 offset:1024\n"
      "s_waitcnt lgkmcnt(4)\n"
      "v_mfma_f32_32x32x16_bf16 v[8:23], v[176:179], v[96:99], v[8:23]\n"
      "v_mfma_f32_32x32x16_bf16 v[24:39], v[180:183], v[96:99], v[24:39]\n"
      "v_mfma_f32_32x32x16_bf16 v[40:55], v[176:179], v[100:103], v[40:55]\n"
      "v_mfma_f32_32x32x16_bf16 v[56:71], v[180:183], v[100:103], v[56:71]\n"
      "s_add_u32 m0, s58, 8192\n"
      "s_add_u32 s70, s55, 327680\n"
      "buffer_load_dwordx4 v6, %[srdw], s70 offen lds\n"
      "s_add_u32 m0, s58, 9216\n"
      "s_add_u32 s70, s55, 335872\n"
      "buffer_load_dwordx4 v6, %[srdw], s70 offen lds\n"
      "s_add_u32 s70, s53, 19456\n"
      "buffer_load_dwordx4 v[128:131], v189, %[srd], s70 offen sc1\n"
      "s_add_u32 s70, s70, 88064\n"
      "buffer_load_dwordx4 v[132:135], v189, %[srd], s70 offen sc1\n"
      "s_lshl_b32 s68, s65, 4\n"
      "s_add_u32 s68, s68, 98304\n"
      "s_and_b32 s68, s68, 0x1ffff\n"
      "s_add_u32 s68, s68, s57\n"
      "s_add_u32 m0, s58, 23552\n"
      "s_add_u32 s70, s68, 0\n"
      "buffer_load_dwordx4 v6, %[srdo], s70 offen lds\n"
      "s_add_u32 m0, s58, 24576\n"
      "s_add_u32 s70, s68, 8192\n"
      "buffer_load_dwordx4 v6, %[srdo], s70 offen lds\n"
      "s_waitcnt vmcnt(20)\n"
      "ds_read_b64_tr_b16 v[176:177], v0 offset:2048\n"
      "ds_read_b64_tr_b16 v[178:179], v0 offset:3072\n"
      "ds_read_b64_tr_b16 v[180:181], v1 offset:2048\n"
      "ds_read_b64_tr_b16 v[182:183], v1 offset:3072\n"
      "s_waitcnt lgkmcnt(4)\n"
      "v_mfma_f32_32x32x16_bf16 v[8:23], v[168:171], v[104:107], v[8:23]\n"
      "v_mfma_f32_32x32x16_bf16 v[24:39], v[172:175], v[104:107], v[24:39]\n"
      "v_mfma_f32_32x32x16_bf16 v[40:55], v[168:171], v[108:111], v[40:55]\n"
      "v_mfma_f32_32x32x16_bf16 v[56:71], v[172:175], v[108:111], v[56:71]\n"
      "s_add_u32 m0, s58, 10240\n"
      "s_add_u32 s70, s56, 0\n"
      "buffer_load_dwordx4 v6, %[srdw], s70 offen lds\n"
      "s_add_u32 m0, s58, 11264\n"
      "s_add_u32 s70, s56, 8192\n"
      "buffer_load_dwordx4 v6, %[srdw], s70 offen lds\n"
      "s_add_u32 s70, s53, 20480\n"
      "buffer_load_dwordx4 v[136:139], v189, %[srd], s70 offen sc1\n"
      "s_add_u32 s70, s70, 88064\n"
      "buffer_load_dwordx4 v[140:143], v189, %[srd], s70 offen sc1\n"
      "s_lshl_b32 s68, s65, 4\n"
      "s_add_u32 s68, s68, 0\n"
      "s_and_b32 s68, s68, 0x1ffff\n"
      "s_add_u32 s68, s68, s57\n"
      "s_add_u32 m0, s58, 25600\n"
      "s_add_u32 s70, s68, 16384\n"
      "buffer_load_dwordx4 v6, %[srdo], s70 offen lds\n"
      "s_add_u32 m0, s58, 26624\n"
      "s_add_u32 s70, s68, 24576\n"
      "buffer_load_dwordx4 v6, %[srdo], s70 offen lds\n"
      "s_waitcnt vmcnt(20)\n"
      "ds_read_b64_tr_b16 v[168:169], v0 offset:4096\n"
      "ds_read_b64_tr_b16 v[170:171], v0 offset:5120\n"
      "ds_read_b64_tr_b16 v[172:173], v1 offset:4096\n"
      "ds_read_b64_tr_b16 v[174:175], v1 offset:5120\n"
      "s_waitcnt lgkmcnt(4)\n"
      "v_mfma_f32_32x32x16_bf16 v[8:23], v[176:179], v[112:115], v[8:23]\n"
      "v_mfma_f32_32x32x16_bf16 v[24:39], v[180:183], v[112:115], v[24:39]\n"
      "v_mfma_f32_32x32x16_bf16 v[40:55], v[176:179], v[116:119], v[40:55]\n"
      "v_mfma_f32_32x32x16_bf16 v[56:71], v[180:183], v[116:119], v[56:71]\n"
      "s_mov_b32 s70, s54\n"
      "buffer_load_dwordx4 v[144:147], v189, %[srd], s70 offen sc1\n"
      "s_add_u32 s70, s70, 88064\n"
      "buffer_load_dwordx4 v[148:151], v189, %[srd], s70 offen sc1\n"
      "s_lshl_b32 s68, s65, 4\n"
      "s_add_u32 s68, s68, 32768\n"
      "s_and_b32 s68, s68, 0x1ffff\n"
      "s_add_u32 s68, s68, s57\n"
      "s_add_u32 m0, s58, 27648\n"
      "s_add_u32 s70, s68, 16384\n"
      "buffer_load_dwordx4 v6, %[srdo], s70 offen lds\n"
      "s_add_u32 m0, s58, 28672\n"
      "s_add_u32 s70, s68, 24576\n"
      "buffer_load_dwordx4 v6, %[srdo], s70 offen lds\n"
      "s_waitcnt vmcnt(18)\n"
      "ds_read_b64_tr_b16 v[176:177], v0 offset:6144\n"
      "ds_read_b64_tr_b16 v[178:179], v0 offset:7168\n"
      "ds_read_b64_tr_b16 v[180:181], v1 offset:6144\n"
      "ds_read_b64_tr_b16 v[182:183], v1 offset:7168\n"
      "s_waitcnt lgkmcnt(4)\n"
      "v_mfma_f32_32x32x16_bf16 v[8:23], v[168:171], v[120:123], v[8:23]\n"
      "v_mfma_f32_32x32x16_bf16 v[24:39], v[172:175], v[120:123], v[24:39]\n"
      "v_mfma_f32_32x32x16_bf16 v[40:55], v[168:171], v[124:127], v[40:55]\n"
      "v_mfma_f32_32x32x16_bf16 v[56:71], v[172:175], v[124:127], v[56:71]\n"
      "s_lshl_b32 s68, s65, 4\n"
      "s_add_u32 s68, s68, 65536\n"
      "s_and_b32 s68, s68, 0x1ffff\n"
      "s_add_u32 s68, s68, s57\n"
      "s_add_u32 m0, s58, 29696\n"
      "s_add_u32 s70, s68, 16384\n"
      "buffer_load_dwordx4 v6, %[srdo], s70 offen lds\n"
      "s_add_u32 m0, s58, 30720\n"
      "s_add_u32 s70, s68, 24576\n"
      "buffer_load_dwordx4 v6, %[srdo], s70 offen lds\n"
      "s_waitcnt vmcnt(14)\n"
      "ds_read_b64_tr_b16 v[168:169], v0 offset:8192\n"
      "ds_read_b64_tr_b16 v[170:171], v0 offset:9216\n"
      "ds_read_b64_tr_b16 v[172:173], v1 offset:8192\n"
      "ds_read_b64_tr_b16 v[174:175], v1 offset:9216\n"
      "s_waitcnt lgkmcnt(4)\n"
      "v_mfma_f32_32x32x16_bf16 v[8:23], v[176:179], v[128:131], v[8:23]\n"
      "v_mfma_f32_32x32x16_bf16 v[24:39], v[180:183], v[128:131], v[24:39]\n"
      "v_mfma_f32_32x32x16_bf16 v[40:55], v[176:179], v[132:135], v[40:55]\n"
      "v_mfma_f32_32x32x16_bf16 v[56:71], v[180:183], v[132:135], v[56:71]\n"
      "s_lshl_b32 s68, s65, 4\n"
      "s_add_u32 s68, s68, 98304\n"
      "s_and_b32 s68, s68, 0x1ffff\n"
      "s_add_u32 s68, s68, s57\n"
      "s_add_u32 m0, s58, 31744\n"
      "s_add_u32 s70, s68, 16384\n"
      "buffer_load_dwordx4 v6, %[srdo], s70 offen lds\n"
      "s_add_u32 m0, s58, 32768\n"
      "s_add_u32 s70, s68, 24576\n"
      "buffer_load_dwordx4 v6, %[srdo], s70 offen lds\n"
      "s_waitcnt vmcnt(10)\n"
      "ds_read_b64_tr_b16 v[176:177], v0 offset:10240\n"
      "ds_read_b64_tr_b16 v[178:179], v0 offset:11264\n"
      "ds_read_b64_tr_b16 v[180:181], v1 offset:10240\n"
      "ds_read_b64_tr_b16 v[182:183], v1 offset:11264\n"
      "s_waitcnt lgkmcnt(4)\n"
      "v_mfma_f32_32x32x16_bf16 v[8:23], v[168:171], v[136:139], v[8:23]\n"
      "v_mfma_f32_32x32x16_bf16 v[24:39], v[172:175], v[136:139], v[24:39]\n"
      "v_mfma_f32_32x32x16_bf16 v[40:55], v[168:171], v[140:143], v[40:55]\n"
      "v_mfma_f32_32x32x16_bf16 v[56:71], v[172:175], v[140:143], v[56:71]\n"
      "s_waitcnt vmcnt(6) lgkmcnt(0)\n"
      "s_cmp_eq_u32 s59, 0\n"
      "s_cbranch_scc1 25f\n"
      "v_mfma_f32_32x32x16_bf16 v[8:23], v[176:179], v[144:147], v[8:23]\n"
      "v_mfma_f32_32x32x16_bf16 v[24:39], v[180:183], v[144:147], v[24:39]\n"
      "v_mfma_f32_32x32x16_bf16 v[40:55], v[176:179], v[148:151], v[40:55]\n"
      "v_mfma_f32_32x32x16_bf16 v[56:71], v[180:183], v[148:151], v[56:71]\n"
      "25:\n"
      "s_setprio 2\n"
      "s_nop 8\n"
      "ds_write_b128 v7, v[8:11] offset:0\n"
      "ds_write_b128 v7, v[12:15] offset:32\n"
      "ds_write_b128 v7, v[16:19] offset:64\n"
      "ds_write_b128 v7, v[20:23] offset:96\n"
      "ds_write_b128 v7, v[24:27] offset:128\n"
      "ds_write_b128 v7, v[28:31] offset:160\n"
      "ds_write_b128 v7, v[32:35] offset:192\n"
      "ds_write_b128 v7, v[36:39] offset:224\n"
      "ds_write_b128 v7, v[40:43] offset:8704\n"
      "ds_write_b128 v7, v[44:47] offset:8736\n"
      "ds_write_b128 v7, v[48:51] offset:8768\n"
      "ds_write_b128 v7, v[52:55] offset:8800\n"
      "ds_write_b128 v7, v[56:59] offset:8832\n"
      "ds_write_b128 v7, v[60:63] offset:8864\n"
      "ds_write_b128 v7, v[64:67] offset:8896\n"
      "ds_write_b128 v7, v[68:71] offset:8928\n"
      "s_branch 21b\n"
      ".Lfused_code_z:\n"
      "99:\n"
        : [bad] "=&s"(bad), [apub] "+s"(apub), "+{v[4:7]}"(nm), "+{v[128:143]}"(x0), "+{v[144:159]}"(x1), "+{v[160:175]}"(x2), "+{v[176:191]}"(x3)
        : PAR_IN, [srd] "s"(wsr), [srdw] "s"(wdr), [srdo] "s"(wor), [srdx] "s"(srdx), [srdout] "s"(outr),
          [usem] "s"(pass), [big] "s"(0x1p98f)
        : "memory", "scc", "vcc", "v0", "v1", "v2", "v3", "v8", "v9", "v10", "v11", "v12", "v13", "v14", "v15", "v16", "v17", "v18", "v19", "v20", "v21", "v22", "v23", "v24", "v25", "v26", "v27", "v28", "v29", "v30", "v31", "v32", "v33", "v34", "v35", "v36", "v37", "v38", "v39", "v40", "v41", "v42", "v43", "v44", "v45", "v46", "v47", "v48", "v49", "v50", "v51", "v52", "v53", "v54", "v55", "v56", "v57", "v58", "v59", "v60", "v61", "v62", "v63", "v64", "v65", "v66", "v67", "v68", "v69", "v70", "v71", "v72", "v73", "v74", "v75", "v76", "v77", "v78", "v79", "v80", "v81", "v82", "v83", "v84", "v85", "v86", "v87", "v88", "v89", "v90", "v91", "v92", "v93", "v94", "v95", "v96", "v97", "v98", "v99", "v100", "v101", "v102", "v103", "v104", "v105", "v106", "v107", "v108", "v109", "v110", "v111", "v112", "v113", "v114", "v115", "v116", "v117", "v118", "v119", "v120", "v121", "v122", "v123", "v124", "v125", "v126", "v127", "v192", "v193", "v194", "v195", "v196", "v197", "v198", "v199", "v200", "v201", "v202", "v203", "v204", "v205", "v206", "v207", "v208", "v209", "v210", "v211", "v212", "v213", "v214", "v215", "v216", "v217", "v218", "v219", "v220", "v221", "v222", "v223", "v224", "v225", "v226", "v227", "v228", "v229", "v230", "v231", "v232", "v233", "v234", "v235", "v236", "v237", "v238", "v239", "v240", "v241", "v242", "v243", "v244", "v245", "v246", "v247", "v248", "v249", "v250", "v251", "v252", "v253", "v254", "v255", "s68", "s69", "s70", "s71", "s72", "s73", "s74", "s75");
      if (rfl(bad) == 0) break;
      RELANE();
#pragma unroll
      for (int q = 0; q < 4; ++q) {
        float m = -INFINITY;
        const u32x4 qf = ld_sc1(wsr, lane * 16, WS_Q + head * 65536 + (4 * g + ((t + q) & 3)) * 1024);
        for (u32 n = 0; n < 64; ++n) {
          const f32x16 s = mfma32(ld_sc1(wsr, lane * 16, WS_K + head * 65536 + n * 1024), qf, f32x16{});
#pragma unroll
          for (int r = 0; r < 16; ++r) m = fmaxf(m, s[r]);
        }
        auto r2 = __builtin_amdgcn_permlane32_swap(__float_as_uint(m), __float_as_uint(m), false, false);
        nm[q] = -fmaxf(__uint_as_float(r2[0]), __uint_as_float(r2[1]));
      }
      wait_bar<0>();
    }
  }
  RELANE();
  if (b == 0 && (w == 0 && lane == 0)) __hip_atomic_store((GAS u64*)ws, ((u64)pa[7] << 32) | pa[6], __ATOMIC_RELAXED, __HIP_MEMORY_SCOPE_AGENT);
  STAMP(11);
  STAMP_OUT();
}

extern "C" void kernel_launch(void* const* d_in, const int* in_sizes, int n_in, void* d_out, int out_size, void* d_ws, size_t ws_size,
                              hipStream_t stream) {
  fused_block<<<256, 512, 0, stream>>>((const char*)d_in[0], (const char*)d_in[1], (const char*)d_in[2], (const char*)d_in[4],
                                       (const char*)d_in[5], (const char*)d_in[6], (char*)d_out, (char*)d_ws);
}
```
